```python
import torch

def _softmax(x, axis=-1):
    return torch.softmax(x, dim=axis)
def _gelu(x):
    return torch.nn.functional.gelu(x, approximate="tanh")
def _silu(x):
    return torch.nn.functional.silu(x)
def _sigmoid(x):
    return torch.sigmoid(x)
def _layernorm(x, axis=-1, eps=1e-5):
    m = x.mean(dim=axis, keepdim=True)
    v = x.var(dim=axis, keepdim=True, unbiased=False)
    return (x - m) / torch.sqrt(v + eps)
def _scatter_add(tgt, idx, src, axis=0):
    return tgt.clone().index_add_(axis, idx, src)
def _segment_sum(src, seg, n):
    out = torch.zeros((n,) + src.shape[1:], dtype=src.dtype)
    return out.index_add_(0, seg, src)
def _pad(x, pw):
    flat = tuple(v for pair in reversed(pw) for v in pair)
    return torch.nn.functional.pad(x, flat)

kernel_name = 'recurrent_scan__ssm_conv_s5__dense_activation_silu__prompt'

def setup_inputs():

    _w = (lambda a: (lambda t: t.to(torch.bfloat16) if t.is_floating_point() else t)(torch.as_tensor(a)))
    def _A_setup():
        B, S, D = 1, 2048, 640
        P = D
        Lambda_re = -0.5 * (lambda _x: torch.exp(torch.as_tensor(_x)))((lambda *s: torch.randn(*s, dtype=torch.bfloat16) * 0.8)(P))
        Lambda_im = (lambda *s: torch.randn(*s, dtype=torch.bfloat16) * 0.8)(P) * 1.0
        B_re = (lambda *s: torch.randn(*s, dtype=torch.bfloat16) * 0.8)(P, D) * (D ** -0.5)
        B_im = (lambda *s: torch.randn(*s, dtype=torch.bfloat16) * 0.8)(P, D) * (D ** -0.5)
        C_re = (lambda *s: torch.randn(*s, dtype=torch.bfloat16) * 0.8)(D, P) * (P ** -0.5)
        C_im = (lambda *s: torch.randn(*s, dtype=torch.bfloat16) * 0.8)(D, P) * (P ** -0.5)
        log_dt = (lambda *s: torch.randn(*s, dtype=torch.bfloat16) * 0.8)(P) * 0.3 - 2.0
        Dskip = (lambda *s: torch.randn(*s, dtype=torch.bfloat16) * 0.4)(D)
        return {
            "x": _w((lambda *s: torch.randn(*s, dtype=torch.bfloat16) * 0.05)(B, S, D)),
            "Lambda_re": _w(Lambda_re),
            "Lambda_im": _w(Lambda_im),
            "B_re": _w(B_re),
            "B_im": _w(B_im),
            "C_re": _w(C_re),
            "C_im": _w(C_im),
            "log_dt": _w(log_dt),
            "Dskip": _w(Dskip),
        }
    _A_in = _A_setup()

    def _B_setup():
        B, S, D, F = 1, 2048, 640, 1536
        return {
            "x": _w((lambda *s: torch.randn(*s, dtype=torch.bfloat16) * 0.05)(B, S, D)),
            "w1": _w((lambda *s: torch.randn(*s, dtype=torch.bfloat16) * 0.8)(D, F) * (D ** -0.5)),
            "w2": _w((lambda *s: torch.randn(*s, dtype=torch.bfloat16) * 0.1)(F, D) * (F ** -0.5)),
        }
    _B_in = _B_setup()

    return {
        "A_x": _A_in["x"],
        "A_Lambda_re": _A_in["Lambda_re"],
        "A_Lambda_im": _A_in["Lambda_im"],
        "A_B_re": _A_in["B_re"],
        "A_B_im": _A_in["B_im"],
        "A_C_re": _A_in["C_re"],
        "A_C_im": _A_in["C_im"],
        "A_log_dt": _A_in["log_dt"],
        "A_Dskip": _A_in["Dskip"],
        "B_x": _B_in["x"],
        "B_w1": _B_in["w1"],
        "B_w2": _B_in["w2"],
    }

def reference(A_x, A_Lambda_re, A_Lambda_im, A_B_re, A_B_im, A_C_re, A_C_im, A_log_dt, A_Dskip, B_x, B_w1, B_w2):
    def _A_ref(x, Lambda_re, Lambda_im, B_re, B_im, C_re, C_im, log_dt, Dskip):
        Bsz, L, H = x.shape
        Lam = Lambda_re + 1j * Lambda_im
        Bmat = B_re + 1j * B_im
        Cmat = C_re + 1j * C_im
        dt = torch.exp(log_dt)
        Lam_bar = torch.exp(dt * Lam)
        B_bar = ((Lam_bar - 1.0) / Lam)[:, None] * Bmat
        Bu = torch.einsum("bld,pd->blp", x + 0j, B_bar)
        h = torch.zeros_like(Bu[:, 0, :])
        hs = []
        for t in range(L):
            h = Lam_bar[None, :] * h + Bu[:, t, :]
            hs.append(h)
        hseq = torch.stack(hs, 1)
        y = 2.0 * torch.einsum("blp,dp->bld", hseq, Cmat).real + x * Dskip[None, None, :]
        return y

    def _B_ref(x, w1, w2):
        h = torch.einsum("bsd,df->bsf", x, w1)
        h = _silu(h)
        out = torch.einsum("bsf,fd->bsd", h, w2)
        return out

    x = A_x
    xn = _layernorm(x, axis=-1)
    s = _A_ref(xn, A_Lambda_re, A_Lambda_im, A_B_re, A_B_im, A_C_re, A_C_im, A_log_dt, A_Dskip)
    g = _silu(_B_ref(xn, B_w1, B_w2))
    return x + s * g
```

```cpp
#include <hip/hip_runtime.h>
#include <hip/hip_bf16.h>
#include <stdint.h>

typedef unsigned short u16;
__device__ __forceinline__ float bf2f(u16 v){ return __uint_as_float(((unsigned)v)<<16); }
__device__ __forceinline__ u16 f2bf(float f){ unsigned u=__float_as_uint(f); u += 0x7fffu + ((u>>16)&1u); return (u16)(u>>16); }
__device__ __forceinline__ float rbf(float f){ return bf2f(f2bf(f)); }

#define S_ 2048
#define D_ 640
#define P_ 640
#define F_ 1536

__global__ void k_ln(const u16* x, u16* xn){
  int t = blockIdx.x; int l = threadIdx.x;
  float v[10]; float s=0;
  for(int i=0;i<10;i++){ v[i]=bf2f(x[t*D_+l+64*i]); s+=v[i]; }
  for(int o=32;o;o>>=1) s+=__shfl_xor(s,o);
  float mean = s/640.f;
  float q=0; for(int i=0;i<10;i++){ float d=v[i]-mean; q+=d*d; }
  for(int o=32;o;o>>=1) q+=__shfl_xor(q,o);
  float m = rbf(mean); float var = rbf(q/640.f);
  float den = rbf(sqrtf(rbf(var+1e-5f)));
  for(int i=0;i<10;i++){ float d=rbf(v[i]-m); xn[t*D_+l+64*i]=f2bf(d/den); }
}
__global__ void k_bu(const u16* xn, const u16* Bre, const u16* Bim, const u16* Lre, const u16* Lim, const u16* ldt, float2* Bu){
  int t = blockIdx.x; int p = blockIdx.y*64+threadIdx.x;
  float ar=0, ai=0;
  for(int d=0;d<D_;d++){ float xv=bf2f(xn[t*D_+d]); ar+=xv*bf2f(Bre[p*D_+d]); ai+=xv*bf2f(Bim[p*D_+d]); }
  float dt = rbf(expf(bf2f(ldt[p])));
  float lr=bf2f(Lre[p]), li=bf2f(Lim[p]);
  float er=expf(dt*lr); float br=er*cosf(dt*li), bi=er*sinf(dt*li);
  float nr=br-1.f, ni=bi; float dd=lr*lr+li*li;
  float cr=(nr*lr+ni*li)/dd, ci=(ni*lr-nr*li)/dd;
  Bu[t*P_+p]=make_float2(cr*ar-ci*ai, cr*ai+ci*ar);
}
__global__ void k_scan(float2* Bu, const u16* Lre, const u16* Lim, const u16* ldt){
  int p = blockIdx.x*64+threadIdx.x;
  float dt = rbf(expf(bf2f(ldt[p])));
  float lr=bf2f(Lre[p]), li=bf2f(Lim[p]);
  float er=expf(dt*lr); float br=er*cosf(dt*li), bi=er*sinf(dt*li);
  float hr=0,hi=0;
  for(int t=0;t<S_;t++){ float2 b=Bu[t*P_+p]; float nr=br*hr-bi*hi+b.x, ni=br*hi+bi*hr+b.y; hr=nr;hi=ni; Bu[t*P_+p]=make_float2(hr,hi);}
}
__global__ void k_h1(const u16* xn, const u16* w1, u16* h1){
  int t=blockIdx.x; int f=blockIdx.y*64+threadIdx.x;
  float a=0; for(int d=0;d<D_;d++) a+=bf2f(xn[t*D_+d])*bf2f(w1[d*F_+f]);
  a=rbf(a); h1[t*F_+f]=f2bf(a/(1.f+expf(-a)));
}
__global__ void k_out(const u16* x, const u16* xn, const float2* h, const u16* Cre, const u16* Cim, const u16* Dsk, const u16* h1, const u16* w2, float* out){
  int t=blockIdx.x; int d=blockIdx.y*64+threadIdx.x;
  float s=0; for(int p=0;p<P_;p++){ float2 hv=h[t*P_+p]; s+=hv.x*bf2f(Cre[d*P_+p])-hv.y*bf2f(Cim[d*P_+p]); }
  s=2.f*s+rbf(bf2f(xn[t*D_+d])*bf2f(Dsk[d]));
  float o=0; for(int f=0;f<F_;f++) o+=bf2f(h1[t*F_+f])*bf2f(w2[f*D_+d]);
  o=rbf(o); float g=rbf(o/(1.f+expf(-o)));
  out[t*D_+d]=bf2f(x[t*D_+d])+s*g;
}

extern "C" void kernel_launch(void* const* d_in, const int* in_sizes, int n_in,
                              void* d_out, int out_size, void* d_ws, size_t ws_size,
                              hipStream_t stream) {
  const u16* x=(const u16*)d_in[0]; const u16* Lre=(const u16*)d_in[1]; const u16* Lim=(const u16*)d_in[2];
  const u16* Bre=(const u16*)d_in[3]; const u16* Bim=(const u16*)d_in[4]; const u16* Cre=(const u16*)d_in[5]; const u16* Cim=(const u16*)d_in[6];
  const u16* ldt=(const u16*)d_in[7]; const u16* Dsk=(const u16*)d_in[8]; const u16* w1=(const u16*)d_in[10]; const u16* w2=(const u16*)d_in[11];
  char* ws=(char*)d_ws;
  u16* xn=(u16*)ws; float2* Bu=(float2*)(ws+(4<<20)); u16* h1=(u16*)(ws+(16<<20));
  k_ln<<<S_,64,0,stream>>>(x,xn);
  k_bu<<<dim3(S_,P_/64),64,0,stream>>>(xn,Bre,Bim,Lre,Lim,ldt,Bu);
  k_scan<<<P_/64,64,0,stream>>>(Bu,Lre,Lim,ldt);
  k_h1<<<dim3(S_,F_/64),64,0,stream>>>(xn,w1,h1);
  k_out<<<dim3(S_,D_/64),64,0,stream>>>(x,xn,Bu,Cre,Cim,Dsk,h1,w2,(float*)d_out);
}
```

```cpp
#include <hip/hip_runtime.h>
#include <stdint.h>

typedef unsigned short u16;
typedef __attribute__((ext_vector_type(8))) __bf16 bf16x8;
typedef __attribute__((ext_vector_type(2))) __bf16 bf16x2;
typedef __attribute__((ext_vector_type(4))) float f32x4;
typedef __attribute__((ext_vector_type(2))) float f32x2;
typedef __attribute__((ext_vector_type(4))) unsigned u32x4;
typedef __attribute__((ext_vector_type(2))) unsigned u32x2;
typedef __attribute__((ext_vector_type(4))) short s16x4;
#define AS3 __attribute__((address_space(3)))
#define AS1 __attribute__((address_space(1)))

#define DEVI __device__ __forceinline__

constexpr int S_ = 2048, D_ = 640, F_ = 1536;
constexpr size_t WS_H1F = 0;
constexpr size_t WS_HF = 6291456;
constexpr size_t WS_HX = 11534336;
constexpr size_t WS_W2F = 16777216;
constexpr size_t WS_CF = 18743296;
constexpr size_t WS_AGG = 20381696;
constexpr size_t WS_ST = 20463616;
constexpr size_t WS_AB = 20480000;
constexpr size_t WS_FLAG = 20485120;

DEVI float bf2f(unsigned v) { return __uint_as_float(v << 16); }
DEVI float bflo(unsigned v) { return __uint_as_float(v << 16); }
DEVI float bfhi(unsigned v) { return __uint_as_float(v & 0xffff0000u); }
DEVI unsigned pk2(float a, float b) {
  bf16x2 r = {(__bf16)a, (__bf16)b};
  return __builtin_bit_cast(unsigned, r);
}
DEVI float rbf(float a) { return bflo(pk2(a, 0.f)); }
template <int N> DEVI float shr(float v) {
  return __builtin_bit_cast(float, __builtin_amdgcn_update_dpp(0, __builtin_bit_cast(int, v), 0x110 + N, 0xf, 0xf, true));
}
DEVI float dot2(unsigned a, unsigned b, float c) {
  return __builtin_amdgcn_fdot2_f32_bf16(__builtin_bit_cast(bf16x2, a), __builtin_bit_cast(bf16x2, b), c, false);
}
DEVI f32x2 cpow(float a, float b, float n) {
  float mg = __builtin_amdgcn_exp2f(n * a * 1.44269504088896f);
  float rv = n * b * 0.15915494309189535f;
  rv = __builtin_amdgcn_fractf(rv);
  f32x2 r;
  r.x = mg * __builtin_amdgcn_cosf(rv);
  r.y = mg * __builtin_amdgcn_sinf(rv);
  return r;
}
DEVI float siluf(float a) { return a * __builtin_amdgcn_rcpf(1.f + __builtin_amdgcn_exp2f(-1.44269504088896f * a)); }

#define WAITV(n) asm volatile("s_waitcnt vmcnt(%0)" ::"n"(n) : "memory")
#define WAITL0() asm volatile("s_waitcnt lgkmcnt(0)" ::: "memory")
#define BARRIER() do { WAITL0(); __builtin_amdgcn_s_barrier(); asm volatile("" ::: "memory"); } while (0)
#define GLDS(src, ldsoff) __builtin_amdgcn_global_load_lds((const AS1 unsigned*)(src), (AS3 unsigned*)(uintptr_t)(ldsoff), 16, 0, 0)

DEVI u32x4 lds128(unsigned off) { return *(const AS3 u32x4*)(uintptr_t)off; }
DEVI f32x4 lds128f(unsigned off) { return *(const AS3 f32x4*)(uintptr_t)off; }
DEVI f32x2 lds64f(unsigned off) { return *(const AS3 f32x2*)(uintptr_t)off; }
DEVI void sts128f(unsigned off, f32x4 v) { *(AS3 f32x4*)(uintptr_t)off = v; }
DEVI void sts64f(unsigned off, f32x2 v) { *(AS3 f32x2*)(uintptr_t)off = v; }
DEVI void sts32f(unsigned off, float v) { *(AS3 float*)(uintptr_t)off = v; }
DEVI u32x2 ldstr(unsigned off) {
  s16x4 r = __builtin_amdgcn_ds_read_tr16_b64_v4i16((AS3 s16x4*)(uintptr_t)off);
  return __builtin_bit_cast(u32x2, r);
}
DEVI f32x4 mfma(u32x4 a, u32x4 b, f32x4 c) {
  return __builtin_amdgcn_mfma_f32_16x16x32_bf16(__builtin_bit_cast(bf16x8, a), __builtin_bit_cast(bf16x8, b), c, 0, 0, 0);
}
DEVI void st128(u32x4 v, __amdgpu_buffer_rsrc_t rs, int off) { __builtin_amdgcn_raw_buffer_store_b128(v, rs, off, 0, 16); }
DEVI __amdgpu_buffer_rsrc_t mkrsrc(const void* p, unsigned bytes) {
  return __builtin_amdgcn_make_buffer_rsrc((void*)p, 0, bytes, 0x00020000);
}

constexpr unsigned SLOT = 40960;
constexpr unsigned EXT = 3 * SLOT;
constexpr unsigned L_ST = EXT;
constexpr unsigned L_WS = EXT + 2048;
constexpr unsigned L_AG = EXT + 5120;
constexpr unsigned L_CO = EXT + 7680;
constexpr unsigned L_PW = EXT + 8320;
constexpr unsigned K1_LDS = EXT + 8320 + 20480;

template <bool ISB>
DEVI void k1_body(unsigned lb, int rc, int g, const u16* x, const u16* Lre, const u16* Lim, const u16* Bre, const u16* Bim,
                  const u16* Cre, const u16* Cim, const u16* ldt, const u16* w1, const u16* w2, char* ws) {
  constexpr int NT = ISB ? 5 : 6;
  const int tid = threadIdx.x, lane = tid & 63;
  const int w = __builtin_amdgcn_readfirstlane(tid >> 6);
  const int mi = w >> 1, nj = w & 1;
  const int c = lane & 15, q = lane >> 4;
  const int l3 = lane >> 3, l7 = lane & 7;

  const char* sx[2];
  const char* sw[3];
#pragma unroll
  for (int v = 0; v < 2; ++v) {
    int rho = 8 * (w + 8 * v) + l3;
    int t = 128 * rc + 32 * (rho >> 5) + 2 * (rho & 15) + ((rho >> 4) & 1);
    sx[v] = (const char*)x + t * 1280 + ((l7 ^ l3) << 4);
  }
#pragma unroll
  for (int v = 0; v < 3; ++v) {
    int pc = w + 8 * v;
    if (ISB) {
      int n = 8 * pc + l3;
      if (n > 159) n = 159;
      int p = 80 * g + (n >> 1);
      sw[v] = (const char*)((n & 1) ? Bim : Bre) + p * 1280 + ((l7 ^ l3) << 4);
    } else {
      int kl = 8 * w + l3;
      int f = ((kl >> 1) & 1) | ((kl >> 2) & 2);
      sw[v] = (const char*)w1 + kl * 3072 + (192 * g + 64 * v) * 2 + ((l7 ^ (f << 1)) << 4);
    }
  }
  const int wstep = ISB ? 128 : 64 * 3072;
  auto issue = [&](int s, unsigned slot) {
    GLDS(sx[0] + s * 128, lb + slot + w * 1024);
    GLDS(sx[1] + s * 128, lb + slot + 8192 + w * 1024);
    GLDS(sw[0] + s * wstep, lb + slot + 16384 + w * 1024);
    GLDS(sw[1] + s * wstep, lb + slot + 16384 + 8192 + w * 1024);
    if (!ISB || w < 4) GLDS(sw[2] + s * wstep, lb + slot + 16384 + 16384 + w * 1024);
  };

  unsigned pLr = 0, pLi = 0, pDt = 0;
  u32x4 cre = {0, 0, 0, 0}, cim = {0, 0, 0, 0};
  const int pl_t = tid % 80, grp_t = tid / 80;
  const int tc = w * 50 + lane, cdl = tc / 10, ca = tc % 10;
  const int unit = (rc * 8 + g) * 4 + (w - 4);
  const bool do_unit = ISB && w >= 4 && unit < 480;
  if (ISB) {
    {
      int p = 80 * g + pl_t;
      const u16* a0 = Lre + p; const u16* a1 = Lim + p; const u16* a2 = ldt + p;
      asm volatile("global_load_ushort %0, %1, off" : "=v"(pLr) : "v"(a0) : "memory");
      asm volatile("global_load_ushort %0, %1, off" : "=v"(pLi) : "v"(a1) : "memory");
      asm volatile("global_load_ushort %0, %1, off" : "=v"(pDt) : "v"(a2) : "memory");
      int dl = lane < 50 ? cdl : 0, aa = lane < 50 ? ca : 0;
      const u16* c0 = Cre + (40 * rc + dl) * 640 + 80 * g + 8 * aa;
      const u16* c1 = Cim + (40 * rc + dl) * 640 + 80 * g + 8 * aa;
      asm volatile("global_load_dwordx4 %0, %1, off" : "=v"(cre) : "v"(c0) : "memory");
      asm volatile("global_load_dwordx4 %0, %1, off" : "=v"(cim) : "v"(c1) : "memory");
    }
    if (do_unit) {
      int ks = unit / 10, ds = unit % 10;
#pragma unroll
      for (int v = 0; v < 4; ++v) {
        int r = 8 * v + l3;
        int f = (r >> 1) & 3;
        const char* src = (const char*)w2 + (32 * ks + r) * 1280 + ds * 128 + ((l7 ^ (f << 1)) << 4);
        GLDS(src, lb + 2 * SLOT + (w - 4) * 4096 + v * 1024);
      }
    }
  }
  issue(0, 0);
  issue(1, SLOT);
  if (ISB) {
    if (w < 4) { WAITV(10); } else { WAITV(8); }
    asm volatile("" : "+v"(pLr), "+v"(pLi), "+v"(pDt), "+v"(cre), "+v"(cim));
    if (tid < 480) {
      float lr = bf2f(pLr), li = bf2f(pLi);
      float dt = rbf(expf(bf2f(pDt)));
      float a = dt * lr, b = dt * li;
      for (int i = grp_t; i < 32; i += 6) {
        f32x2 pw = cpow(a, b, (float)(i + 1));
        sts64f(lb + L_PW + (pl_t * 32 + i) * 8, pw);
      }
      if (grp_t == 0) {
        f32x2 e = cpow(a, b, 1.f);
        float nr = e.x - 1.f, ni = e.y, dd = 2.f / (lr * lr + li * li);
        f32x2 co; co.x = (nr * lr + ni * li) * dd; co.y = (ni * lr - nr * li) * dd;
        sts64f(lb + L_CO + pl_t * 8, co);
        if (rc == 0) { f32x2 ab = {a, b}; *(f32x2*)(ws + WS_AB + (80 * g + pl_t) * 8) = ab; }
      }
    }
    if (do_unit) {
      int ks = unit / 10, ds = unit % 10;
      unsigned sb = lb + 2 * SLOT + (w - 4) * 4096;
      int r0 = 4 * q + (c >> 2);
      int f = (r0 >> 1) & 3;
      unsigned a0 = sb + r0 * 128 + (c & 3) * 8;
#pragma unroll
      for (int j = 0; j < 4; ++j) {
        u32x2 lo = ldstr(a0 + ((j ^ f) << 5));
        u32x2 hi = ldstr(a0 + 2048 + ((j ^ f) << 5));
        u32x4 v = {lo.x, lo.y, hi.x, hi.y};
        int Dt = 4 * ds + j, cg = Dt / 5, nt = Dt % 5;
        *(u32x4*)(ws + WS_W2F + (size_t)(((cg * 48 + ks) * 5 + nt) * 1024) + lane * 16) = v;
      }
    }
    BARRIER();
    if (lane < 50) {
      int d = 40 * rc + cdl, cg = d / 80, dr = d % 80, nt = dr >> 4, idx = dr & 15;
      int ks = 5 * g + (ca >> 1), half = ca & 1;
      char* dst = ws + WS_CF + (size_t)(((cg * 40 + ks) * 5 + nt) * 1024) + idx * 16 + half * 8;
#pragma unroll
      for (int qq = 0; qq < 4; ++qq) {
        f32x4 co = lds128f(lb + L_CO + (8 * ca + 2 * qq) * 8);
        float r0 = bflo(cre[qq]), r1 = bfhi(cre[qq]), i0 = bflo(cim[qq]), i1 = bfhi(cim[qq]);
        u32x2 o;
        o.x = pk2(co.x * r0 - co.y * i0, -(co.x * i0 + co.y * r0));
        o.y = pk2(co.z * r1 - co.w * i1, -(co.z * i1 + co.w * r1));
        *(u32x2*)(dst + qq * 256) = o;
      }
    }
  }
  if (tid == 0) {
    int b = blockIdx.x;
    *(unsigned*)(ws + WS_FLAG + (b >> 3) * 128 + (b & 7) * 4) = 0u;
  }

  const int kA = nj ^ (mi & 1);
  unsigned xa[2], wa[2][ISB ? 1 : 2];
#pragma unroll
  for (int p = 0; p < 2; ++p) {
    int kk = kA ^ p;
    xa[p] = lb + (32 * mi + c) * 128 + (((4 * kk + q) ^ (c & 7)) << 4);
    if (ISB) {
      wa[p][0] = lb + 16384 + (80 * nj + c) * 128 + (((4 * kk + q) ^ (c & 7)) << 4);
    } else {
      int r = c >> 2;
      int f = ((r >> 1) & 1) | ((q & 1) << 1);
      unsigned base = lb + 16384 + (32 * kk + 8 * q + r) * 128 + (c & 3) * 8;
      wa[p][0] = base + (f << 5);
      wa[p][1] = 0;
    }
  }
  f32x4 acc[2][NT];
#pragma unroll
  for (int i = 0; i < 2; ++i)
#pragma unroll
    for (int t = 0; t < NT; ++t) acc[i][t] = (f32x4){0.f, 0.f, 0.f, 0.f};
  float sxs[2] = {0.f, 0.f}, sxq[2] = {0.f, 0.f};
  float wsm[NT];
#pragma unroll
  for (int t = 0; t < NT; ++t) wsm[t] = 0.f;
  const unsigned ONES = 0x3f803f80u;
  unsigned woff[NT];
#pragma unroll
  for (int t = 0; t < NT; ++t) {
    if (ISB) woff[t] = t * 2048;
    else { int T = 6 * nj + t; woff[t] = ((T >> 2) << 13) | ((T & 3) << 5); }
  }

  unsigned slot = 0, nslot = 2 * SLOT;
  for (int s = 0; s < 10; ++s) {
    if (s < 9) { if (!ISB || w < 4) { WAITV(5); } else { WAITV(4); } } else { WAITV(0); }
    BARRIER();
    if (s < 8) issue(s + 2, nslot);
#pragma unroll
    for (int p = 0; p < 2; ++p) {
      u32x4 xf[2], wf[NT];
      xf[0] = lds128(xa[p] + slot);
      xf[1] = lds128(xa[p] + slot + 2048);
#pragma unroll
      for (int t = 0; t < NT; ++t) {
        if (ISB) wf[t] = lds128(wa[p][0] + slot + woff[t]);
        else {
          unsigned a = ((wa[p][0] + slot) ^ (woff[t] & 0x60u)) + (woff[t] & ~0x60u);
          u32x2 lo = ldstr(a), hi = ldstr(a + 512);
          wf[t] = (u32x4){lo.x, lo.y, hi.x, hi.y};
        }
      }
#pragma unroll
      for (int t = 0; t < NT; ++t) {
        acc[0][t] = mfma(wf[t], xf[0], acc[0][t]);
        acc[1][t] = mfma(wf[t], xf[1], acc[1][t]);
      }
      if (p == 0) {
#pragma unroll
        for (int i = 0; i < 2; ++i)
#pragma unroll
          for (int r = 0; r < 4; ++r) {
            sxs[i] = dot2(xf[i][r], ONES, sxs[i]);
            sxq[i] = dot2(xf[i][r], xf[i][r], sxq[i]);
          }
        if (((s ^ (mi >> 1)) & 1) == 0) {
#pragma unroll
          for (int t = 0; t < NT; ++t)
#pragma unroll
            for (int r = 0; r < 4; ++r) wsm[t] = dot2(wf[t][r], ONES, wsm[t]);
        }
      }
    }
    slot += SLOT; if (slot == 3 * SLOT) slot = 0;
    nslot += SLOT; if (nslot == 3 * SLOT) nslot = 0;
  }

#pragma unroll
  for (int i = 0; i < 2; ++i) {
    sxs[i] += __shfl_xor(sxs[i], 16); sxs[i] += __shfl_xor(sxs[i], 32);
    sxq[i] += __shfl_xor(sxq[i], 16); sxq[i] += __shfl_xor(sxq[i], 32);
  }
#pragma unroll
  for (int t = 0; t < NT; ++t) { wsm[t] += __shfl_xor(wsm[t], 16); wsm[t] += __shfl_xor(wsm[t], 32); }
  if (q == 0) {
#pragma unroll
    for (int i = 0; i < 2; ++i) { f32x2 v = {sxs[i], sxq[i]}; sts64f(lb + L_ST + (nj * 128 + 32 * mi + 16 * i + c) * 8, v); }
#pragma unroll
    for (int t = 0; t < NT; ++t) sts32f(lb + L_WS + (mi * 192 + 16 * (NT * nj + t) + c) * 4, wsm[t]);
  }
  BARRIER();
  float mm[2], rr[2];
#pragma unroll
  for (int i = 0; i < 2; ++i) {
    f32x2 a = lds64f(lb + L_ST + (32 * mi + 16 * i + c) * 8), b = lds64f(lb + L_ST + (128 + 32 * mi + 16 * i + c) * 8);
    float mean = (a.x + b.x) * (1.f / 640.f);
    float var = fmaxf((a.y + b.y) * (1.f / 640.f) - mean * mean, 0.f);
    float m = rbf(mean);
    float den = rbf(sqrtf(rbf(rbf(var) + 1e-5f)));
    mm[i] = m; rr[i] = 1.f / den;
    if (!ISB && g == 0 && nj == 0 && q == 0) {
      f32x2 v = {m, den};
      *(f32x2*)(ws + WS_ST + (128 * rc + 32 * mi + 2 * c + i) * 8) = v;
    }
  }
#pragma unroll
  for (int t = 0; t < NT; ++t) {
    unsigned a = lb + L_WS + (16 * (NT * nj + t) + 4 * q) * 4;
    f32x4 wsv = lds128f(a) + lds128f(a + 768) + lds128f(a + 1536) + lds128f(a + 2304);
#pragma unroll
    for (int i = 0; i < 2; ++i)
#pragma unroll
      for (int j = 0; j < 4; ++j) acc[i][t][j] = (acc[i][t][j] - mm[i] * wsv[j]) * rr[i];
  }
  const int rc64 = 2 * rc + (mi >> 1);
  const int mt0 = 2 * (mi & 1);
  if (!ISB) {
    __amdgpu_buffer_rsrc_t rs = mkrsrc(ws + WS_H1F, 6291456);
#pragma unroll
    for (int u = 0; u < 3; ++u)
#pragma unroll
      for (int i = 0; i < 2; ++i) {
        u32x4 o;
#pragma unroll
        for (int h = 0; h < 2; ++h) {
          f32x4 v = acc[i][2 * u + h];
          unsigned p0 = pk2(v[0], v[1]), p1 = pk2(v[2], v[3]);
          o[2 * h] = pk2(siluf(bflo(p0)), siluf(bfhi(p0)));
          o[2 * h + 1] = pk2(siluf(bflo(p1)), siluf(bfhi(p1)));
        }
        int ks = 6 * g + 3 * nj + u;
        st128(o, rs, lane * 16 + ((rc64 * 48 + ks) * 4 + mt0 + i) * 1024);
      }
  } else {
#pragma unroll
    for (int t = 0; t < NT; ++t) {
#pragma unroll
      for (int pp = 0; pp < 2; ++pp) {
        unsigned pb = lb + L_PW + (8 * (5 * nj + t) + 2 * q + pp) * 256;
        f32x4 l12 = lds128f(pb);
        f32x2 l4 = lds64f(pb + 3 * 8), l8 = lds64f(pb + 7 * 8), l16 = lds64f(pb + 15 * 8);
        float e0r = acc[0][t][2 * pp], e0i = acc[0][t][2 * pp + 1];
        float ar = acc[1][t][2 * pp] + l12.x * e0r - l12.y * e0i;
        float ai = acc[1][t][2 * pp + 1] + l12.x * e0i + l12.y * e0r;
        float sr, si;
        sr = shr<1>(ar); si = shr<1>(ai); ar += l12.z * sr - l12.w * si; ai += l12.z * si + l12.w * sr;
        sr = shr<2>(ar); si = shr<2>(ai); ar += l4.x * sr - l4.y * si; ai += l4.x * si + l4.y * sr;
        sr = shr<4>(ar); si = shr<4>(ai); ar += l8.x * sr - l8.y * si; ai += l8.x * si + l8.y * sr;
        sr = shr<8>(ar); si = shr<8>(ai); ar += l16.x * sr - l16.y * si; ai += l16.x * si + l16.y * sr;
        sr = shr<1>(ar); si = shr<1>(ai);
        acc[0][t][2 * pp] = e0r + l12.x * sr - l12.y * si;
        acc[0][t][2 * pp + 1] = e0i + l12.x * si + l12.y * sr;
        acc[1][t][2 * pp] = ar; acc[1][t][2 * pp + 1] = ai;
      }
      if (c == 15) sts128f(lb + L_AG + (mi * 160 + 16 * (5 * nj + t) + 4 * q) * 4, acc[1][t]);
    }
    BARRIER();
    if (mi > 0) {
#pragma unroll
      for (int t = 0; t < NT; ++t) {
        unsigned ab = lb + L_AG + (16 * (5 * nj + t) + 4 * q) * 4;
        f32x4 ci = lds128f(ab);
        f32x2 l32a = lds64f(lb + L_PW + (8 * (5 * nj + t) + 2 * q) * 256 + 31 * 8);
        f32x2 l32b = lds64f(lb + L_PW + (8 * (5 * nj + t) + 2 * q + 1) * 256 + 31 * 8);
        for (int m = 1; m < mi; ++m) {
          f32x4 a = lds128f(ab + m * 640);
          f32x4 n;
          n.x = a.x + l32a.x * ci.x - l32a.y * ci.y; n.y = a.y + l32a.x * ci.y + l32a.y * ci.x;
          n.z = a.z + l32b.x * ci.z - l32b.y * ci.w; n.w = a.w + l32b.x * ci.w + l32b.y * ci.z;
          ci = n;
        }
#pragma unroll
        for (int pp = 0; pp < 2; ++pp) {
          f32x4 pw = lds128f(lb + L_PW + (8 * (5 * nj + t) + 2 * q + pp) * 256 + c * 16);
          float cr = ci[2 * pp], cm = ci[2 * pp + 1];
          acc[0][t][2 * pp] += pw.x * cr - pw.y * cm; acc[0][t][2 * pp + 1] += pw.x * cm + pw.y * cr;
          acc[1][t][2 * pp] += pw.z * cr - pw.w * cm; acc[1][t][2 * pp + 1] += pw.z * cm + pw.w * cr;
        }
        if (mi == 3 && c == 15) *(f32x4*)(ws + WS_AGG + (rc * 640 + 80 * g + 8 * (5 * nj + t) + 2 * q) * 8) = acc[1][t];
      }
    }
    __amdgpu_buffer_rsrc_t rs = mkrsrc(ws + WS_HF, 5242880);
#pragma unroll
    for (int i = 0; i < 2; ++i) {
      unsigned pk[NT][2];
#pragma unroll
      for (int t = 0; t < NT; ++t) { pk[t][0] = pk2(acc[i][t][0], acc[i][t][1]); pk[t][1] = pk2(acc[i][t][2], acc[i][t][3]); }
      int fb = ((rc64 * 40 + 5 * g) * 4 + mt0 + i) * 1024;
      if (nj == 0) {
        st128((u32x4){pk[0][0], pk[0][1], pk[1][0], pk[1][1]}, rs, lane * 16 + fb);
        st128((u32x4){pk[2][0], pk[2][1], pk[3][0], pk[3][1]}, rs, lane * 16 + fb + 4096);
        __builtin_amdgcn_raw_buffer_store_b64((u32x2){pk[4][0], pk[4][1]}, rs, lane * 16, fb + 8192, 16);
      } else {
        __builtin_amdgcn_raw_buffer_store_b64((u32x2){pk[0][0], pk[0][1]}, rs, lane * 16 + 8, fb + 8192, 16);
        st128((u32x4){pk[1][0], pk[1][1], pk[2][0], pk[2][1]}, rs, lane * 16 + fb + 12288);
        st128((u32x4){pk[3][0], pk[3][1], pk[4][0], pk[4][1]}, rs, lane * 16 + fb + 16384);
      }
    }
  }
}

__global__ __launch_bounds__(512, 2) void k1(const u16* x, const u16* Lre, const u16* Lim, const u16* Bre, const u16* Bim,
                                             const u16* Cre, const u16* Cim, const u16* ldt, const u16* w1, const u16* w2,
                                             char* ws) {
  __shared__ __attribute__((aligned(1024))) char smem[K1_LDS];
  unsigned lb = (unsigned)(uintptr_t)(AS3 char*)smem;
  int b = blockIdx.x, xcd = b & 7, j = b >> 3;
  int rc = (xcd & 1) * 8 + (j & 7), sub = j >> 3;
  int g = (xcd >> 1) * 2 + (sub >> 1);
  if (sub & 1) k1_body<true>(lb, rc, g, x, Lre, Lim, Bre, Bim, Cre, Cim, ldt, w1, w2, ws);
  else k1_body<false>(lb, rc, g, x, Lre, Lim, Bre, Bim, Cre, Cim, ldt, w1, w2, ws);
}

constexpr unsigned M_RO = 0, M_RS = 61440;
constexpr unsigned M_CIN = 122880;
constexpr unsigned M_AB = M_CIN + 2560;
constexpr unsigned M_CTL = M_AB + 2560;
constexpr unsigned K2_LDS = M_CTL + 64;

__global__ __launch_bounds__(512, 2) void k2(const u16* x, const u16* Dsk, char* ws, float* out) {
  __shared__ __attribute__((aligned(1024))) char smem[K2_LDS];
  const unsigned lb = (unsigned)(uintptr_t)(AS3 char*)smem;
  const int tid = threadIdx.x, lane = tid & 63;
  const int w = __builtin_amdgcn_readfirstlane(tid >> 6);
  const int c = lane & 15, q = lane >> 4;
  const int b = blockIdx.x, xcd = b & 7, j = b >> 3;
  const int rc64 = xcd * 4 + (j >> 3), cg = j & 7;
  volatile AS3 unsigned* ctl = (volatile AS3 unsigned*)(uintptr_t)(lb + M_CTL);
  if (tid == 256) { ctl[0] = 0u; ctl[1] = 0u; }

  if (w >= 4) {
    const int hw = w - 4;
    const int rc128 = rc64 >> 1;
    __amdgpu_buffer_rsrc_t rH = mkrsrc(ws + WS_HF, 5242880);
    __amdgpu_buffer_rsrc_t rX = mkrsrc(ws + WS_HX, 5242880);
    u32x4 hf[5];
#pragma unroll
    for (int u = 0; u < 5; ++u)
      hf[u] = __builtin_amdgcn_raw_buffer_load_b128(rH, lane * 16, ((rc64 * 40 + 5 * cg + u) * 4 + hw) * 1024, 0);
    const int pl1 = lane < 16 ? 64 + lane : lane;
    const f32x2* AB = (const f32x2*)(ws + WS_AB) + 80 * cg;
    const f32x2* AG = (const f32x2*)(ws + WS_AGG) + 80 * cg;
    f32x2 ab0 = AB[lane], ab1 = AB[pl1];
    f32x2 ag0[15], ag1[15];
#pragma unroll
    for (int m = 0; m < 15; ++m) {
      int mm = m < rc128 ? m : 0;
      ag0[m] = AG[mm * 640 + lane]; ag1[m] = AG[mm * 640 + pl1];
    }
    BARRIER();
    f32x2 L0 = cpow(ab0.x, ab0.y, 128.f), L1 = cpow(ab1.x, ab1.y, 128.f);
    f32x2 c0 = {0.f, 0.f}, c1 = {0.f, 0.f};
#pragma unroll
    for (int m = 0; m < 15; ++m) {
      if (m < rc128) {
        f32x2 n0, n1;
        n0.x = ag0[m].x + L0.x * c0.x - L0.y * c0.y; n0.y = ag0[m].y + L0.x * c0.y + L0.y * c0.x;
        n1.x = ag1[m].x + L1.x * c1.x - L1.y * c1.y; n1.y = ag1[m].y + L1.x * c1.y + L1.y * c1.x;
        c0 = n0; c1 = n1;
      }
    }
    sts64f(lb + M_CIN + (hw * 80 + lane) * 8, c0);
    sts64f(lb + M_AB + (hw * 80 + lane) * 8, ab0);
    if (lane < 16) { sts64f(lb + M_CIN + (hw * 80 + pl1) * 8, c1); sts64f(lb + M_AB + (hw * 80 + pl1) * 8, ab1); }
    WAITL0();
    const float n1f = (float)(64 * (rc64 & 1) + 32 * (hw >> 1) + 2 * c + (hw & 1) + 1);
#pragma unroll
    for (int u = 0; u < 5; ++u) {
      u32x4 o;
#pragma unroll
      for (int h = 0; h < 2; ++h) {
        int pl = 16 * u + 8 * h + 2 * q;
        f32x4 ci = lds128f(lb + M_CIN + (hw * 80 + pl) * 8);
        f32x4 ab = lds128f(lb + M_AB + (hw * 80 + pl) * 8);
        f32x2 pa = cpow(ab.x, ab.y, n1f), pb = cpow(ab.z, ab.w, n1f);
        unsigned v0 = hf[u][2 * h], v1 = hf[u][2 * h + 1];
        o[2 * h] = pk2(bflo(v0) + pa.x * ci.x - pa.y * ci.y, bfhi(v0) + pa.x * ci.y + pa.y * ci.x);
        o[2 * h + 1] = pk2(bflo(v1) + pb.x * ci.z - pb.y * ci.w, bfhi(v1) + pb.x * ci.w + pb.y * ci.z);
      }
      st128(o, rX, lane * 16 + ((rc64 * 40 + 5 * cg + u) * 4 + hw) * 1024);
    }
    WAITV(0);
    AS1 unsigned* fl = (AS1 unsigned*)(ws + WS_FLAG + rc64 * 128);
    unsigned old = 0;
    if (lane == 0) old = __hip_atomic_fetch_add((AS3 unsigned*)(uintptr_t)(lb + M_CTL), 1u, __ATOMIC_RELAXED, __HIP_MEMORY_SCOPE_WORKGROUP);
    old = __builtin_amdgcn_readfirstlane(old);
    if (old == 3) {
      if (lane == 0) __hip_atomic_store(fl + cg, 1u, __ATOMIC_RELAXED, __HIP_MEMORY_SCOPE_AGENT);
      for (int it = 0; it < (1 << 22); ++it) {
        unsigned v = __hip_atomic_load(fl + (lane & 7), __ATOMIC_RELAXED, __HIP_MEMORY_SCOPE_AGENT);
        if (__all(v == 1u)) break;
        __builtin_amdgcn_s_sleep(2);
      }
      asm volatile("" ::: "memory");
      if (lane == 0) ctl[1] = 1u;
    }
    return;
  }

  __amdgpu_buffer_rsrc_t rA1 = mkrsrc(ws + WS_H1F + (size_t)rc64 * 48 * 4096, 48 * 4096);
  __amdgpu_buffer_rsrc_t rB1 = mkrsrc(ws + WS_W2F + (size_t)cg * 48 * 5120, 48 * 5120);
  __amdgpu_buffer_rsrc_t rA2 = mkrsrc(ws + WS_HX + (size_t)rc64 * 40 * 4096, 40 * 4096);
  __amdgpu_buffer_rsrc_t rB2 = mkrsrc(ws + WS_CF + (size_t)cg * 40 * 5120, 40 * 5120);
  u32x4 A[3][4], B[3][5];
  const int vo = lane * 16;
  auto load = [&](int i) {
    int bf = i % 3;
    if (i < 12) {
      int ks = w + 4 * i;
#pragma unroll
      for (int m = 0; m < 4; ++m) A[bf][m] = __builtin_amdgcn_raw_buffer_load_b128(rA1, vo + m * 1024, ks * 4096, 0);
#pragma unroll
      for (int n = 0; n < 5; ++n) B[bf][n] = __builtin_amdgcn_raw_buffer_load_b128(rB1, vo + n * 1024, ks * 5120, 0);
    } else {
      int ks = w + 4 * (i - 12);
#pragma unroll
      for (int n = 0; n < 5; ++n) B[bf][n] = __builtin_amdgcn_raw_buffer_load_b128(rB2, vo + n * 1024, ks * 5120, 0);
#pragma unroll
      for (int m = 0; m < 4; ++m) A[bf][m] = __builtin_amdgcn_raw_buffer_load_b128(rA2, vo + m * 1024, ks * 4096, 16);
    }
  };
  load(0);
  load(1);
  BARRIER();
  f32x4 acc[4][5], oown[5];
#pragma unroll
  for (int m = 0; m < 4; ++m)
#pragma unroll
    for (int n = 0; n < 5; ++n) acc[m][n] = (f32x4){0.f, 0.f, 0.f, 0.f};
  const int t = 64 * rc64 + 32 * (w >> 1) + 2 * c + (w & 1);
  u32x2 xv[5], dk[5];
  f32x2 st;
  auto spill = [&](unsigned region) {
#pragma unroll
    for (int s3 = 0; s3 < 3; ++s3) {
#pragma unroll
      for (int m = 0; m < 4; ++m) {
        if (m == ((w + 1 + s3) & 3)) {
#pragma unroll
          for (int n = 0; n < 5; ++n) sts128f(lb + region + ((w * 3 + s3) * 5 + n) * 1024 + vo, acc[m][n]);
        }
      }
    }
  };
#pragma unroll
  for (int i = 0; i < 22; ++i) {
    if (i + 2 == 12) {
      for (int it = 0; it < (1 << 24); ++it) {
        if (ctl[1] != 0u) break;
        __builtin_amdgcn_s_sleep(1);
      }
      asm volatile("" ::: "memory");
    }
    if (i + 2 < 22) load(i + 2);
    if (i == 19) {
#pragma unroll
      for (int n = 0; n < 5; ++n) {
        xv[n] = *(const u32x2*)(x + t * 640 + 80 * cg + 16 * n + 4 * q);
        dk[n] = *(const u32x2*)(Dsk + 80 * cg + 16 * n + 4 * q);
      }
      st = *(const f32x2*)(ws + WS_ST + t * 8);
    }
    __builtin_amdgcn_sched_barrier(0);
#pragma unroll
    for (int m = 0; m < 4; ++m)
#pragma unroll
      for (int n = 0; n < 5; ++n) acc[m][n] = mfma(B[i % 3][n], A[i % 3][m], acc[m][n]);
    if (i == 11) {
      spill(M_RO);
#pragma unroll
      for (int m = 0; m < 4; ++m)
        if (m == w) {
#pragma unroll
          for (int n = 0; n < 5; ++n) oown[n] = acc[m][n];
        }
#pragma unroll
      for (int m = 0; m < 4; ++m)
#pragma unroll
        for (int n = 0; n < 5; ++n) acc[m][n] = (f32x4){0.f, 0.f, 0.f, 0.f};
    }
    __builtin_amdgcn_sched_barrier(0);
  }
  spill(M_RS);
  f32x4 sown[5];
#pragma unroll
  for (int m = 0; m < 4; ++m)
    if (m == w) {
#pragma unroll
      for (int n = 0; n < 5; ++n) sown[n] = acc[m][n];
    }
  BARRIER();
#pragma unroll
  for (int s3 = 0; s3 < 3; ++s3) {
    int src = (w + 3 - s3) & 3;
#pragma unroll
    for (int n = 0; n < 5; ++n) {
      oown[n] += lds128f(lb + M_RO + ((src * 3 + s3) * 5 + n) * 1024 + vo);
      sown[n] += lds128f(lb + M_RS + ((src * 3 + s3) * 5 + n) * 1024 + vo);
    }
  }
#pragma unroll
  for (int n = 0; n < 5; ++n) {
    f32x4 r;
#pragma unroll
    for (int jj = 0; jj < 4; ++jj) {
      unsigned xw = xv[n][jj >> 1], dw = dk[n][jj >> 1];
      float xx = (jj & 1) ? bfhi(xw) : bflo(xw);
      float dd = (jj & 1) ? bfhi(dw) : bflo(dw);
      float o = rbf(oown[n][jj]);
      float gte = rbf(siluf(o));
      float xn = rbf(rbf(xx - st.x) / st.y);
      float sk = rbf(xn * dd);
      r[jj] = xx + (sown[n][jj] + sk) * gte;
    }
    *(f32x4*)(out + t * 640 + 80 * cg + 16 * n + 4 * q) = r;
  }
}

extern "C" void kernel_launch(void* const* d_in, const int* in_sizes, int n_in, void* d_out, int out_size, void* d_ws,
                              size_t ws_size, hipStream_t stream) {
  const u16* x = (const u16*)d_in[0];
  k1<<<256, 512, 0, stream>>>(x, (const u16*)d_in[1], (const u16*)d_in[2], (const u16*)d_in[3], (const u16*)d_in[4],
                              (const u16*)d_in[5], (const u16*)d_in[6], (const u16*)d_in[7], (const u16*)d_in[10],
                              (const u16*)d_in[11], (char*)d_ws);
  k2<<<256, 512, 0, stream>>>(x, (const u16*)d_in[8], (char*)d_ws, (float*)d_out);
}
```

```cpp
#include <hip/hip_runtime.h>
#include <stdint.h>

typedef unsigned short u16;
typedef __attribute__((ext_vector_type(8))) __bf16 bf16x8;
typedef __attribute__((ext_vector_type(2))) __bf16 bf16x2;
typedef __attribute__((ext_vector_type(4))) float f32x4;
typedef __attribute__((ext_vector_type(2))) float f32x2;
typedef __attribute__((ext_vector_type(4))) unsigned u32x4;
typedef __attribute__((ext_vector_type(2))) unsigned u32x2;
typedef __attribute__((ext_vector_type(4))) short s16x4;
#define AS3 __attribute__((address_space(3)))
#define AS1 __attribute__((address_space(1)))
#define DEVI __device__ __forceinline__

constexpr size_t WS_H1F = 0;
constexpr size_t WS_HF = 6291456;
constexpr size_t WS_W2F = 11534336;
constexpr size_t WS_CF = 13500416;
constexpr size_t WS_AGG = 15138816;
constexpr size_t WS_ST = 15220736;

DEVI float bf2f(unsigned v) { return __uint_as_float(v << 16); }
DEVI float bflo(unsigned v) { return __uint_as_float(v << 16); }
DEVI float bfhi(unsigned v) { return __uint_as_float(v & 0xffff0000u); }
DEVI unsigned pk2(float a, float b) {
  bf16x2 r = {(__bf16)a, (__bf16)b};
  return __builtin_bit_cast(unsigned, r);
}
DEVI float rbf(float a) { return bflo(pk2(a, 0.f)); }
template <int N> DEVI float shr(float v) {
  return __builtin_bit_cast(float, __builtin_amdgcn_update_dpp(0, __builtin_bit_cast(int, v), 0x110 + N, 0xf, 0xf, true));
}
DEVI float dot2(unsigned a, unsigned b, float c) {
  return __builtin_amdgcn_fdot2_f32_bf16(__builtin_bit_cast(bf16x2, a), __builtin_bit_cast(bf16x2, b), c, false);
}
DEVI f32x2 cpow(float a, float b, float n) {
  float mg = __builtin_amdgcn_exp2f(n * a);
  float rv = __builtin_amdgcn_fractf(n * b);
  f32x2 r;
  r.x = mg * __builtin_amdgcn_cosf(rv);
  r.y = mg * __builtin_amdgcn_sinf(rv);
  return r;
}
DEVI f32x2 cmul(f32x2 a, f32x2 b) { f32x2 r; r.x = a.x * b.x - a.y * b.y; r.y = a.x * b.y + a.y * b.x; return r; }
DEVI f32x2 cfma(f32x2 a, f32x2 b, f32x2 c) {
  f32x2 r; r.x = fmaf(-a.y, b.y, fmaf(a.x, b.x, c.x)); r.y = fmaf(a.y, b.x, fmaf(a.x, b.y, c.y)); return r;
}
DEVI float siluf(float a) { return a * __builtin_amdgcn_rcpf(1.f + __builtin_amdgcn_exp2f(-1.44269504088896f * a)); }

#define WAITV(n) asm volatile("s_waitcnt vmcnt(%0)" ::"n"(n) : "memory")
#define WAITL0() asm volatile("s_waitcnt lgkmcnt(0)" ::: "memory")
#define BARRIER() do { WAITL0(); __builtin_amdgcn_s_barrier(); asm volatile("" ::: "memory"); } while (0)
DEVI void glds(const void* p, unsigned ldsaddr) {
  unsigned keep;
  asm volatile("s_mov_b32 %0, m0\n\ts_mov_b32 m0, %2\n\ts_nop 0\n\tglobal_load_lds_dwordx4 %1, off\n\ts_mov_b32 m0, %0"
               : "=&s"(keep) : "v"(p), "s"(ldsaddr) : "memory");
}

DEVI u32x4 lds128(unsigned off) { return *(const AS3 u32x4*)(uintptr_t)off; }
DEVI f32x4 lds128f(unsigned off) { return *(const AS3 f32x4*)(uintptr_t)off; }
DEVI f32x2 lds64f(unsigned off) { return *(const AS3 f32x2*)(uintptr_t)off; }
DEVI float lds32f(unsigned off) { return *(const AS3 float*)(uintptr_t)off; }
DEVI void sts128f(unsigned off, f32x4 v) { *(AS3 f32x4*)(uintptr_t)off = v; }
DEVI void sts64f(unsigned off, f32x2 v) { *(AS3 f32x2*)(uintptr_t)off = v; }
DEVI void sts32f(unsigned off, float v) { *(AS3 float*)(uintptr_t)off = v; }
DEVI u32x2 ldstr(unsigned off) {
  s16x4 r = __builtin_amdgcn_ds_read_tr16_b64_v4i16((AS3 s16x4*)(uintptr_t)off);
  return __builtin_bit_cast(u32x2, r);
}
DEVI f32x4 mfma(u32x4 a, u32x4 b, f32x4 c) {
  return __builtin_amdgcn_mfma_f32_16x16x32_bf16(__builtin_bit_cast(bf16x8, a), __builtin_bit_cast(bf16x8, b), c, 0, 0, 0);
}
DEVI void st128(u32x4 v, __amdgpu_buffer_rsrc_t rs, int off) { __builtin_amdgcn_raw_buffer_store_b128(v, rs, off, 0, 16); }
DEVI __amdgpu_buffer_rsrc_t mkrsrc(const void* p, unsigned bytes) {
  return __builtin_amdgcn_make_buffer_rsrc((void*)p, 0, bytes, 0x00020000);
}
#define TSDECL
#define TS(k)
#define TSDUMP(base)

constexpr unsigned SLOT = 38912, OB = 16384, OW = 26624;
constexpr unsigned SPARE = 4 * SLOT;
constexpr unsigned K1_LDS = SPARE + 8192;
constexpr unsigned E_ST = 2 * SLOT;
constexpr unsigned E_MR = E_ST + 16384;
constexpr unsigned E_CW = E_MR + 1024;
constexpr unsigned E_C5 = E_CW + 6144;
constexpr unsigned E_CB = E_C5 + 1024;
constexpr unsigned E_CF = E_CB + 1024;
constexpr unsigned E_A5 = E_CF + 1024;

struct Tab { f32x2 L, L8, L16, L32, L64, co; };
DEVI void mktab(Tab& t, float lr, float li, float ld) {
  float dt = rbf(expf(ld));
  float a = dt * lr * 1.44269504088896f, b = dt * li * 0.15915494309189535f;
  t.L = cpow(a, b, 1.f); t.L8 = cpow(a, b, 8.f); t.L16 = cpow(a, b, 16.f); t.L32 = cpow(a, b, 32.f); t.L64 = cpow(a, b, 64.f);
  float nr = t.L.x - 1.f, ni = t.L.y, dd = 1.f / (lr * lr + li * li);
  t.co.x = (nr * lr + ni * li) * dd; t.co.y = (ni * lr - nr * li) * dd;
}
DEVI f32x2 shr2(f32x2 v, int n) {
  f32x2 r;
  if (n == 1) { r.x = shr<1>(v.x); r.y = shr<1>(v.y); }
  else if (n == 2) { r.x = shr<2>(v.x); r.y = shr<2>(v.y); }
  else if (n == 4) { r.x = shr<4>(v.x); r.y = shr<4>(v.y); }
  else { r.x = shr<8>(v.x); r.y = shr<8>(v.y); }
  return r;
}
DEVI void scan_tile(f32x4* acc, const Tab* tb, const f32x4* mr, f32x4 cs, int rc, int T, int lane, char* ws) {
  f32x2 u[2][8];
#pragma unroll
  for (int i = 0; i < 8; ++i) {
    float m = mr[i >> 1][2 * (i & 1)], r = mr[i >> 1][2 * (i & 1) + 1];
#pragma unroll
    for (int s = 0; s < 2; ++s) {
      f32x2 v;
      v.x = (acc[i][2 * s] - m * cs[2 * s]) * r; v.y = (acc[i][2 * s + 1] - m * cs[2 * s + 1]) * r;
      u[s][i] = cmul(tb[s].co, v);
    }
  }
  f32x2 h[2];
#pragma unroll
  for (int s = 0; s < 2; ++s) h[s] = u[s][0];
#pragma unroll
  for (int i = 1; i < 8; ++i)
#pragma unroll
    for (int s = 0; s < 2; ++s) h[s] = cfma(tb[s].L, h[s], u[s][i]);
#pragma unroll
  for (int s = 0; s < 2; ++s) h[s] = cfma(tb[s].L8, shr2(h[s], 1), h[s]);
#pragma unroll
  for (int s = 0; s < 2; ++s) h[s] = cfma(tb[s].L16, shr2(h[s], 2), h[s]);
#pragma unroll
  for (int s = 0; s < 2; ++s) h[s] = cfma(tb[s].L32, shr2(h[s], 4), h[s]);
#pragma unroll
  for (int s = 0; s < 2; ++s) h[s] = cfma(tb[s].L64, shr2(h[s], 8), h[s]);
  if ((lane & 15) == 15) {
    f32x4 a = {h[0].x, h[0].y, h[1].x, h[1].y};
    *(f32x4*)(ws + WS_AGG + (rc * 640 + 8 * T + 2 * (lane >> 4)) * 8) = a;
  }
#pragma unroll
  for (int s = 0; s < 2; ++s) h[s] = shr2(h[s], 1);
  __amdgpu_buffer_rsrc_t rs = mkrsrc(ws + WS_HF, 5242880);
#pragma unroll
  for (int mp = 0; mp < 4; ++mp) {
    u32x4 o;
#pragma unroll
    for (int e = 0; e < 2; ++e)
#pragma unroll
      for (int s = 0; s < 2; ++s) {
        h[s] = cfma(tb[s].L, h[s], u[s][2 * mp + e]);
        o[2 * e + s] = pk2(h[s].x, h[s].y);
      }
    st128(o, rs, lane * 16 + (((2 * rc + (mp >> 1)) * 80 + T) * 2 + (mp & 1)) * 1024);
  }
}

__global__ __launch_bounds__(512, 2) void k1(const u16* x, const u16* Lre, const u16* Lim, const u16* Bre, const u16* Bim,
                                             const u16* Cre, const u16* Cim, const u16* ldt, const u16* w1, const u16* w2,
                                             char* ws) {
  __shared__ __attribute__((aligned(1024))) char smem[K1_LDS];
  const unsigned lb = (unsigned)(uintptr_t)(AS3 char*)smem;
  const int b = blockIdx.x, xcd = b & 7, jb = b >> 3;
  const int rc = (xcd & 1) * 8 + (jb & 7), g = (xcd >> 1) * 4 + (jb >> 3);
  const int tid = threadIdx.x, lane = tid & 63;
  const int w = __builtin_amdgcn_readfirstlane(tid >> 6);
  const int c = lane & 15, q = lane >> 4;
  const int l3 = lane >> 3, l7 = lane & 7;
  TSDECL;
  TS(0);

  const char* src[5];
  unsigned dsto[5];
  int step3, step4 = 196608;
#pragma unroll
  for (int v = 0; v < 2; ++v) {
    int rho = 8 * (w + 8 * v) + l3;
    int t = 128 * rc + 8 * (rho & 15) + (rho >> 4);
    src[v] = (const char*)x + t * 1280 + ((l7 ^ l3) << 4);
    dsto[v] = (w + 8 * v) * 1024;
  }
  {
    int n = 8 * w + l3;
    src[2] = (const char*)((n & 1) ? Bim : Bre) + (40 * g + (n >> 1)) * 1280 + ((l7 ^ l3) << 4);
    dsto[2] = OB + w * 1024;
    auto wsrc = [&](int e) {
      int idx = 64 * e + lane, k = idx / 12, uu = idx - 12 * k;
      return (const char*)w1 + k * 3072 + 192 * g + (((uu >> 1) ^ ((k >> 3) & 1)) << 5) + ((uu & 1) << 4);
    };
    if (w < 2) {
      n = 64 + 8 * w + l3;
      src[3] = (const char*)((n & 1) ? Bim : Bre) + (40 * g + (n >> 1)) * 1280 + ((l7 ^ l3) << 4);
      dsto[3] = OB + (8 + w) * 1024; step3 = 128;
    } else {
      src[3] = wsrc(w - 2); dsto[3] = OW + (w - 2) * 1024; step3 = 196608;
    }
    src[4] = wsrc(w < 6 ? 6 + w : 0); dsto[4] = OW + (6 + w) * 1024;
  }
  auto issue = [&](unsigned slot) {
    glds(src[0], lb + slot + dsto[0]); glds(src[1], lb + slot + dsto[1]); glds(src[2], lb + slot + dsto[2]);
    glds(src[3], lb + slot + dsto[3]);
    if (w < 6) glds(src[4], lb + slot + dsto[4]);
    src[0] += 128; src[1] += 128; src[2] += 128; src[3] += step3; src[4] += step4;
  };
#define STAGE_WAIT(s) do { if (w < 6) { if ((s) < 8) WAITV(10); else if ((s) == 8) WAITV(5); else WAITV(0); } \
                           else { if ((s) < 8) WAITV(8); else if ((s) == 8) WAITV(4); else WAITV(0); } } while (0)
  const unsigned ONES = 0x3f803f80u;

  if (w < 4) {
    const int mi = w;
    u32x4 sj[4];
    const int unit = w < 2 ? 2 * b + w : 2 * (b - 56) + (w - 2);
    const bool sjon = w < 2 ? (b < 240) : (b >= 56);
    const int uks = unit / 10, udb = unit - 10 * uks;
    if (sjon) {
      if (w < 2) {
#pragma unroll
        for (int v = 0; v < 4; ++v) {
          int r = 8 * v + l3;
          int f = (r >> 1) & 3;
          glds((const char*)w2 + (32 * uks + r) * 1280 + udb * 128 + ((l7 ^ (f << 1)) << 4), lb + SPARE + w * 4096 + v * 1024);
        }
      } else {
        const u16* c0 = Cre + (64 * udb + lane) * 640 + 16 * uks;
        const u16* c1 = Cim + (64 * udb + lane) * 640 + 16 * uks;
        asm volatile("global_load_dwordx4 %0, %1, off" : "=v"(sj[0]) : "v"(c0) : "memory");
        asm volatile("global_load_dwordx4 %0, %1, off offset:16" : "=v"(sj[1]) : "v"(c0) : "memory");
        asm volatile("global_load_dwordx4 %0, %1, off" : "=v"(sj[2]) : "v"(c1) : "memory");
        asm volatile("global_load_dwordx4 %0, %1, off offset:16" : "=v"(sj[3]) : "v"(c1) : "memory");
      }
    }
    issue(0); issue(SLOT); issue(2 * SLOT);
    if (sjon) {
      WAITV(15);
      if (w < 2) {
        int r0 = 4 * q + (c >> 2);
        int f = (r0 >> 1) & 3;
        unsigned a0 = lb + SPARE + w * 4096 + r0 * 128 + (c & 3) * 8;
#pragma unroll
        for (int j = 0; j < 4; ++j) {
          u32x2 lo = ldstr(a0 + ((j ^ f) << 5));
          u32x2 hi = ldstr(a0 + 2048 + ((j ^ f) << 5));
          sj[j] = (u32x4){lo.x, lo.y, hi.x, hi.y};
        }
      } else {
        asm volatile("" : "+v"(sj[0]), "+v"(sj[1]), "+v"(sj[2]), "+v"(sj[3]));
        u32x4 o[4];
#pragma unroll
        for (int qq = 0; qq < 4; ++qq) {
          unsigned ia = sj[2][qq] ^ 0x80008000u, ib = sj[3][qq] ^ 0x80008000u;
          o[qq].x = __builtin_amdgcn_perm(ia, sj[0][qq], 0x05040100u);
          o[qq].y = __builtin_amdgcn_perm(ia, sj[0][qq], 0x07060302u);
          o[qq].z = __builtin_amdgcn_perm(ib, sj[1][qq], 0x05040100u);
          o[qq].w = __builtin_amdgcn_perm(ib, sj[1][qq], 0x07060302u);
        }
#pragma unroll
        for (int qq = 0; qq < 4; ++qq) sj[qq] = o[qq];
      }
    }
    unsigned xa[2], we[2], wo[2];
#pragma unroll
    for (int p = 0; p < 2; ++p) {
      int kk = (mi & 1) ^ p;
      xa[p] = lb + (32 * mi + c) * 128 + (((4 * kk + q) ^ (c & 7)) << 4);
      unsigned base = lb + OW + (32 * kk + 8 * q + (c >> 2)) * 192 + (c & 3) * 8;
      we[p] = base + ((q & 1) << 5);
      wo[p] = base + (((q & 1) ^ 1) << 5);
    }
    f32x4 acc[2][6];
    float wsm[6];
#pragma unroll
    for (int t = 0; t < 6; ++t) { acc[0][t] = (f32x4){0.f, 0.f, 0.f, 0.f}; acc[1][t] = acc[0][t]; wsm[t] = 0.f; }
    unsigned slot = 0, nslot = 3 * SLOT;
    TS(1);
    for (int s = 0; s < 10; ++s) {
      STAGE_WAIT(s);
      BARRIER();
      TS(2 + s);
      if (s < 7) issue(nslot);
#pragma unroll
      for (int p = 0; p < 2; ++p) {
        u32x4 xf[2], wf[6];
        xf[0] = lds128(xa[p] + slot);
        xf[1] = lds128(xa[p] + slot + 2048);
#pragma unroll
        for (int t = 0; t < 6; ++t) {
          unsigned a = ((t & 1) ? wo[p] : we[p]) + slot + (t & ~1) * 32;
          u32x2 lo = ldstr(a), hi = ldstr(a + 768);
          wf[t] = (u32x4){lo.x, lo.y, hi.x, hi.y};
        }
#pragma unroll
        for (int t = 0; t < 6; ++t) {
          acc[0][t] = mfma(wf[t], xf[0], acc[0][t]);
          acc[1][t] = mfma(wf[t], xf[1], acc[1][t]);
        }
        if (p == 0 && ((s ^ (mi >> 1)) & 1) == 0) {
#pragma unroll
          for (int r = 0; r < 4; ++r)
#pragma unroll
            for (int t = 0; t < 6; ++t) wsm[t] = dot2(wf[t][r], ONES, wsm[t]);
        }
      }
      nslot = slot;
      slot += SLOT; if (slot == 4 * SLOT) slot = 0;
    }
    TS(12);
    if (sjon) {
      if (w < 2) {
#pragma unroll
        for (int j = 0; j < 4; ++j) {
          int Dt = 4 * udb + j, cg = Dt / 5, nt = Dt - 5 * cg;
          *(u32x4*)(ws + WS_W2F + (size_t)(((cg * 48 + uks) * 5 + nt) * 1024) + lane * 16) = sj[j];
        }
      } else {
        int Dt = 4 * udb + q, cg = Dt / 5, nt = Dt - 5 * cg;
        char* dst = ws + WS_CF + (size_t)(((cg * 40 + uks) * 5 + nt) * 1024) + c * 16;
#pragma unroll
        for (int qq = 0; qq < 4; ++qq) *(u32x4*)(dst + qq * 256) = sj[qq];
      }
    }
#pragma unroll
    for (int t = 0; t < 6; ++t) sts32f(lb + E_CW + ((mi * 4 + q) * 96 + 16 * t + c) * 4, wsm[t]);
    BARRIER();
    if (w < 2) {
      float sx = 0.f, sq = 0.f;
#pragma unroll
      for (int k = 0; k < 16; ++k) { f32x2 v = lds64f(lb + E_ST + (k * 128 + tid) * 8); sx += v.x; sq += v.y; }
      float mean = sx * (1.f / 640.f);
      float var = fmaxf(sq * (1.f / 640.f) - mean * mean, 0.f);
      float den = rbf(sqrtf(rbf(rbf(var) + 1e-5f)));
      f32x2 v = {rbf(mean), 1.f / den};
      sts64f(lb + E_MR + tid * 8, v);
      if (g == 0) *(f32x2*)(ws + WS_ST + (128 * rc + tid) * 8) = v;
    } else {
      int col = tid - 128;
      if (col < 112) {
        unsigned a = col < 96 ? lb + E_CW + col * 4 : lb + E_C5 + (col - 96) * 4;
        unsigned st = col < 96 ? 384 : 64;
        float sm = 0.f;
#pragma unroll
        for (int k = 0; k < 16; ++k) sm += lds32f(a + k * st);
        sts32f(lb + E_CF + col * 4, sm);
      }
    }
    BARRIER();
    TS(13);
    f32x4 mr = lds128f(lb + E_MR + (8 * c + 2 * mi) * 8);
    __amdgpu_buffer_rsrc_t rs = mkrsrc(ws + WS_H1F, 6291456);
    const int fb = lane * 16 + (((2 * rc + (mi >> 1)) * 48 + 3 * g) * 4 + 2 * (mi & 1)) * 1024;
#pragma unroll
    for (int u = 0; u < 3; ++u) {
      f32x4 cs0 = lds128f(lb + E_CF + (32 * u + 4 * q) * 4), cs1 = lds128f(lb + E_CF + (32 * u + 16 + 4 * q) * 4);
#pragma unroll
      for (int i = 0; i < 2; ++i) {
        float m = mr[2 * i], r = mr[2 * i + 1];
        float v0[4], v1[4];
#pragma unroll
        for (int j = 0; j < 4; ++j) {
          v0[j] = siluf((acc[i][2 * u][j] - m * cs0[j]) * r);
          v1[j] = siluf((acc[i][2 * u + 1][j] - m * cs1[j]) * r);
        }
        u32x4 o = {pk2(v0[0], v0[1]), pk2(v0[2], v0[3]), pk2(v1[0], v1[1]), pk2(v1[2], v1[3])};
        st128(o, rs, fb + (u * 4 + i) * 1024);
      }
    }
    TS(14);
  } else {
    const int j = w - 4;
    unsigned pr[6];
    {
      int p0 = 40 * g + 8 * j + 2 * q, p1 = 40 * g + 32 + 2 * q;
      const u16* a0 = Lre + p0; const u16* a1 = Lim + p0; const u16* a2 = ldt + p0;
      const u16* a3 = Lre + p1; const u16* a4 = Lim + p1; const u16* a5 = ldt + p1;
      asm volatile("global_load_dword %0, %1, off" : "=v"(pr[0]) : "v"(a0) : "memory");
      asm volatile("global_load_dword %0, %1, off" : "=v"(pr[1]) : "v"(a1) : "memory");
      asm volatile("global_load_dword %0, %1, off" : "=v"(pr[2]) : "v"(a2) : "memory");
      asm volatile("global_load_dword %0, %1, off" : "=v"(pr[3]) : "v"(a3) : "memory");
      asm volatile("global_load_dword %0, %1, off" : "=v"(pr[4]) : "v"(a4) : "memory");
      asm volatile("global_load_dword %0, %1, off" : "=v"(pr[5]) : "v"(a5) : "memory");
    }
    issue(0); issue(SLOT); issue(2 * SLOT);
    if (w < 6) { WAITV(15); } else { WAITV(12); }
    asm volatile("" : "+v"(pr[0]), "+v"(pr[1]), "+v"(pr[2]), "+v"(pr[3]), "+v"(pr[4]), "+v"(pr[5]));
    Tab tb[2], t5[2];
    mktab(tb[0], bflo(pr[0]), bflo(pr[1]), bflo(pr[2]));
    mktab(tb[1], bfhi(pr[0]), bfhi(pr[1]), bfhi(pr[2]));
    mktab(t5[0], bflo(pr[3]), bflo(pr[4]), bflo(pr[5]));
    mktab(t5[1], bfhi(pr[3]), bfhi(pr[4]), bfhi(pr[5]));
    unsigned xa[2], wb[2];
#pragma unroll
    for (int p = 0; p < 2; ++p) {
      int kk = (j & 1) ^ p;
      unsigned sw = ((4 * kk + q) ^ (c & 7)) << 4;
      xa[p] = lb + c * 128 + sw;
      wb[p] = lb + OB + (16 * j + c) * 128 + sw;
    }
    const unsigned w5 = xa[0] + OB + 64 * 128;
    f32x4 acc[8], ac5[8];
    float sxs[8], sxq[8], cs = 0.f, cs5 = 0.f;
#pragma unroll
    for (int i = 0; i < 8; ++i) { acc[i] = (f32x4){0.f, 0.f, 0.f, 0.f}; ac5[i] = acc[i]; sxs[i] = 0.f; sxq[i] = 0.f; }
    unsigned slot = 0, nslot = 3 * SLOT;
    TS(1);
    for (int s = 0; s < 10; ++s) {
      STAGE_WAIT(s);
      BARRIER();
      TS(2 + s);
      if (s < 7) issue(nslot);
#pragma unroll
      for (int p = 0; p < 2; ++p) {
        u32x4 xf[8], wf;
        wf = lds128(wb[p] + slot);
#pragma unroll
        for (int i = 0; i < 8; ++i) xf[i] = lds128(xa[p] + slot + i * 2048);
#pragma unroll
        for (int i = 0; i < 8; ++i) acc[i] = mfma(wf, xf[i], acc[i]);
#pragma unroll
        for (int r = 0; r < 4; ++r) cs = dot2(wf[r], ONES, cs);
        if (p == 0) {
          if (((s ^ (j >> 1)) & 1) == 0) {
#pragma unroll
            for (int r = 0; r < 4; ++r)
#pragma unroll
              for (int i = 0; i < 8; ++i) {
                sxs[i] = dot2(xf[i][r], ONES, sxs[i]);
                sxq[i] = dot2(xf[i][r], xf[i][r], sxq[i]);
              }
          } else {
            u32x4 w5f = lds128(w5 + slot);
#pragma unroll
            for (int i = 0; i < 8; ++i) ac5[i] = mfma(w5f, xf[i], ac5[i]);
#pragma unroll
            for (int r = 0; r < 4; ++r) cs5 = dot2(w5f[r], ONES, cs5);
          }
        }
      }
      nslot = slot;
      slot += SLOT; if (slot == 4 * SLOT) slot = 0;
    }
    TS(12);
#pragma unroll
    for (int i = 0; i < 4; ++i) {
      f32x4 v = {sxs[2 * i], sxq[2 * i], sxs[2 * i + 1], sxq[2 * i + 1]};
      sts128f(lb + E_ST + ((j * 4 + q) * 128 + 8 * c + 2 * i) * 8, v);
    }
    sts32f(lb + E_CB + ((j * 4 + q) * 16 + c) * 4, cs);
    sts32f(lb + E_C5 + ((j * 4 + q) * 16 + c) * 4, cs5);
    if (j > 0) {
#pragma unroll
      for (int i = 0; i < 8; ++i) sts128f(lb + E_A5 + ((j - 1) * 8 + i) * 1024 + lane * 16, ac5[i]);
    }
    BARRIER();
    if (j == 0) {
#pragma unroll
      for (int k = 0; k < 3; ++k)
#pragma unroll
        for (int i = 0; i < 8; ++i) ac5[i] += lds128f(lb + E_A5 + (k * 8 + i) * 1024 + lane * 16);
    } else if (j == 1) {
      unsigned a = lb + E_CB + (q * 64 + c) * 4;
      sts32f(lb + E_CF + (112 + lane) * 4, lds32f(a) + lds32f(a + 64) + lds32f(a + 128) + lds32f(a + 192));
    }
    BARRIER();
    TS(13);
    f32x4 mr[4];
#pragma unroll
    for (int i = 0; i < 4; ++i) mr[i] = lds128f(lb + E_MR + (8 * c + 2 * i) * 8);
    f32x4 csv = lds128f(lb + E_CF + (112 + 16 * j + 4 * q) * 4);
    scan_tile(acc, tb, mr, csv, rc, 5 * g + j, lane, ws);
    if (j == 0) {
      csv = lds128f(lb + E_CF + (96 + 4 * q) * 4);
      scan_tile(ac5, t5, mr, csv, rc, 5 * g + 4, lane, ws);
    }
    TS(14);
  }
}

constexpr unsigned M_RO = 0;
constexpr unsigned M_RS = 61440;
constexpr unsigned M_CTL = M_RS + 81920;
constexpr unsigned K2_LDS = M_CTL + 64;

__global__ __launch_bounds__(512, 2) void k2(const u16* x, const u16* Lre, const u16* Lim, const u16* ldt, const u16* Dsk,
                                             char* ws, float* out) {
  __shared__ __attribute__((aligned(1024))) char smem[K2_LDS];
  const unsigned lb = (unsigned)(uintptr_t)(AS3 char*)smem;
  const int tid = threadIdx.x, lane = tid & 63;
  const int w = __builtin_amdgcn_readfirstlane(tid >> 6);
  const int c = lane & 15, q = lane >> 4;
  const int b = blockIdx.x, xcd = b & 7, jb = b >> 3;
  const int rb = xcd * 4 + (jb >> 3), cg = jb & 7;
  const int rc = rb >> 1, par = rb & 1;
  const int vo = lane * 16;
  volatile AS3 unsigned* ctl = (volatile AS3 unsigned*)(uintptr_t)(lb + M_CTL);
  TSDECL;
  TS(0);
  f32x4 acc[4][5];

  if (w >= 4) {
    const int sw = w - 4;
    __amdgpu_buffer_rsrc_t rA = mkrsrc(ws + WS_HF + (size_t)(rb * 40 + 10 * sw) * 4096, 10 * 4096);
    __amdgpu_buffer_rsrc_t rB = mkrsrc(ws + WS_CF + (size_t)(cg * 40 + 10 * sw) * 5120, 10 * 5120);
    u32x4 A[3][4], B[3][5];
    auto load = [&](int i) {
      int bf = i % 3;
#pragma unroll
      for (int m = 0; m < 4; ++m) A[bf][m] = __builtin_amdgcn_raw_buffer_load_b128(rA, vo + m * 1024, i * 4096, 0);
#pragma unroll
      for (int n = 0; n < 5; ++n) B[bf][n] = __builtin_amdgcn_raw_buffer_load_b128(rB, vo + n * 1024, i * 5120, 0);
    };
    const unsigned tb = lb + M_RS + sw * 20480;
    {
      int pl[3] = {lane, lane + 64, lane < 32 ? lane + 128 : lane};
      float lr[3], li[3], ld[3];
      f32x2 ag[3][15];
      const f32x2* AG = (const f32x2*)(ws + WS_AGG) + 160 * sw;
#pragma unroll
      for (int r = 0; r < 3; ++r) {
        lr[r] = bf2f(Lre[160 * sw + pl[r]]); li[r] = bf2f(Lim[160 * sw + pl[r]]); ld[r] = bf2f(ldt[160 * sw + pl[r]]);
      }
#pragma unroll
      for (int m = 0; m < 15; ++m) {
        int mm = m < rc ? m : 0;
#pragma unroll
        for (int r = 0; r < 3; ++r) ag[r][m] = AG[mm * 640 + pl[r]];
      }
      load(0);
      load(1);
      BARRIER();
#pragma unroll
      for (int r = 0; r < 3; ++r) {
        float dt = rbf(expf(ld[r]));
        float a = dt * lr[r] * 1.44269504088896f, bb = dt * li[r] * 0.15915494309189535f;
        f32x2 L = cpow(a, bb, 1.f), L128 = cpow(a, bb, 128.f);
        f32x2 cr = {0.f, 0.f};
#pragma unroll
        for (int m = 0; m < 15; ++m)
          if (m < rc) cr = cfma(L128, cr, ag[r][m]);
        if (r < 2 || lane < 32) {
          f32x4 t1 = {a, bb, cr.x, cr.y};
          sts128f(tb + pl[r] * 16, t1);
          sts64f(tb + 2560 + pl[r] * 8, L);
        }
      }
    }
    TS(1);
#pragma unroll
    for (int m = 0; m < 4; ++m)
#pragma unroll
      for (int n = 0; n < 5; ++n) acc[m][n] = (f32x4){0.f, 0.f, 0.f, 0.f};
    const float nf = (float)(8 * c + 4 * par + 1);
#pragma unroll
    for (int i = 0; i < 10; ++i) {
      if (i + 2 < 10) load(i + 2);
      u32x4 fr[4];
#pragma unroll
      for (int h = 0; h < 2; ++h) {
        int pl = 16 * i + 8 * h + 2 * q;
        f32x4 ta = lds128f(tb + pl * 16), tc = lds128f(tb + pl * 16 + 16), tl = lds128f(tb + 2560 + pl * 8);
        f32x2 z0 = cmul(cpow(ta.x, ta.y, nf), (f32x2){ta.z, ta.w});
        f32x2 z1 = cmul(cpow(tc.x, tc.y, nf), (f32x2){tc.z, tc.w});
        f32x2 L0 = {tl.x, tl.y}, L1 = {tl.z, tl.w};
#pragma unroll
        for (int m = 0; m < 4; ++m) {
          unsigned v0 = A[i % 3][2 * h + (m >> 1)][2 * (m & 1)], v1 = A[i % 3][2 * h + (m >> 1)][2 * (m & 1) + 1];
          fr[m][2 * h] = pk2(bflo(v0) + z0.x, bfhi(v0) + z0.y);
          fr[m][2 * h + 1] = pk2(bflo(v1) + z1.x, bfhi(v1) + z1.y);
          if (m < 3) { z0 = cmul(L0, z0); z1 = cmul(L1, z1); }
        }
      }
#pragma unroll
      for (int m = 0; m < 4; ++m)
#pragma unroll
        for (int n = 0; n < 5; ++n) acc[m][n] = mfma(B[i % 3][n], fr[m], acc[m][n]);
      TS(2 + i);
    }
#pragma unroll
    for (int m = 0; m < 4; ++m)
#pragma unroll
      for (int n = 0; n < 5; ++n) sts128f(tb + (m * 5 + n) * 1024 + vo, acc[m][n]);
    TS(14);
    BARRIER();
    TS(15);
    TSDUMP(33554432 + 1048576);
    return;
  }

  __amdgpu_buffer_rsrc_t rA = mkrsrc(ws + WS_H1F + (size_t)rb * 48 * 4096, 48 * 4096);
  __amdgpu_buffer_rsrc_t rB = mkrsrc(ws + WS_W2F + (size_t)cg * 48 * 5120, 48 * 5120);
  u32x4 A[4][4], B[4][5];
  auto load = [&](int i) {
    int bf = i & 3, ks = w + 4 * i;
#pragma unroll
    for (int m = 0; m < 4; ++m) A[bf][m] = __builtin_amdgcn_raw_buffer_load_b128(rA, vo + m * 1024, ks * 4096, 0);
#pragma unroll
    for (int n = 0; n < 5; ++n) B[bf][n] = __builtin_amdgcn_raw_buffer_load_b128(rB, vo + n * 1024, ks * 5120, 0);
  };
  load(0);
  load(1);
  load(2);
  if (tid == 0) ctl[0] = 0u;
  BARRIER();
  TS(1);
#pragma unroll
  for (int m = 0; m < 4; ++m)
#pragma unroll
    for (int n = 0; n < 5; ++n) acc[m][n] = (f32x4){0.f, 0.f, 0.f, 0.f};
  const int t = 128 * rc + 8 * c + 4 * par + w;
  u32x2 xv[5], dk[5];
  f32x2 st;
#pragma unroll
  for (int i = 0; i < 12; ++i) {
    if (i + 3 < 12) load(i + 3);
    if (i == 9) {
#pragma unroll
      for (int n = 0; n < 5; ++n) {
        xv[n] = *(const u32x2*)(x + t * 640 + 80 * cg + 16 * n + 4 * q);
        dk[n] = *(const u32x2*)(Dsk + 80 * cg + 16 * n + 4 * q);
      }
      st = *(const f32x2*)(ws + WS_ST + t * 8);
    }
    __builtin_amdgcn_sched_barrier(0);
#pragma unroll
    for (int m = 0; m < 4; ++m)
#pragma unroll
      for (int n = 0; n < 5; ++n) acc[m][n] = mfma(B[i & 3][n], A[i & 3][m], acc[m][n]);
    __builtin_amdgcn_sched_barrier(0);
    TS(2 + i);
  }
  f32x4 own[5];
#pragma unroll
  for (int m = 0; m < 4; ++m) {
    if (m == w) {
#pragma unroll
      for (int n = 0; n < 5; ++n) own[n] = acc[m][n];
    } else {
      unsigned a = lb + M_RO + (w * 3 + ((m - w - 1) & 3)) * 5120 + vo;
#pragma unroll
      for (int n = 0; n < 5; ++n) sts128f(a + n * 1024, acc[m][n]);
    }
  }
  WAITL0();
  if (lane == 0) __hip_atomic_fetch_add((AS3 unsigned*)(uintptr_t)(lb + M_CTL), 1u, __ATOMIC_RELAXED, __HIP_MEMORY_SCOPE_WORKGROUP);
  while (ctl[0] < 4u) __builtin_amdgcn_s_sleep(1);
  asm volatile("" ::: "memory");
#pragma unroll
  for (int s3 = 0; s3 < 3; ++s3) {
    int src = (w + 3 - s3) & 3;
#pragma unroll
    for (int n = 0; n < 5; ++n) own[n] += lds128f(lb + M_RO + ((src * 3 + s3) * 5 + n) * 1024 + vo);
  }
  f32x4 xs[5];
#pragma unroll
  for (int n = 0; n < 5; ++n) {
#pragma unroll
    for (int jj = 0; jj < 4; ++jj) {
      unsigned xw = xv[n][jj >> 1], dw = dk[n][jj >> 1];
      float xx = (jj & 1) ? bfhi(xw) : bflo(xw);
      float dd = (jj & 1) ? bfhi(dw) : bflo(dw);
      float gte = rbf(siluf(rbf(own[n][jj])));
      float xn = rbf(rbf(xx - st.x) * st.y);
      float sk = rbf(xn * dd);
      xs[n][jj] = xx + sk * gte;
      own[n][jj] = 2.f * gte;
    }
  }
  TS(14);
  BARRIER();
  TS(15);
#pragma unroll
  for (int n = 0; n < 5; ++n) {
    unsigned a = lb + M_RS + (w * 5 + n) * 1024 + vo;
    f32x4 s = (lds128f(a) + lds128f(a + 20480)) + (lds128f(a + 40960) + lds128f(a + 61440));
    *(f32x4*)(out + t * 640 + 80 * cg + 16 * n + 4 * q) = xs[n] + s * own[n];
  }
}

extern "C" void kernel_launch(void* const* d_in, const int* in_sizes, int n_in, void* d_out, int out_size, void* d_ws,
                              size_t ws_size, hipStream_t stream) {
  const u16* x = (const u16*)d_in[0];
  k1<<<256, 512, 0, stream>>>(x, (const u16*)d_in[1], (const u16*)d_in[2], (const u16*)d_in[3], (const u16*)d_in[4],
                              (const u16*)d_in[5], (const u16*)d_in[6], (const u16*)d_in[7], (const u16*)d_in[10],
                              (const u16*)d_in[11], (char*)d_ws);
  k2<<<256, 512, 0, stream>>>(x, (const u16*)d_in[1], (const u16*)d_in[2], (const u16*)d_in[7], (const u16*)d_in[8],
                              (char*)d_ws, (float*)d_out);
}
```

```cpp
#include <hip/hip_runtime.h>
#include <stdint.h>

typedef unsigned short u16;
typedef __attribute__((ext_vector_type(8))) __bf16 bf16x8;
typedef __attribute__((ext_vector_type(2))) __bf16 bf16x2;
typedef __attribute__((ext_vector_type(4))) float f32x4;
typedef __attribute__((ext_vector_type(2))) float f32x2;
typedef __attribute__((ext_vector_type(4))) unsigned u32x4;
typedef __attribute__((ext_vector_type(2))) unsigned u32x2;
typedef __attribute__((ext_vector_type(4))) short s16x4;
#define AS3 __attribute__((address_space(3)))
#define DEVI __device__ __forceinline__

constexpr size_t WS_H1F = 0;
constexpr size_t WS_HF = 6291456;
constexpr size_t WS_W2F = 11534336;
constexpr size_t WS_CF = 13500416;
constexpr size_t WS_AGG = 15138816;
constexpr size_t WS_AGL = 15220736;
constexpr size_t WS_TAB = 15302656;
constexpr size_t WS_ST = 15323136;

DEVI float bf2f(unsigned v) { return __uint_as_float(v << 16); }
DEVI float bflo(unsigned v) { return __uint_as_float(v << 16); }
DEVI float bfhi(unsigned v) { return __uint_as_float(v & 0xffff0000u); }
DEVI unsigned pk2(float a, float b) {
  bf16x2 r = {(__bf16)a, (__bf16)b};
  return __builtin_bit_cast(unsigned, r);
}
DEVI float rbf(float a) { return bflo(pk2(a, 0.f)); }
template <int N> DEVI float shr(float v) {
  return __builtin_bit_cast(float, __builtin_amdgcn_update_dpp(0, __builtin_bit_cast(int, v), 0x110 + N, 0xf, 0xf, true));
}
template <int N> DEVI f32x2 shr2(f32x2 v) { f32x2 r; r.x = shr<N>(v.x); r.y = shr<N>(v.y); return r; }
DEVI f32x2 cpow(float a, float b, float n) {
  float mg = __builtin_amdgcn_exp2f(n * a);
  float rv = __builtin_amdgcn_fractf(n * b);
  f32x2 r;
  r.x = mg * __builtin_amdgcn_cosf(rv);
  r.y = mg * __builtin_amdgcn_sinf(rv);
  return r;
}
DEVI f32x2 cmul(f32x2 a, f32x2 b) { f32x2 r; r.x = a.x * b.x - a.y * b.y; r.y = a.x * b.y + a.y * b.x; return r; }
DEVI f32x2 pfma(f32x2 a, f32x2 b, f32x2 c) { return __builtin_elementwise_fma(a, b, c); }
DEVI f32x2 cfmap(f32x2 a, f32x2 b, f32x2 c) {
  f32x2 ax = {a.x, a.x}, ay = {-a.y, a.y}, bs = {b.y, b.x};
  return pfma(ay, bs, pfma(ax, b, c));
}
DEVI f32x2 cmulp(f32x2 a, f32x2 b) {
  f32x2 ax = {a.x, a.x}, ay = {-a.y, a.y}, bs = {b.y, b.x};
  return pfma(ay, bs, ax * b);
}
DEVI float siluf(float a) { return a * __builtin_amdgcn_rcpf(1.f + __builtin_amdgcn_exp2f(-1.44269504088896f * a)); }
DEVI void swap16(unsigned& a, unsigned& b) { auto r = __builtin_amdgcn_permlane16_swap(a, b, false, false); a = r[0]; b = r[1]; }
DEVI void swap32(unsigned& a, unsigned& b) { auto r = __builtin_amdgcn_permlane32_swap(a, b, false, false); a = r[0]; b = r[1]; }
DEVI void swap16f(float& a, float& b) { unsigned x = __float_as_uint(a), y = __float_as_uint(b); swap16(x, y); a = __uint_as_float(x); b = __uint_as_float(y); }
DEVI void swap32f(float& a, float& b) { unsigned x = __float_as_uint(a), y = __float_as_uint(b); swap32(x, y); a = __uint_as_float(x); b = __uint_as_float(y); }

#define WAITV(n) asm volatile("s_waitcnt vmcnt(%0)" ::"n"(n) : "memory")
#define WAITL0() asm volatile("s_waitcnt lgkmcnt(0)" ::: "memory")
#define BARRIER() do { WAITL0(); __builtin_amdgcn_s_barrier(); asm volatile("" ::: "memory"); } while (0)
#define BARRIER_NW() do { asm volatile("" ::: "memory"); __builtin_amdgcn_s_barrier(); asm volatile("" ::: "memory"); } while (0)
#define SB() __builtin_amdgcn_sched_barrier(0)
DEVI void glds(const void* p, unsigned ldsaddr) {
  unsigned keep;
  asm volatile("s_mov_b32 %0, m0\n\ts_mov_b32 m0, %2\n\ts_nop 0\n\tglobal_load_lds_dwordx4 %1, off\n\ts_mov_b32 m0, %0"
               : "=&s"(keep) : "v"(p), "s"(ldsaddr) : "memory");
}

DEVI u32x4 lds128(unsigned off) { return *(const AS3 u32x4*)(uintptr_t)off; }
DEVI f32x4 lds128f(unsigned off) { return *(const AS3 f32x4*)(uintptr_t)off; }
DEVI f32x2 lds64f(unsigned off) { return *(const AS3 f32x2*)(uintptr_t)off; }
DEVI void sts128f(unsigned off, f32x4 v) { *(AS3 f32x4*)(uintptr_t)off = v; }
DEVI void sts64f(unsigned off, f32x2 v) { *(AS3 f32x2*)(uintptr_t)off = v; }
DEVI void sts32f(unsigned off, float v) { *(AS3 float*)(uintptr_t)off = v; }
DEVI u32x2 ldstr(unsigned off) {
  s16x4 r = __builtin_amdgcn_ds_read_tr16_b64_v4i16((AS3 s16x4*)(uintptr_t)off);
  return __builtin_bit_cast(u32x2, r);
}
DEVI u32x4 ldstr2(unsigned a, unsigned d) { u32x2 lo = ldstr(a), hi = ldstr(a + d); return (u32x4){lo.x, lo.y, hi.x, hi.y}; }
DEVI f32x4 mfma(u32x4 a, u32x4 b, f32x4 c) {
  return __builtin_amdgcn_mfma_f32_16x16x32_bf16(__builtin_bit_cast(bf16x8, a), __builtin_bit_cast(bf16x8, b), c, 0, 0, 0);
}
DEVI void st128(u32x4 v, __amdgpu_buffer_rsrc_t rs, int off) { __builtin_amdgcn_raw_buffer_store_b128(v, rs, off, 0, 16); }
DEVI __amdgpu_buffer_rsrc_t mkrsrc(const void* p, unsigned bytes) {
  return __builtin_amdgcn_make_buffer_rsrc((void*)p, 0, bytes, 0x00020000);
}
#define TSDECL
#define TS(k)
#define TSDUMP(base)

constexpr unsigned SLOT = 38912, OB = 16384, OW = 26624;
constexpr unsigned SPARE = 4 * SLOT;
constexpr unsigned T_TAB = SPARE;
constexpr unsigned T_AG = SPARE + 2560;
constexpr unsigned T_FLAG = SPARE + 3072;
constexpr unsigned K1_LDS = SPARE + 4096;
constexpr unsigned S_W2 = 2 * SLOT;
constexpr unsigned E_ST = 3 * SLOT;
constexpr unsigned E_CS = E_ST + 2048;
constexpr unsigned E_A5 = E_CS + 2048;

DEVI void stat(u32x4 f, float& s, float& q) {
#pragma unroll
  for (int r = 0; r < 4; ++r) {
    float lo = bflo(f[r]), hi = bfhi(f[r]);
    s += lo; q = fmaf(lo, lo, q);
    s += hi; q = fmaf(hi, hi, q);
  }
}
template <int NM, int NV> DEVI void ileave() {
#pragma unroll
  for (int i = 0; i < NM; ++i) {
    __builtin_amdgcn_sched_group_barrier(0x008, 1, 0);
    __builtin_amdgcn_sched_group_barrier(0x002, NV, 0);
  }
}
struct Tab { f32x2 L, L4, L8, L16, L32, L64, co; };
DEVI void rdtab(Tab& t, unsigned a) {
  f32x4 v0 = lds128f(a), v1 = lds128f(a + 16), v2 = lds128f(a + 32);
  f32x2 v3 = lds64f(a + 48);
  t.L = (f32x2){v0.x, v0.y}; t.L4 = (f32x2){v0.z, v0.w}; t.L8 = (f32x2){v1.x, v1.y}; t.L16 = (f32x2){v1.z, v1.w};
  t.L32 = (f32x2){v2.x, v2.y}; t.L64 = (f32x2){v2.z, v2.w}; t.co = v3;
}

__global__ __launch_bounds__(512, 2) void k1(const u16* x, const u16* Lre, const u16* Lim, const u16* Bre, const u16* Bim,
                                             const u16* Cre, const u16* Cim, const u16* ldt, const u16* w1, const u16* w2,
                                             char* ws) {
  __shared__ __attribute__((aligned(1024))) char smem[K1_LDS];
  const unsigned lb = (unsigned)(uintptr_t)(AS3 char*)smem;
  const int b = blockIdx.x, xcd = b & 7, jb = b >> 3;
  const int rc = (xcd & 1) * 8 + (jb & 7), g = (xcd >> 1) * 4 + (jb >> 3);
  const int tid = threadIdx.x, lane = tid & 63;
  const int w = __builtin_amdgcn_readfirstlane(tid >> 6);
  const int c = lane & 15, q = lane >> 4;
  const int l3 = lane >> 3, l7 = lane & 7;
  const int h = (w >> 1) & 1, a = w & 1;
  const int kk0 = h ^ a;
  TSDECL;
  TS(0);

  unsigned pr[3];
  if (w == 7) {
    int p = 40 * g + (lane < 40 ? lane : 0);
    const u16* a0 = Lre + p; const u16* a1 = Lim + p; const u16* a2 = ldt + p;
    asm volatile("global_load_ushort %0, %1, off" : "=v"(pr[0]) : "v"(a0) : "memory");
    asm volatile("global_load_ushort %0, %1, off" : "=v"(pr[1]) : "v"(a1) : "memory");
    asm volatile("global_load_ushort %0, %1, off" : "=v"(pr[2]) : "v"(a2) : "memory");
  }
  const char* src[5];
  unsigned dsto[5];
  int step3;
#pragma unroll
  for (int v = 0; v < 2; ++v) {
    int rho = 8 * (w + 8 * v) + l3;
    int t = 128 * rc + 64 * v + 4 * (rho & 15) + ((rho >> 4) & 3);
    src[v] = (const char*)x + t * 1280 + ((l7 ^ l3) << 4);
    dsto[v] = (w + 8 * v) * 1024;
  }
  {
    int n = 8 * w + l3;
    src[2] = (const char*)((n & 1) ? Bim : Bre) + (40 * g + (n >> 1)) * 1280 + ((l7 ^ l3) << 4);
    dsto[2] = OB + w * 1024;
    auto wsrc = [&](int e) {
      int idx = 64 * e + lane, k = idx / 12, uu = idx - 12 * k;
      return (const char*)w1 + k * 3072 + 192 * g + (((uu >> 1) ^ ((k >> 3) & 1)) << 5) + ((uu & 1) << 4);
    };
    if (w < 2) {
      n = 64 + 8 * w + l3;
      src[3] = (const char*)((n & 1) ? Bim : Bre) + (40 * g + (n >> 1)) * 1280 + ((l7 ^ l3) << 4);
      dsto[3] = OB + (8 + w) * 1024; step3 = 128;
    } else {
      src[3] = wsrc(w - 2); dsto[3] = OW + (w - 2) * 1024; step3 = 196608;
    }
    src[4] = wsrc(w < 6 ? 6 + w : 0); dsto[4] = OW + (6 + w) * 1024;
  }
  auto issue = [&](unsigned slot) {
    glds(src[0], lb + slot + dsto[0]); glds(src[1], lb + slot + dsto[1]); glds(src[2], lb + slot + dsto[2]);
    glds(src[3], lb + slot + dsto[3]);
    if (w < 6) glds(src[4], lb + slot + dsto[4]);
    src[0] += 128; src[1] += 128; src[2] += 128; src[3] += step3; src[4] += 196608;
  };
#define WAIT1() do { if (w < 6) WAITV(5); else WAITV(4); } while (0)
  issue(0); issue(SLOT); issue(2 * SLOT);
  const u32x4 ONES = {0x3f803f80u, 0x3f803f80u, 0x3f803f80u, 0x3f803f80u};
  unsigned xo[2];
#pragma unroll
  for (int p = 0; p < 2; ++p) xo[p] = lb + 8192 * h + c * 128 + (((4 * (kk0 ^ p) + q) ^ (c & 7)) << 4);
  float sv[4] = {0.f, 0.f, 0.f, 0.f};
  const int rb = 2 * rc + h;

  auto mr4 = [&](float* m, float* r) {
#pragma unroll
    for (int i = 0; i < 4; ++i) {
      f32x4 v = lds128f(lb + E_ST + (64 * h + 4 * c + i) * 16);
      float mean = (v.x + v.z) * (1.f / 640.f);
      float var = fmaxf((v.y + v.w) * (1.f / 640.f) - mean * mean, 0.f);
      float den = rbf(__builtin_amdgcn_sqrtf(rbf(rbf(var) + 1e-5f)));
      m[i] = rbf(mean); r[i] = __builtin_amdgcn_rcpf(den);
    }
  };
  auto put_stats = [&](int i0) {
    swap32f(sv[0], sv[2]); swap32f(sv[1], sv[3]);
    float w0 = sv[0] + sv[2], w1v = sv[1] + sv[3];
    swap16f(w0, w1v);
    sts32f(lb + E_ST + ((64 * h + 4 * c + i0 + (q >> 1)) * 4 + 2 * a + (q & 1)) * 4, w0 + w1v);
  };

  if (w < 4) {
    unsigned wo[3];
#pragma unroll
    for (int u = 0; u < 3; ++u) {
      int tl = a ? (u == 2 ? 3 : 4 + u) : u;
      wo[u] = lb + OW + (8 * q + (c >> 2)) * 192 + (c & 3) * 8 + ((tl ^ (q & 1)) << 5);
    }
    const unsigned kq0 = kk0 * 6144, kq1 = 6144 - kq0;
    f32x4 acc[4][3], cs[3];
#pragma unroll
    for (int u = 0; u < 3; ++u) {
      cs[u] = (f32x4){0.f, 0.f, 0.f, 0.f};
#pragma unroll
      for (int i = 0; i < 4; ++i) acc[i][u] = cs[u];
    }
    u32x4 X0[4], W0[3], X1[4], W1[3];
    auto rd0 = [&](unsigned slot) {
#pragma unroll
      for (int u = 0; u < 3; ++u) W0[u] = ldstr2(wo[u] + slot + kq0, 768);
#pragma unroll
      for (int i = 0; i < 4; ++i) X0[i] = lds128(xo[0] + slot + 2048 * i);
    };
    auto rd1 = [&](unsigned slot) {
#pragma unroll
      for (int u = 0; u < 3; ++u) W1[u] = ldstr2(wo[u] + slot + kq1, 768);
#pragma unroll
      for (int i = 0; i < 4; ++i) X1[i] = lds128(xo[1] + slot + 2048 * i);
    };
    auto mm0 = [&]() {
#pragma unroll
      for (int i = 0; i < 4; ++i)
#pragma unroll
        for (int u = 0; u < 3; ++u) acc[i][u] = mfma(W0[u], X0[i], acc[i][u]);
      stat(X0[0], sv[0], sv[1]);
      ileave<12, 2>();
    };
    auto mm1 = [&]() {
#pragma unroll
      for (int i = 0; i < 4; ++i)
#pragma unroll
        for (int u = 0; u < 3; ++u) acc[i][u] = mfma(W1[u], X1[i], acc[i][u]);
#pragma unroll
      for (int u = 0; u < 3; ++u) cs[u] = mfma(W1[u], ONES, cs[u]);
      stat(X1[1], sv[2], sv[3]);
      ileave<12, 2>();
    };
    int unit = w < 2 ? 2 * b + w : 2 * b + (w - 2);
    if (w < 2) { if (unit >= 480) unit -= 32; } else { if (unit >= 400) unit -= 112; }
    const int uks = unit / 10, udb = unit - 10 * uks;
    u32x4 sj[4];

    WAITV(10);
    BARRIER_NW();
    TS(1);
    rd0(0);
    unsigned slot = 0, nslot = SLOT, fslot = 3 * SLOT;
    for (int s = 0; s < 7; ++s) {
      rd1(slot); SB();
      mm0(); SB();
      WAIT1();
      BARRIER_NW();
      TS(2 + s);
      issue(fslot);
      rd0(nslot); SB();
      mm1(); SB();
      fslot = slot; slot = nslot;
      nslot += SLOT; if (nslot == 4 * SLOT) nslot = 0;
    }
    rd1(3 * SLOT); SB();
    mm0(); SB();
    WAIT1();
    BARRIER_NW();
    TS(9);
    if (w < 2) {
#pragma unroll
      for (int v = 0; v < 4; ++v) {
        int r = 8 * v + l3;
        int f = (r >> 1) & 3;
        glds((const char*)w2 + (32 * uks + r) * 1280 + udb * 128 + ((l7 ^ (f << 1)) << 4), lb + S_W2 + w * 4096 + v * 1024);
      }
    } else {
      int G = uks / 5, kr = uks - 5 * G;
      int p0 = 80 * G + (kr < 4 ? 40 * (kr >> 1) + 16 * (kr & 1) : 32);
      int d1 = kr < 4 ? 8 : 40;
      const u16* c0 = Cre + (64 * udb + lane) * 640 + p0;
      const u16* c1 = Cim + (64 * udb + lane) * 640 + p0;
      const u16* c2 = c0 + d1; const u16* c3 = c1 + d1;
      asm volatile("global_load_dwordx4 %0, %1, off" : "=v"(sj[0]) : "v"(c0) : "memory");
      asm volatile("global_load_dwordx4 %0, %1, off" : "=v"(sj[1]) : "v"(c2) : "memory");
      asm volatile("global_load_dwordx4 %0, %1, off" : "=v"(sj[2]) : "v"(c1) : "memory");
      asm volatile("global_load_dwordx4 %0, %1, off" : "=v"(sj[3]) : "v"(c3) : "memory");
    }
    rd0(0); SB();
    mm1(); SB();
    rd1(0); SB();
    mm0(); SB();
    WAITV(4);
    BARRIER_NW();
    TS(10);
    rd0(SLOT); SB();
    mm1(); SB();
    rd1(SLOT); SB();
    mm0(); SB();
    WAITV(0);
    if (w < 2) {
      const bool nat = (uks % 3) == 1;
      int r0 = (nat ? 8 * q : 4 * q) + (c >> 2);
      int dr = nat ? 4 : 16;
      unsigned a0 = lb + S_W2 + w * 4096 + r0 * 128 + (c & 3) * 8;
      int f0 = (r0 >> 1) & 3, f1 = ((r0 + dr) >> 1) & 3;
#pragma unroll
      for (int j = 0; j < 4; ++j) {
        u32x2 lo = ldstr(a0 + ((j ^ f0) << 5));
        u32x2 hi = ldstr(a0 + dr * 128 + ((j ^ f1) << 5));
        int Dt = 4 * udb + j, cg = Dt / 5, nt = Dt - 5 * cg;
        *(u32x4*)(ws + WS_W2F + (size_t)(((cg * 48 + uks) * 5 + nt) * 1024) + lane * 16) = (u32x4){lo.x, lo.y, hi.x, hi.y};
      }
    } else {
      asm volatile("" : "+v"(sj[0]), "+v"(sj[1]), "+v"(sj[2]), "+v"(sj[3]));
      unsigned P[2][4][2];
#pragma unroll
      for (int ga = 0; ga < 2; ++ga)
#pragma unroll
        for (int j = 0; j < 4; ++j) {
          unsigned im = sj[2 + ga][j] ^ 0x80008000u;
          P[ga][j][0] = __builtin_amdgcn_perm(im, sj[ga][j], 0x05040100u);
          P[ga][j][1] = __builtin_amdgcn_perm(im, sj[ga][j], 0x07060302u);
        }
      int Dt = 4 * udb + q, cg = Dt / 5, nt = Dt - 5 * cg;
      char* dst = ws + WS_CF + (size_t)(((cg * 40 + uks) * 5 + nt) * 1024) + c * 16;
      if (uks % 5 < 4) {
#pragma unroll
        for (int j = 0; j < 4; ++j) *(u32x4*)(dst + j * 256) = (u32x4){P[0][j][0], P[0][j][1], P[1][j][0], P[1][j][1]};
      } else {
#pragma unroll
        for (int j = 0; j < 4; ++j)
          *(u32x4*)(dst + j * 256) = (u32x4){P[j >> 1][0][j & 1], P[j >> 1][1][j & 1], P[j >> 1][2][j & 1], P[j >> 1][3][j & 1]};
      }
    }
    SB();
    mm1(); SB();
    TS(11);
    put_stats(0);
    if (c == 0) {
#pragma unroll
      for (int u = 0; u < 3; ++u) sts128f(lb + E_CS + ((w * 3 + u) * 4 + q) * 16, cs[u]);
    }
    BARRIER();
    TS(12);
    float m[4], r[4];
    mr4(m, r);
#pragma unroll
    for (int u = 0; u < 3; ++u) cs[u] += lds128f(lb + E_CS + (((w ^ 2) * 3 + u) * 4 + q) * 16);
    if (g == 0 && a == 0 && q == 0) {
      f32x4* st = (f32x4*)(ws + WS_ST + (64 * rb + 4 * c) * 8);
      st[0] = (f32x4){m[0], r[0], m[1], r[1]};
      st[1] = (f32x4){m[2], r[2], m[3], r[3]};
    }
    __amdgpu_buffer_rsrc_t rs = mkrsrc(ws + WS_H1F, 6291456);
    unsigned o[4][6];
#pragma unroll
    for (int i = 0; i < 4; ++i) {
      f32x2 nm = {-m[i], -m[i]}, rr = {r[i], r[i]}, re = {-1.44269504088896f * r[i], -1.44269504088896f * r[i]};
#pragma unroll
      for (int u = 0; u < 3; ++u)
#pragma unroll
        for (int hf = 0; hf < 2; ++hf) {
          f32x2 v = pfma((f32x2){cs[u][2 * hf], cs[u][2 * hf + 1]}, nm, (f32x2){acc[i][u][2 * hf], acc[i][u][2 * hf + 1]});
          f32x2 e = v * re;
          e.x = __builtin_amdgcn_exp2f(e.x); e.y = __builtin_amdgcn_exp2f(e.y);
          e = e + (f32x2){1.f, 1.f};
          e.x = __builtin_amdgcn_rcpf(e.x); e.y = __builtin_amdgcn_rcpf(e.y);
          v = (v * rr) * e;
          o[i][2 * u + hf] = pk2(v.x, v.y);
        }
      st128((u32x4){o[i][0], o[i][1], o[i][2], o[i][3]}, rs, lane * 16 + ((rb * 48 + 3 * g + 2 * a) * 4 + i) * 1024);
      if (i & 1) {
        swap16(o[i - 1][4], o[i][4]); swap16(o[i - 1][5], o[i][5]);
        st128((u32x4){o[i - 1][4], o[i - 1][5], o[i][4], o[i][5]}, rs,
              (c + 16 * (2 * a + (q >> 1))) * 16 + ((rb * 48 + 3 * g + 1) * 4 + (i - 1) + (q & 1)) * 1024);
      }
    }
    TS(13);
  } else {
    if (w == 7) {
      WAITV(12);
      asm volatile("" : "+v"(pr[0]), "+v"(pr[1]), "+v"(pr[2]));
      float lr = bf2f(pr[0]), li = bf2f(pr[1]), ld = bf2f(pr[2]);
      float dt = rbf(expf(ld));
      float ea = dt * lr * 1.44269504088896f, eb = dt * li * 0.15915494309189535f;
      f32x2 L = cpow(ea, eb, 1.f), L4 = cpow(ea, eb, 4.f), L8 = cpow(ea, eb, 8.f), L16 = cpow(ea, eb, 16.f);
      f32x2 L32 = cpow(ea, eb, 32.f), L64 = cpow(ea, eb, 64.f), L128 = cpow(ea, eb, 128.f);
      float nr = L.x - 1.f, ni = L.y, dd = 1.f / (lr * lr + li * li);
      f32x2 co = {(nr * lr + ni * li) * dd, (ni * lr - nr * li) * dd};
      if (lane < 40) {
        unsigned ta = lb + T_TAB + lane * 64;
        sts128f(ta, (f32x4){L.x, L.y, L4.x, L4.y});
        sts128f(ta + 16, (f32x4){L8.x, L8.y, L16.x, L16.y});
        sts128f(ta + 32, (f32x4){L32.x, L32.y, L64.x, L64.y});
        sts64f(ta + 48, co);
        if (rc == 0) {
          int sg = lane < 32 ? (5 * (g >> 1) + 2 * (g & 1) + (lane >> 4)) * 16 + 4 * ((lane >> 1) & 3) + 2 * ((lane >> 3) & 1) + (lane & 1)
                             : (5 * (g >> 1) + 4) * 16 + 8 * (g & 1) + 4 * (lane & 1) + ((lane - 32) >> 1);
          f32x4* tg = (f32x4*)(ws + WS_TAB + sg * 32);
          tg[0] = (f32x4){ea, eb, L.x, L.y};
          tg[1] = (f32x4){L64.x, L64.y, L128.x, L128.y};
        }
      }
      if (lane < 2) sts32f(lb + T_FLAG + lane * 4, 0.f);
    }
    unsigned bo[2];
#pragma unroll
    for (int p = 0; p < 2; ++p) bo[p] = xo[p] - 8192 * h + OB + 4096 * a;
    const int r5 = c ^ (2 * a);
    const unsigned f5 = lb + OB + 8192 + r5 * 128 + (((4 * kk0 + q) ^ (r5 & 7)) << 4);
    f32x4 acc[4][2], ac5[5], cs[2];
#pragma unroll
    for (int i = 0; i < 4; ++i) { acc[i][0] = (f32x4){0.f, 0.f, 0.f, 0.f}; acc[i][1] = acc[i][0]; ac5[i] = acc[i][0]; }
    ac5[4] = ac5[0]; cs[0] = ac5[0]; cs[1] = ac5[0];
    u32x4 X0[4], W0[2], W5, X1[4], W1[2];
    auto rd0 = [&](unsigned slot) {
      W0[0] = lds128(bo[0] + slot); W0[1] = lds128(bo[0] + slot + 2048);
#pragma unroll
      for (int i = 0; i < 4; ++i) X0[i] = lds128(xo[0] + slot + 2048 * i);
      W5 = lds128(f5 + slot);
    };
    auto rd1 = [&](unsigned slot) {
      W1[0] = lds128(bo[1] + slot); W1[1] = lds128(bo[1] + slot + 2048);
#pragma unroll
      for (int i = 0; i < 4; ++i) X1[i] = lds128(xo[1] + slot + 2048 * i);
    };
    auto mm0 = [&]() {
#pragma unroll
      for (int i = 0; i < 4; ++i) {
        acc[i][0] = mfma(W0[0], X0[i], acc[i][0]);
        acc[i][1] = mfma(W0[1], X0[i], acc[i][1]);
      }
#pragma unroll
      for (int i = 0; i < 4; ++i) ac5[i] = mfma(W5, X0[i], ac5[i]);
      ac5[4] = mfma(W5, ONES, ac5[4]);
      stat(X0[2], sv[0], sv[1]);
      ileave<12, 2>();
    };
    auto mm1 = [&]() {
#pragma unroll
      for (int i = 0; i < 4; ++i) {
        acc[i][0] = mfma(W1[0], X1[i], acc[i][0]);
        acc[i][1] = mfma(W1[1], X1[i], acc[i][1]);
      }
      cs[0] = mfma(W1[0], ONES, cs[0]);
      cs[1] = mfma(W1[1], ONES, cs[1]);
      stat(X1[3], sv[2], sv[3]);
      ileave<8, 3>();
    };
    if (w < 6) WAITV(10); else WAITV(8);
    BARRIER();
    TS(1);
    rd0(0);
    unsigned slot = 0, nslot = SLOT, fslot = 3 * SLOT;
    for (int s = 0; s < 7; ++s) {
      rd1(slot); SB();
      mm0(); SB();
      WAIT1();
      BARRIER_NW();
      TS(2 + s);
      issue(fslot);
      rd0(nslot); SB();
      mm1(); SB();
      fslot = slot; slot = nslot;
      nslot += SLOT; if (nslot == 4 * SLOT) nslot = 0;
    }
    rd1(3 * SLOT); SB();
    mm0(); SB();
    WAIT1();
    BARRIER_NW();
    TS(9);
    rd0(0); SB();
    mm1(); SB();
    rd1(0); SB();
    mm0(); SB();
    WAITV(0);
    BARRIER_NW();
    TS(10);
    rd0(SLOT); SB();
    mm1(); SB();
    rd1(SLOT); SB();
    mm0(); SB();
    Tab tb[5];
#pragma unroll
    for (int k = 0; k < 4; ++k) rdtab(tb[k], lb + T_TAB + (16 * a + 8 * (k >> 1) + 2 * q + (k & 1)) * 64);
    rdtab(tb[4], lb + T_TAB + (32 + 2 * q + a) * 64);
    mm1(); SB();
    TS(11);
    put_stats(2);
    if (c == 0) {
      sts128f(lb + E_CS + ((w * 3 + 0) * 4 + q) * 16, cs[0]);
      sts128f(lb + E_CS + ((w * 3 + 1) * 4 + q) * 16, cs[1]);
    }
#pragma unroll
    for (int i = 0; i < 5; ++i) sts64f(lb + E_A5 + (((w - 4) * 5 + i) * 64 + lane) * 8, (f32x2){ac5[i].z, ac5[i].w});
    BARRIER();
    TS(12);
    float m[4], r[4];
    mr4(m, r);
    cs[0] += lds128f(lb + E_CS + (((w ^ 2) * 3 + 0) * 4 + q) * 16);
    cs[1] += lds128f(lb + E_CS + (((w ^ 2) * 3 + 1) * 4 + q) * 16);
    f32x2 u[5][4], csv[5];
#pragma unroll
    for (int i = 0; i < 4; ++i) {
      u[0][i] = (f32x2){acc[i][0].x, acc[i][0].y}; u[1][i] = (f32x2){acc[i][0].z, acc[i][0].w};
      u[2][i] = (f32x2){acc[i][1].x, acc[i][1].y}; u[3][i] = (f32x2){acc[i][1].z, acc[i][1].w};
      u[4][i] = (f32x2){ac5[i].x, ac5[i].y} + lds64f(lb + E_A5 + ((((w - 4) ^ 1) * 5 + i) * 64 + lane) * 8);
    }
    csv[0] = (f32x2){cs[0].x, cs[0].y}; csv[1] = (f32x2){cs[0].z, cs[0].w};
    csv[2] = (f32x2){cs[1].x, cs[1].y}; csv[3] = (f32x2){cs[1].z, cs[1].w};
    csv[4] = (f32x2){ac5[4].x, ac5[4].y} + lds64f(lb + E_A5 + ((((w - 4) ^ 1) * 5 + 4) * 64 + lane) * 8);
#pragma unroll
    for (int i = 0; i < 4; ++i) {
      f32x2 nm = {-m[i], -m[i]}, rr = {r[i], r[i]};
#pragma unroll
      for (int k = 0; k < 5; ++k) u[k][i] = cmulp(tb[k].co, pfma(csv[k], nm, u[k][i]) * rr);
    }
    f32x2 hh[5];
#pragma unroll
    for (int k = 0; k < 5; ++k) hh[k] = u[k][0];
#pragma unroll
    for (int i = 1; i < 4; ++i)
#pragma unroll
      for (int k = 0; k < 5; ++k) hh[k] = cfmap(tb[k].L, hh[k], u[k][i]);
#pragma unroll
    for (int k = 0; k < 5; ++k) hh[k] = cfmap(tb[k].L4, shr2<1>(hh[k]), hh[k]);
#pragma unroll
    for (int k = 0; k < 5; ++k) hh[k] = cfmap(tb[k].L8, shr2<2>(hh[k]), hh[k]);
#pragma unroll
    for (int k = 0; k < 5; ++k) hh[k] = cfmap(tb[k].L16, shr2<4>(hh[k]), hh[k]);
#pragma unroll
    for (int k = 0; k < 5; ++k) hh[k] = cfmap(tb[k].L32, shr2<8>(hh[k]), hh[k]);
    const int ksr = 5 * (g >> 1) + 2 * (g & 1) + a, ks5 = 5 * (g >> 1) + 4;
    f32x2 ag[5];
#pragma unroll
    for (int k = 0; k < 5; ++k) ag[k] = hh[k];
    if (h == 0) {
      if (c == 15) {
        unsigned ta = lb + T_AG + (a * 4 + q) * 48;
        sts128f(ta, (f32x4){ag[0].x, ag[0].y, ag[1].x, ag[1].y});
        sts128f(ta + 16, (f32x4){ag[2].x, ag[2].y, ag[3].x, ag[3].y});
        sts64f(ta + 32, ag[4]);
      }
      WAITL0();
      if (lane == 0) *(volatile AS3 unsigned*)(uintptr_t)(lb + T_FLAG + a * 4) = 1u;
    }
#pragma unroll
    for (int k = 0; k < 5; ++k) hh[k] = shr2<1>(hh[k]);
    __amdgpu_buffer_rsrc_t rs = mkrsrc(ws + WS_HF, 5242880);
    unsigned o5[4];
#pragma unroll
    for (int i = 0; i < 4; ++i) {
      u32x4 o;
#pragma unroll
      for (int k = 0; k < 5; ++k) {
        hh[k] = cfmap(tb[k].L, hh[k], u[k][i]);
        if (k < 4) o[k] = pk2(hh[k].x, hh[k].y); else o5[i] = pk2(hh[k].x, hh[k].y);
      }
      st128(o, rs, lane * 16 + ((rb * 40 + ksr) * 4 + i) * 1024);
    }
    swap32(o5[0], o5[2]); swap32(o5[1], o5[3]);
    swap16(o5[0], o5[1]); swap16(o5[2], o5[3]);
    st128((u32x4){o5[0], o5[1], o5[2], o5[3]}, rs, (c + 16 * (2 * (g & 1) + a)) * 16 + ((rb * 40 + ks5) * 4 + q) * 1024);
    if (h == 1) {
      while (*(volatile AS3 unsigned*)(uintptr_t)(lb + T_FLAG + a * 4) == 0u) __builtin_amdgcn_s_sleep(1);
      asm volatile("" ::: "memory");
      unsigned ta = lb + T_AG + (a * 4 + q) * 48;
      f32x4 l0 = lds128f(ta), l1 = lds128f(ta + 16);
      f32x2 l2 = lds64f(ta + 32);
      ag[0] = cfmap(tb[0].L64, (f32x2){l0.x, l0.y}, ag[0]); ag[1] = cfmap(tb[1].L64, (f32x2){l0.z, l0.w}, ag[1]);
      ag[2] = cfmap(tb[2].L64, (f32x2){l1.x, l1.y}, ag[2]); ag[3] = cfmap(tb[3].L64, (f32x2){l1.z, l1.w}, ag[3]);
      ag[4] = cfmap(tb[4].L64, l2, ag[4]);
    }
    if (c == 15) {
      char* base = ws + (h ? WS_AGG : WS_AGL) + rc * 5120;
      f32x4* d0 = (f32x4*)(base + (ksr * 16 + 4 * q) * 8);
      d0[0] = (f32x4){ag[0].x, ag[0].y, ag[1].x, ag[1].y};
      d0[1] = (f32x4){ag[2].x, ag[2].y, ag[3].x, ag[3].y};
      *(f32x2*)(base + (ks5 * 16 + 8 * (g & 1) + 4 * a + q) * 8) = ag[4];
    }
    TS(13);
  }
}

constexpr unsigned M_RO = 0;
constexpr unsigned M_RS = 61440;
constexpr unsigned M_T1 = M_RS + 81920;
constexpr unsigned M_T2 = M_T1 + 10240;
constexpr unsigned M_CTL = M_T2 + 5120;
constexpr unsigned K2_LDS = M_CTL + 64;

__global__ __launch_bounds__(512, 2) void k2(const u16* x, const u16* Dsk, char* ws, float* out) {
  __shared__ __attribute__((aligned(1024))) char smem[K2_LDS];
  const unsigned lb = (unsigned)(uintptr_t)(AS3 char*)smem;
  const int tid = threadIdx.x, lane = tid & 63;
  const int w = __builtin_amdgcn_readfirstlane(tid >> 6);
  const int c = lane & 15, q = lane >> 4;
  const int b = blockIdx.x, xcd = b & 7, jb = b >> 3;
  const int rb = xcd * 4 + (jb >> 3), cg = jb & 7;
  const int rc = rb >> 1, par = rb & 1;
  const int vo = lane * 16;
  volatile AS3 unsigned* ctl = (volatile AS3 unsigned*)(uintptr_t)(lb + M_CTL);
  TSDECL;
  TS(0);
  if (tid < 2) ctl[tid] = 0u;
  BARRIER();
  f32x4 acc[4][5];

  f32x4 ag[15], al, tv[4];
  auto carry_load = [&](int pair) {
    const char* pa = ws + WS_AGG + pair * 16;
#pragma unroll
    for (int k = 0; k < 4; ++k) tv[k] = *(const f32x4*)(ws + WS_TAB + pair * 64 + k * 16);
#pragma unroll
    for (int m = 0; m < 15; ++m)
      if (m < rc) ag[m] = *(const f32x4*)(pa + m * 5120);
    if (par) al = *(const f32x4*)(ws + WS_AGL + rc * 5120 + pair * 16);
  };
  auto carry_fin = [&](int pair) {
    f32x2 g0 = {0.f, 0.f}, g1 = {0.f, 0.f};
    f32x2 P0 = {tv[1].z, tv[1].w}, P1 = {tv[3].z, tv[3].w};
#pragma unroll
    for (int m = 0; m < 15; ++m)
      if (m < rc) { g0 = cfmap(P0, g0, (f32x2){ag[m].x, ag[m].y}); g1 = cfmap(P1, g1, (f32x2){ag[m].z, ag[m].w}); }
    if (par) {
      g0 = cfmap((f32x2){tv[1].x, tv[1].y}, g0, (f32x2){al.x, al.y});
      g1 = cfmap((f32x2){tv[3].x, tv[3].y}, g1, (f32x2){al.z, al.w});
    }
    sts128f(lb + M_T1 + pair * 32, (f32x4){tv[0].x, tv[0].y, g0.x, g0.y});
    sts128f(lb + M_T1 + pair * 32 + 16, (f32x4){tv[2].x, tv[2].y, g1.x, g1.y});
    sts128f(lb + M_T2 + pair * 16, (f32x4){tv[0].z, tv[0].w, tv[2].z, tv[2].w});
  };

  if (w >= 4) {
    const int sw = w - 4;
    __builtin_amdgcn_s_setprio(2);
    __amdgpu_buffer_rsrc_t rA = mkrsrc(ws + WS_HF + (size_t)(rb * 40 + 10 * sw) * 4096, 10 * 4096);
    __amdgpu_buffer_rsrc_t rB = mkrsrc(ws + WS_CF + (size_t)(cg * 40 + 10 * sw) * 5120, 10 * 5120);
    u32x4 A[3][4], B[3][5];
    auto load = [&](int i) {
      int bf = i % 3;
#pragma unroll
      for (int n = 0; n < 5; ++n) B[bf][n] = __builtin_amdgcn_raw_buffer_load_b128(rB, vo + n * 1024, i * 5120, 0);
#pragma unroll
      for (int m = 0; m < 4; ++m) A[bf][m] = __builtin_amdgcn_raw_buffer_load_b128(rA, vo + m * 1024, i * 4096, 0);
    };
    carry_load(80 * sw + lane);
    SB();
    load(0);
    load(1);
    SB();
    carry_fin(80 * sw + lane);
    WAITL0();
    TS(1);
#pragma unroll
    for (int m = 0; m < 4; ++m)
#pragma unroll
      for (int n = 0; n < 5; ++n) acc[m][n] = (f32x4){0.f, 0.f, 0.f, 0.f};
    const float nf = (float)(4 * c + 1);
    u32x4 z[4];
    auto mkz = [&](int i) {
      unsigned t1 = lb + M_T1 + ((10 * sw + i) * 16 + 4 * q) * 16, t2 = lb + M_T2 + ((10 * sw + i) * 16 + 4 * q) * 8;
      f32x4 la = lds128f(t2), lc = lds128f(t2 + 16);
      f32x2 Ls[4] = {{la.x, la.y}, {la.z, la.w}, {lc.x, lc.y}, {lc.z, lc.w}};
#pragma unroll
      for (int d = 0; d < 4; ++d) {
        f32x4 t = lds128f(t1 + d * 16);
        f32x2 zz = cmul(cpow(t.x, t.y, nf), (f32x2){t.z, t.w});
#pragma unroll
        for (int m = 0; m < 4; ++m) {
          z[m][d] = pk2(zz.x, zz.y);
          if (m < 3) zz = cmul(Ls[d], zz);
        }
      }
    };
    mkz(0);
#pragma unroll
    for (int i = 0; i < 10; ++i) {
      if (i + 2 < 10) load(i + 2);
      SB();
#pragma unroll
      for (int m = 0; m < 4; ++m)
#pragma unroll
        for (int n = 0; n < 5; ++n) acc[m][n] = mfma(B[i % 3][n], z[m], acc[m][n]);
      SB();
      if (i == 7) {
        while (ctl[1] == 0u) __builtin_amdgcn_s_sleep(1);
        asm volatile("" ::: "memory");
      }
      if (i < 9) mkz(i + 1);
#pragma unroll
      for (int m = 0; m < 4; ++m)
#pragma unroll
        for (int n = 0; n < 5; ++n) acc[m][n] = mfma(B[i % 3][n], A[i % 3][m], acc[m][n]);
      if (i < 9) ileave<20, 5>();
      SB();
      TS(2 + i);
    }
    const unsigned tb = lb + M_RS + sw * 20480;
#pragma unroll
    for (int m = 0; m < 4; ++m)
#pragma unroll
      for (int n = 0; n < 5; ++n) sts128f(tb + (m * 5 + n) * 1024 + vo, acc[m][n]);
    TS(14);
    BARRIER();
    TS(15);
    TSDUMP(33554432 + 1048576);
    return;
  }

  __amdgpu_buffer_rsrc_t rA = mkrsrc(ws + WS_H1F + (size_t)rb * 48 * 4096, 48 * 4096);
  __amdgpu_buffer_rsrc_t rB = mkrsrc(ws + WS_W2F + (size_t)cg * 48 * 5120, 48 * 5120);
  u32x4 A[4][4], B[4][5];
  auto load = [&](int i) {
    int bf = i & 3, ks = w + 4 * i;
#pragma unroll
    for (int m = 0; m < 4; ++m) A[bf][m] = __builtin_amdgcn_raw_buffer_load_b128(rA, vo + m * 1024, ks * 4096, 0);
#pragma unroll
    for (int n = 0; n < 5; ++n) B[bf][n] = __builtin_amdgcn_raw_buffer_load_b128(rB, vo + n * 1024, ks * 5120, 0);
  };
  if (w == 0) {
    carry_load(80 * q + 64 + c);
    SB();
  }
  load(0);
  load(1);
  load(2);
  if (w == 0) {
    SB();
    carry_fin(80 * q + 64 + c);
    WAITL0();
    if (lane == 0) ctl[1] = 1u;
  }
  TS(1);
#pragma unroll
  for (int m = 0; m < 4; ++m)
#pragma unroll
    for (int n = 0; n < 5; ++n) acc[m][n] = (f32x4){0.f, 0.f, 0.f, 0.f};
  u32x2 xv[5], dk[5];
  f32x2 st;
  int eo = 0;
#pragma unroll
  for (int i = 0; i < 12; ++i) {
    if (i + 3 < 12) load(i + 3);
    if (i == 9) {
      int l2 = lane;
      asm volatile("" : "+v"(l2));
      const int t = 64 * rb + 4 * (l2 & 15) + w;
      eo = t * 640 + 80 * cg + 4 * (l2 >> 4);
      __amdgpu_buffer_rsrc_t rx = mkrsrc(x, 2621440), rd = mkrsrc(Dsk, 1280), rt = mkrsrc(ws + WS_ST, 16384);
#pragma unroll
      for (int n = 0; n < 5; ++n) {
        xv[n] = __builtin_amdgcn_raw_buffer_load_b64(rx, eo * 2 + n * 32, 0, 0);
        dk[n] = __builtin_amdgcn_raw_buffer_load_b64(rd, (l2 >> 4) * 8 + n * 32, 160 * cg, 0);
      }
      st = __builtin_bit_cast(f32x2, __builtin_amdgcn_raw_buffer_load_b64(rt, t * 8, 0, 0));
    }
    SB();
#pragma unroll
    for (int m = 0; m < 4; ++m)
#pragma unroll
      for (int n = 0; n < 5; ++n) acc[m][n] = mfma(B[i & 3][n], A[i & 3][m], acc[m][n]);
    SB();
    TS(2 + i);
  }
  f32x4 own[5];
#pragma unroll
  for (int m = 0; m < 4; ++m) {
    if (m == w) {
#pragma unroll
      for (int n = 0; n < 5; ++n) own[n] = acc[m][n];
    } else {
      unsigned a = lb + M_RO + (w * 3 + ((m - w - 1) & 3)) * 5120 + vo;
#pragma unroll
      for (int n = 0; n < 5; ++n) sts128f(a + n * 1024, acc[m][n]);
    }
  }
  WAITL0();
  if (lane == 0) __hip_atomic_fetch_add((AS3 unsigned*)(uintptr_t)(lb + M_CTL), 1u, __ATOMIC_RELAXED, __HIP_MEMORY_SCOPE_WORKGROUP);
  while (ctl[0] < 4u) __builtin_amdgcn_s_sleep(1);
  asm volatile("" ::: "memory");
#pragma unroll
  for (int s3 = 0; s3 < 3; ++s3) {
    int src = (w + 3 - s3) & 3;
#pragma unroll
    for (int n = 0; n < 5; ++n) own[n] += lds128f(lb + M_RO + ((src * 3 + s3) * 5 + n) * 1024 + vo);
  }
  f32x4 xs[5];
#pragma unroll
  for (int n = 0; n < 5; ++n) {
#pragma unroll
    for (int jj = 0; jj < 4; ++jj) {
      unsigned xw = xv[n][jj >> 1], dw = dk[n][jj >> 1];
      float xx = (jj & 1) ? bfhi(xw) : bflo(xw);
      float dd = (jj & 1) ? bfhi(dw) : bflo(dw);
      float gte = rbf(siluf(rbf(own[n][jj])));
      float xn = rbf(rbf(xx - st.x) * st.y);
      float sk = rbf(xn * dd);
      xs[n][jj] = xx + sk * gte;
      own[n][jj] = 2.f * gte;
    }
  }
  TS(14);
  BARRIER();
  TS(15);
  __amdgpu_buffer_rsrc_t ro = mkrsrc(out, 5242880);
#pragma unroll
  for (int n = 0; n < 5; ++n) {
    unsigned a = lb + M_RS + (w * 5 + n) * 1024 + vo;
    f32x4 s = (lds128f(a) + lds128f(a + 20480)) + (lds128f(a + 40960) + lds128f(a + 61440));
    __builtin_amdgcn_raw_buffer_store_b128(__builtin_bit_cast(u32x4, xs[n] + s * own[n]), ro, eo * 4 + n * 64, 0, 0);
  }
}

extern "C" void kernel_launch(void* const* d_in, const int* in_sizes, int n_in, void* d_out, int out_size, void* d_ws,
                              size_t ws_size, hipStream_t stream) {
  const u16* x = (const u16*)d_in[0];
  k1<<<256, 512, 0, stream>>>(x, (const u16*)d_in[1], (const u16*)d_in[2], (const u16*)d_in[3], (const u16*)d_in[4],
                              (const u16*)d_in[5], (const u16*)d_in[6], (const u16*)d_in[7], (const u16*)d_in[10],
                              (const u16*)d_in[11], (char*)d_ws);
  k2<<<256, 512, 0, stream>>>(x, (const u16*)d_in[8], (char*)d_ws, (float*)d_out);
}
```

```cpp
#include <hip/hip_runtime.h>
#include <stdint.h>

typedef unsigned short u16;
typedef __attribute__((ext_vector_type(8))) __bf16 bf16x8;
typedef __attribute__((ext_vector_type(2))) __bf16 bf16x2;
typedef __attribute__((ext_vector_type(4))) float f32x4;
typedef __attribute__((ext_vector_type(2))) float f32x2;
typedef __attribute__((ext_vector_type(4))) unsigned u32x4;
typedef __attribute__((ext_vector_type(2))) unsigned u32x2;
typedef __attribute__((ext_vector_type(4))) short s16x4;
#define AS3 __attribute__((address_space(3)))
#define DEVI __device__ __forceinline__

constexpr size_t WS_H1F = 0;
constexpr size_t WS_HF = 6291456;
constexpr size_t WS_W2F = 11534336;
constexpr size_t WS_CF = 13500416;
constexpr size_t WS_AGG = 15138816;
constexpr size_t WS_AGL = 15220736;
constexpr size_t WS_TAB = 15302656;
constexpr size_t WS_ST = 15323136;

DEVI float bf2f(unsigned v) { return __uint_as_float(v << 16); }
DEVI float bflo(unsigned v) { return __uint_as_float(v << 16); }
DEVI float bfhi(unsigned v) { return __uint_as_float(v & 0xffff0000u); }
DEVI unsigned pk2(float a, float b) {
  bf16x2 r = {(__bf16)a, (__bf16)b};
  return __builtin_bit_cast(unsigned, r);
}
DEVI unsigned pk2a(float a, float b) { unsigned r; asm("v_cvt_pk_bf16_f32 %0, %1, %2" : "=v"(r) : "v"(a), "v"(b)); return r; }
DEVI float rbf(float a) { return bflo(pk2(a, 0.f)); }
template <int N> DEVI float shr(float v) {
  return __builtin_bit_cast(float, __builtin_amdgcn_update_dpp(0, __builtin_bit_cast(int, v), 0x110 + N, 0xf, 0xf, true));
}
template <int N> DEVI f32x2 shr2(f32x2 v) { f32x2 r; r.x = shr<N>(v.x); r.y = shr<N>(v.y); return r; }
DEVI f32x2 cpow(float a, float b, float n) {
  float mg = __builtin_amdgcn_exp2f(n * a);
  float rv = __builtin_amdgcn_fractf(n * b);
  f32x2 r;
  r.x = mg * __builtin_amdgcn_cosf(rv);
  r.y = mg * __builtin_amdgcn_sinf(rv);
  return r;
}
DEVI f32x2 cmul(f32x2 a, f32x2 b) { f32x2 r; r.x = a.x * b.x - a.y * b.y; r.y = a.x * b.y + a.y * b.x; return r; }
DEVI f32x2 pfma(f32x2 a, f32x2 b, f32x2 c) { return __builtin_elementwise_fma(a, b, c); }
DEVI f32x2 cfmap(f32x2 a, f32x2 b, f32x2 c) {
  f32x2 ax = {a.x, a.x}, ay = {-a.y, a.y}, bs = {b.y, b.x};
  return pfma(ay, bs, pfma(ax, b, c));
}
DEVI f32x2 cmulp(f32x2 a, f32x2 b) {
  f32x2 ax = {a.x, a.x}, ay = {-a.y, a.y}, bs = {b.y, b.x};
  return pfma(ay, bs, ax * b);
}
extern "C" __device__ __attribute__((const)) float mul_legacy_f32(float, float) __asm("llvm.amdgcn.fmul.legacy");
DEVI float vmul(float a, float b) { return mul_legacy_f32(a, b); }
DEVI float vfma(float a, float b, float c) { float r; asm("v_fma_f32 %0, %1, %2, %3" : "=v"(r) : "v"(a), "v"(b), "v"(c)); return r; }
DEVI float vadd(float a, float b) { float r; asm("v_add_f32 %0, %1, %2" : "=v"(r) : "v"(a), "v"(b)); return r; }
DEVI float vsub(float a, float b) { float r; asm("v_sub_f32 %0, %1, %2" : "=v"(r) : "v"(a), "v"(b)); return r; }
DEVI float vfms(float a, float b, float c) { float r; asm("v_fma_f32 %0, %1, %2, -%3" : "=v"(r) : "v"(a), "v"(b), "v"(c)); return r; }
DEVI float siluf(float a) { return a * __builtin_amdgcn_rcpf(1.f + __builtin_amdgcn_exp2f(-1.44269504088896f * a)); }
DEVI void swap16(unsigned& a, unsigned& b) { auto r = __builtin_amdgcn_permlane16_swap(a, b, false, false); a = r[0]; b = r[1]; }
DEVI void swap32(unsigned& a, unsigned& b) { auto r = __builtin_amdgcn_permlane32_swap(a, b, false, false); a = r[0]; b = r[1]; }
DEVI void swap16f(float& a, float& b) { unsigned x = __float_as_uint(a), y = __float_as_uint(b); swap16(x, y); a = __uint_as_float(x); b = __uint_as_float(y); }
DEVI void swap32f(float& a, float& b) { unsigned x = __float_as_uint(a), y = __float_as_uint(b); swap32(x, y); a = __uint_as_float(x); b = __uint_as_float(y); }

#define WAITV(n) asm volatile("s_waitcnt vmcnt(%0)" ::"n"(n) : "memory")
#define WAITL0() asm volatile("s_waitcnt lgkmcnt(0)" ::: "memory")
#define BARRIER() do { WAITL0(); __builtin_amdgcn_s_barrier(); asm volatile("" ::: "memory"); } while (0)
#define BARRIER_NW() do { asm volatile("" ::: "memory"); __builtin_amdgcn_s_barrier(); asm volatile("" ::: "memory"); } while (0)
#define SB() __builtin_amdgcn_sched_barrier(0)
DEVI void glds(const void* p, unsigned ldsaddr) {
  unsigned keep;
  asm volatile("s_mov_b32 %0, m0\n\ts_mov_b32 m0, %2\n\ts_nop 0\n\tglobal_load_lds_dwordx4 %1, off\n\ts_mov_b32 m0, %0"
               : "=&s"(keep) : "v"(p), "s"(ldsaddr) : "memory");
}

DEVI u32x4 lds128(unsigned off) { return *(const AS3 u32x4*)(uintptr_t)off; }
DEVI f32x4 lds128f(unsigned off) { return *(const AS3 f32x4*)(uintptr_t)off; }
DEVI f32x2 lds64f(unsigned off) { return *(const AS3 f32x2*)(uintptr_t)off; }
DEVI void sts128f(unsigned off, f32x4 v) { *(AS3 f32x4*)(uintptr_t)off = v; }
DEVI void sts64f(unsigned off, f32x2 v) { *(AS3 f32x2*)(uintptr_t)off = v; }
DEVI void sts32f(unsigned off, float v) { *(AS3 float*)(uintptr_t)off = v; }
DEVI u32x2 ldstr(unsigned off) {
  s16x4 r = __builtin_amdgcn_ds_read_tr16_b64_v4i16((AS3 s16x4*)(uintptr_t)off);
  return __builtin_bit_cast(u32x2, r);
}
DEVI u32x4 ldstr2(unsigned a, unsigned d) { u32x2 lo = ldstr(a), hi = ldstr(a + d); return (u32x4){lo.x, lo.y, hi.x, hi.y}; }
DEVI f32x4 mfma(u32x4 a, u32x4 b, f32x4 c) {
  return __builtin_amdgcn_mfma_f32_16x16x32_bf16(__builtin_bit_cast(bf16x8, a), __builtin_bit_cast(bf16x8, b), c, 0, 0, 0);
}
#define STAUX 16
#define SPRIO 2
#define OPRIO 0
#define OLP OPRIO
#define SLP SPRIO
#define OLI 0
#define OTP OPRIO
#define OTI 9
DEVI void st128(u32x4 v, __amdgpu_buffer_rsrc_t rs, int off) { __builtin_amdgcn_raw_buffer_store_b128(v, rs, off, 0, STAUX); }
DEVI __amdgpu_buffer_rsrc_t mkrsrc(const void* p, unsigned bytes) {
  return __builtin_amdgcn_make_buffer_rsrc((void*)p, 0, bytes, 0x00020000);
}
#define TSDECL
#define TS(k)
#define TSDUMP(base)

constexpr unsigned SLOT = 38912, OB = 16384, OW = 26624;
constexpr unsigned SPARE = 4 * SLOT;
constexpr unsigned T_TAB = SPARE;
constexpr unsigned T_AG = SPARE + 2560;
constexpr unsigned T_FLAG = SPARE + 3072;
constexpr unsigned K1_LDS = SPARE + 4096;
constexpr unsigned S_W2 = 2 * SLOT;
constexpr unsigned E_ST = 3 * SLOT;
constexpr unsigned E_CS = E_ST + 2048;
constexpr unsigned E_A5 = E_CS + 2048;

DEVI void stat(u32x4 f, float& s, float& q) {
#pragma unroll
  for (int r = 0; r < 4; ++r) {
    float lo = bflo(f[r]), hi = bfhi(f[r]);
    s += lo; q = fmaf(lo, lo, q);
    s += hi; q = fmaf(hi, hi, q);
  }
}
template <int NM, int NV> DEVI void ileave() {
#pragma unroll
  for (int i = 0; i < NM; ++i) {
    __builtin_amdgcn_sched_group_barrier(0x008, 1, 0);
    __builtin_amdgcn_sched_group_barrier(0x002, NV, 0);
  }
}
struct Tab { f32x2 L, L4, L8, L16, L32, L64, co; };
DEVI void rdtab(Tab& t, unsigned a) {
  f32x4 v0 = lds128f(a), v1 = lds128f(a + 16), v2 = lds128f(a + 32);
  f32x2 v3 = lds64f(a + 48);
  t.L = (f32x2){v0.x, v0.y}; t.L4 = (f32x2){v0.z, v0.w}; t.L8 = (f32x2){v1.x, v1.y}; t.L16 = (f32x2){v1.z, v1.w};
  t.L32 = (f32x2){v2.x, v2.y}; t.L64 = (f32x2){v2.z, v2.w}; t.co = v3;
}

__global__ __launch_bounds__(512, 2) void k1(const u16* x, const u16* Lre, const u16* Lim, const u16* Bre, const u16* Bim,
                                             const u16* Cre, const u16* Cim, const u16* ldt, const u16* w1, const u16* w2,
                                             char* ws) {
  __shared__ __attribute__((aligned(1024))) char smem[K1_LDS];
  const unsigned lb = (unsigned)(uintptr_t)(AS3 char*)smem;
  const int b = blockIdx.x, xcd = b & 7, jb = b >> 3;
  const int rc = (xcd >> 1) * 4 + (jb & 3), g = (xcd & 1) * 8 + (jb >> 2);
  const int tid = threadIdx.x, lane = tid & 63;
  const int w = __builtin_amdgcn_readfirstlane(tid >> 6);
  const int c = lane & 15, q = lane >> 4;
  const int l3 = lane >> 3, l7 = lane & 7;
  const int h = (w >> 1) & 1, a = w & 1;
  const int kk0 = h ^ a;
  TSDECL;
  TS(0);

  unsigned pr[3];
  if (w == 7) {
    int p = 40 * g + (lane < 40 ? lane : 0);
    const u16* a0 = Lre + p; const u16* a1 = Lim + p; const u16* a2 = ldt + p;
    asm volatile("global_load_ushort %0, %1, off" : "=v"(pr[0]) : "v"(a0) : "memory");
    asm volatile("global_load_ushort %0, %1, off" : "=v"(pr[1]) : "v"(a1) : "memory");
    asm volatile("global_load_ushort %0, %1, off" : "=v"(pr[2]) : "v"(a2) : "memory");
  }
  const char* src[5];
  unsigned dsto[5];
  int step3;
#pragma unroll
  for (int v = 0; v < 2; ++v) {
    int rho = 8 * (w + 8 * v) + l3;
    int t = 128 * rc + 64 * v + 4 * (rho & 15) + ((rho >> 4) & 3);
    src[v] = (const char*)x + t * 1280 + ((l7 ^ l3) << 4);
    dsto[v] = (w + 8 * v) * 1024;
  }
  {
    int n = 8 * w + l3;
    src[2] = (const char*)((n & 1) ? Bim : Bre) + (40 * g + (n >> 1)) * 1280 + ((l7 ^ l3) << 4);
    dsto[2] = OB + w * 1024;
    auto wsrc = [&](int e) {
      int idx = 64 * e + lane, k = idx / 12, uu = idx - 12 * k;
      return (const char*)w1 + k * 3072 + 192 * g + (((uu >> 1) ^ ((k >> 3) & 1)) << 5) + ((uu & 1) << 4);
    };
    if (w < 2) {
      n = 64 + 8 * w + l3;
      src[3] = (const char*)((n & 1) ? Bim : Bre) + (40 * g + (n >> 1)) * 1280 + ((l7 ^ l3) << 4);
      dsto[3] = OB + (8 + w) * 1024; step3 = 128;
    } else {
      src[3] = wsrc(w - 2); dsto[3] = OW + (w - 2) * 1024; step3 = 196608;
    }
    src[4] = wsrc(w < 6 ? 6 + w : 0); dsto[4] = OW + (6 + w) * 1024;
  }
  auto issue = [&](unsigned slot) {
    glds(src[0], lb + slot + dsto[0]); glds(src[1], lb + slot + dsto[1]); glds(src[2], lb + slot + dsto[2]);
    glds(src[3], lb + slot + dsto[3]);
    if (w < 6) glds(src[4], lb + slot + dsto[4]);
    src[0] += 128; src[1] += 128; src[2] += 128; src[3] += step3; src[4] += 196608;
  };
#define WAIT1() do { if (w < 6) WAITV(5); else WAITV(4); } while (0)
  issue(0); issue(SLOT); issue(2 * SLOT);
  const u32x4 ONES = {0x3f803f80u, 0x3f803f80u, 0x3f803f80u, 0x3f803f80u};
  unsigned xo[2];
#pragma unroll
  for (int p = 0; p < 2; ++p) xo[p] = lb + 8192 * h + c * 128 + (((4 * (kk0 ^ p) + q) ^ (c & 7)) << 4);
  float sv[4] = {0.f, 0.f, 0.f, 0.f};
  const int rb = 2 * rc + h;

  auto mr4 = [&](float* m, float* r) {
#pragma unroll
    for (int i = 0; i < 4; ++i) {
      f32x4 v = lds128f(lb + E_ST + (64 * h + 4 * c + i) * 16);
      float mean = (v.x + v.z) * (1.f / 640.f);
      float var = fmaxf((v.y + v.w) * (1.f / 640.f) - mean * mean, 0.f);
      float den = rbf(__builtin_amdgcn_sqrtf(rbf(rbf(var) + 1e-5f)));
      m[i] = rbf(mean); r[i] = __builtin_amdgcn_rcpf(den);
    }
  };
  auto put_stats = [&](int i0) {
    swap32f(sv[0], sv[2]); swap32f(sv[1], sv[3]);
    float w0 = sv[0] + sv[2], w1v = sv[1] + sv[3];
    swap16f(w0, w1v);
    sts32f(lb + E_ST + ((64 * h + 4 * c + i0 + (q >> 1)) * 4 + 2 * a + (q & 1)) * 4, w0 + w1v);
  };

  if (w < 4) {
    unsigned wo[3];
#pragma unroll
    for (int u = 0; u < 3; ++u) {
      int tl = a ? (u == 2 ? 3 : 4 + u) : u;
      wo[u] = lb + OW + (8 * q + (c >> 2)) * 192 + (c & 3) * 8 + ((tl ^ (q & 1)) << 5);
    }
    const unsigned kq0 = kk0 * 6144, kq1 = 6144 - kq0;
    f32x4 acc[4][3], cs[3];
#pragma unroll
    for (int u = 0; u < 3; ++u) {
      cs[u] = (f32x4){0.f, 0.f, 0.f, 0.f};
#pragma unroll
      for (int i = 0; i < 4; ++i) acc[i][u] = cs[u];
    }
    u32x4 X0[4], W0[3], X1[4], W1[3];
    auto rd0 = [&](unsigned slot) {
#pragma unroll
      for (int u = 0; u < 3; ++u) W0[u] = ldstr2(wo[u] + slot + kq0, 768);
#pragma unroll
      for (int i = 0; i < 4; ++i) X0[i] = lds128(xo[0] + slot + 2048 * i);
    };
    auto rd1 = [&](unsigned slot) {
#pragma unroll
      for (int u = 0; u < 3; ++u) W1[u] = ldstr2(wo[u] + slot + kq1, 768);
#pragma unroll
      for (int i = 0; i < 4; ++i) X1[i] = lds128(xo[1] + slot + 2048 * i);
    };
    auto mm0 = [&]() {
#pragma unroll
      for (int i = 0; i < 4; ++i)
#pragma unroll
        for (int u = 0; u < 3; ++u) acc[i][u] = mfma(W0[u], X0[i], acc[i][u]);
      stat(X0[0], sv[0], sv[1]);
      ileave<12, 2>();
    };
    auto mm1 = [&]() {
#pragma unroll
      for (int i = 0; i < 4; ++i)
#pragma unroll
        for (int u = 0; u < 3; ++u) acc[i][u] = mfma(W1[u], X1[i], acc[i][u]);
#pragma unroll
      for (int u = 0; u < 3; ++u) cs[u] = mfma(W1[u], ONES, cs[u]);
      stat(X1[1], sv[2], sv[3]);
      ileave<12, 2>();
    };
    int unit = 2 * jb + (w & 1);
    if (w < 2) { if (unit >= 60) unit -= 4; } else { if (unit >= 50) unit -= 14; }
    const int u5 = unit / 5;
    const int uks = (w < 2 ? 12 : 10) * (xcd >> 1) + u5, udb = 5 * (xcd & 1) + (unit - 5 * u5);
    u32x4 sj[4];

    WAITV(10);
    BARRIER_NW();
    TS(1);
    rd0(0);
    unsigned slot = 0, nslot = SLOT, fslot = 3 * SLOT;
    for (int s = 0; s < 7; ++s) {
      rd1(slot); SB();
      mm0(); SB();
      WAIT1();
      BARRIER_NW();
      TS(2 + s);
      issue(fslot);
      rd0(nslot); SB();
      mm1(); SB();
      fslot = slot; slot = nslot;
      nslot += SLOT; if (nslot == 4 * SLOT) nslot = 0;
    }
    rd1(3 * SLOT); SB();
    mm0(); SB();
    WAIT1();
    BARRIER_NW();
    TS(9);
    if (w < 2) {
#pragma unroll
      for (int v = 0; v < 4; ++v) {
        int r = 8 * v + l3;
        int f = (r >> 1) & 3;
        glds((const char*)w2 + (32 * uks + r) * 1280 + udb * 128 + ((l7 ^ (f << 1)) << 4), lb + S_W2 + w * 4096 + v * 1024);
      }
    } else {
      int G = uks / 5, kr = uks - 5 * G;
      int p0 = 80 * G + (kr < 4 ? 40 * (kr >> 1) + 16 * (kr & 1) : 32);
      int d1 = kr < 4 ? 8 : 40;
      const u16* c0 = Cre + (64 * udb + lane) * 640 + p0;
      const u16* c1 = Cim + (64 * udb + lane) * 640 + p0;
      const u16* c2 = c0 + d1; const u16* c3 = c1 + d1;
      asm volatile("global_load_dwordx4 %0, %1, off" : "=v"(sj[0]) : "v"(c0) : "memory");
      asm volatile("global_load_dwordx4 %0, %1, off" : "=v"(sj[1]) : "v"(c2) : "memory");
      asm volatile("global_load_dwordx4 %0, %1, off" : "=v"(sj[2]) : "v"(c1) : "memory");
      asm volatile("global_load_dwordx4 %0, %1, off" : "=v"(sj[3]) : "v"(c3) : "memory");
    }
    rd0(0); SB();
    mm1(); SB();
    rd1(0); SB();
    mm0(); SB();
    WAITV(4);
    BARRIER_NW();
    TS(10);
    rd0(SLOT); SB();
    mm1(); SB();
    rd1(SLOT); SB();
    mm0(); SB();
    WAITV(0);
    if (w < 2) {
      const bool nat = (uks % 3) == 1;
      int r0 = (nat ? 8 * q : 4 * q) + (c >> 2);
      int dr = nat ? 4 : 16;
      unsigned a0 = lb + S_W2 + w * 4096 + r0 * 128 + (c & 3) * 8;
      int f0 = (r0 >> 1) & 3, f1 = ((r0 + dr) >> 1) & 3;
#pragma unroll
      for (int j = 0; j < 4; ++j) {
        u32x2 lo = ldstr(a0 + ((j ^ f0) << 5));
        u32x2 hi = ldstr(a0 + dr * 128 + ((j ^ f1) << 5));
        int Dt = 4 * udb + j, cg = Dt / 5, nt = Dt - 5 * cg;
        *(u32x4*)(ws + WS_W2F + (size_t)(((cg * 48 + uks) * 5 + nt) * 1024) + lane * 16) = (u32x4){lo.x, lo.y, hi.x, hi.y};
      }
    } else {
      asm volatile("" : "+v"(sj[0]), "+v"(sj[1]), "+v"(sj[2]), "+v"(sj[3]));
      unsigned P[2][4][2];
#pragma unroll
      for (int ga = 0; ga < 2; ++ga)
#pragma unroll
        for (int j = 0; j < 4; ++j) {
          unsigned im = sj[2 + ga][j] ^ 0x80008000u;
          P[ga][j][0] = __builtin_amdgcn_perm(im, sj[ga][j], 0x05040100u);
          P[ga][j][1] = __builtin_amdgcn_perm(im, sj[ga][j], 0x07060302u);
        }
      int Dt = 4 * udb + q, cg = Dt / 5, nt = Dt - 5 * cg;
      char* dst = ws + WS_CF + (size_t)(((cg * 40 + uks) * 5 + nt) * 1024) + c * 16;
      if (uks % 5 < 4) {
#pragma unroll
        for (int j = 0; j < 4; ++j) *(u32x4*)(dst + j * 256) = (u32x4){P[0][j][0], P[0][j][1], P[1][j][0], P[1][j][1]};
      } else {
#pragma unroll
        for (int j = 0; j < 4; ++j)
          *(u32x4*)(dst + j * 256) = (u32x4){P[j >> 1][0][j & 1], P[j >> 1][1][j & 1], P[j >> 1][2][j & 1], P[j >> 1][3][j & 1]};
      }
    }
    SB();
    mm1(); SB();
    TS(11);
    put_stats(0);
    if (c == 0) {
#pragma unroll
      for (int u = 0; u < 3; ++u) sts128f(lb + E_CS + ((w * 3 + u) * 4 + q) * 16, cs[u]);
    }
    BARRIER();
    TS(12);
    float m[4], r[4];
    mr4(m, r);
#pragma unroll
    for (int u = 0; u < 3; ++u) cs[u] += lds128f(lb + E_CS + (((w ^ 2) * 3 + u) * 4 + q) * 16);
    if (g == 0 && a == 0 && q == 0) {
      f32x4* st = (f32x4*)(ws + WS_ST + (64 * rb + 4 * c) * 8);
      st[0] = (f32x4){m[0], r[0], m[1], r[1]};
      st[1] = (f32x4){m[2], r[2], m[3], r[3]};
    }
    __amdgpu_buffer_rsrc_t rs = mkrsrc(ws + WS_H1F, 6291456);
    unsigned o[4][6];
#pragma unroll
    for (int i = 0; i < 4; ++i) {
      f32x2 nm = {-m[i], -m[i]}, rr = {r[i], r[i]}, re = {-1.44269504088896f * r[i], -1.44269504088896f * r[i]};
#pragma unroll
      for (int u = 0; u < 3; ++u)
#pragma unroll
        for (int hf = 0; hf < 2; ++hf) {
          f32x2 v = pfma((f32x2){cs[u][2 * hf], cs[u][2 * hf + 1]}, nm, (f32x2){acc[i][u][2 * hf], acc[i][u][2 * hf + 1]});
          f32x2 e = v * re;
          e.x = __builtin_amdgcn_exp2f(e.x); e.y = __builtin_amdgcn_exp2f(e.y);
          e = e + (f32x2){1.f, 1.f};
          e.x = __builtin_amdgcn_rcpf(e.x); e.y = __builtin_amdgcn_rcpf(e.y);
          v = (v * rr) * e;
          o[i][2 * u + hf] = pk2(v.x, v.y);
        }
      st128((u32x4){o[i][0], o[i][1], o[i][2], o[i][3]}, rs, lane * 16 + ((rb * 48 + 3 * g + 2 * a) * 4 + i) * 1024);
      if (i & 1) {
        swap16(o[i - 1][4], o[i][4]); swap16(o[i - 1][5], o[i][5]);
        st128((u32x4){o[i - 1][4], o[i - 1][5], o[i][4], o[i][5]}, rs,
              (c + 16 * (2 * a + (q >> 1))) * 16 + ((rb * 48 + 3 * g + 1) * 4 + (i - 1) + (q & 1)) * 1024);
      }
    }
    TS(13);
  } else {
    if (w == 7) {
      WAITV(12);
      asm volatile("" : "+v"(pr[0]), "+v"(pr[1]), "+v"(pr[2]));
      float lr = bf2f(pr[0]), li = bf2f(pr[1]), ld = bf2f(pr[2]);
      float dt = rbf(expf(ld));
      float ea = dt * lr * 1.44269504088896f, eb = dt * li * 0.15915494309189535f;
      f32x2 L = cpow(ea, eb, 1.f), L4 = cpow(ea, eb, 4.f), L8 = cpow(ea, eb, 8.f), L16 = cpow(ea, eb, 16.f);
      f32x2 L32 = cpow(ea, eb, 32.f), L64 = cpow(ea, eb, 64.f);
      float nr = L.x - 1.f, ni = L.y, dd = 2.f / (lr * lr + li * li);
      f32x2 co = {(nr * lr + ni * li) * dd, (ni * lr - nr * li) * dd};
      if (lane < 40) {
        unsigned ta = lb + T_TAB + lane * 64;
        sts128f(ta, (f32x4){L.x, L.y, L4.x, L4.y});
        sts128f(ta + 16, (f32x4){L8.x, L8.y, L16.x, L16.y});
        sts128f(ta + 32, (f32x4){L32.x, L32.y, L64.x, L64.y});
        sts64f(ta + 48, co);
        if (rc == 0) {
          int sg = lane < 32 ? (5 * (g >> 1) + 2 * (g & 1) + (lane >> 4)) * 16 + 4 * ((lane >> 1) & 3) + 2 * ((lane >> 3) & 1) + (lane & 1)
                             : (5 * (g >> 1) + 4) * 16 + 8 * (g & 1) + 4 * (lane & 1) + ((lane - 32) >> 1);
          *(f32x2*)(ws + WS_TAB + sg * 8) = (f32x2){ea, eb};
        }
      }
      if (lane < 2) sts32f(lb + T_FLAG + lane * 4, 0.f);
    }
    unsigned bo[2];
#pragma unroll
    for (int p = 0; p < 2; ++p) bo[p] = xo[p] - 8192 * h + OB + 4096 * a;
    const int r5 = c ^ (2 * a);
    const unsigned f5 = lb + OB + 8192 + r5 * 128 + (((4 * kk0 + q) ^ (r5 & 7)) << 4);
    f32x4 acc[4][2], ac5[5], cs[2];
#pragma unroll
    for (int i = 0; i < 4; ++i) { acc[i][0] = (f32x4){0.f, 0.f, 0.f, 0.f}; acc[i][1] = acc[i][0]; ac5[i] = acc[i][0]; }
    ac5[4] = ac5[0]; cs[0] = ac5[0]; cs[1] = ac5[0];
    u32x4 X0[4], W0[2], W5, X1[4], W1[2];
    auto rd0 = [&](unsigned slot) {
      W0[0] = lds128(bo[0] + slot); W0[1] = lds128(bo[0] + slot + 2048);
#pragma unroll
      for (int i = 0; i < 4; ++i) X0[i] = lds128(xo[0] + slot + 2048 * i);
      W5 = lds128(f5 + slot);
    };
    auto rd1 = [&](unsigned slot) {
      W1[0] = lds128(bo[1] + slot); W1[1] = lds128(bo[1] + slot + 2048);
#pragma unroll
      for (int i = 0; i < 4; ++i) X1[i] = lds128(xo[1] + slot + 2048 * i);
    };
    auto mm0 = [&]() {
#pragma unroll
      for (int i = 0; i < 4; ++i) {
        acc[i][0] = mfma(W0[0], X0[i], acc[i][0]);
        acc[i][1] = mfma(W0[1], X0[i], acc[i][1]);
      }
#pragma unroll
      for (int i = 0; i < 4; ++i) ac5[i] = mfma(W5, X0[i], ac5[i]);
      ac5[4] = mfma(W5, ONES, ac5[4]);
      stat(X0[2], sv[0], sv[1]);
      ileave<12, 2>();
    };
    auto mm1 = [&]() {
#pragma unroll
      for (int i = 0; i < 4; ++i) {
        acc[i][0] = mfma(W1[0], X1[i], acc[i][0]);
        acc[i][1] = mfma(W1[1], X1[i], acc[i][1]);
      }
      cs[0] = mfma(W1[0], ONES, cs[0]);
      cs[1] = mfma(W1[1], ONES, cs[1]);
      stat(X1[3], sv[2], sv[3]);
      ileave<8, 3>();
    };
    if (w < 6) WAITV(10); else WAITV(8);
    BARRIER();
    TS(1);
    rd0(0);
    unsigned slot = 0, nslot = SLOT, fslot = 3 * SLOT;
    for (int s = 0; s < 7; ++s) {
      rd1(slot); SB();
      mm0(); SB();
      WAIT1();
      BARRIER_NW();
      TS(2 + s);
      issue(fslot);
      rd0(nslot); SB();
      mm1(); SB();
      fslot = slot; slot = nslot;
      nslot += SLOT; if (nslot == 4 * SLOT) nslot = 0;
    }
    rd1(3 * SLOT); SB();
    mm0(); SB();
    WAIT1();
    BARRIER_NW();
    TS(9);
    rd0(0); SB();
    mm1(); SB();
    rd1(0); SB();
    mm0(); SB();
    WAITV(0);
    BARRIER_NW();
    TS(10);
    rd0(SLOT); SB();
    mm1(); SB();
    rd1(SLOT); SB();
    mm0(); SB();
    Tab tb[5];
#pragma unroll
    for (int k = 0; k < 4; ++k) rdtab(tb[k], lb + T_TAB + (16 * a + 8 * (k >> 1) + 2 * q + (k & 1)) * 64);
    rdtab(tb[4], lb + T_TAB + (32 + 2 * q + a) * 64);
    mm1(); SB();
    TS(11);
    put_stats(2);
    if (c == 0) {
      sts128f(lb + E_CS + ((w * 3 + 0) * 4 + q) * 16, cs[0]);
      sts128f(lb + E_CS + ((w * 3 + 1) * 4 + q) * 16, cs[1]);
    }
#pragma unroll
    for (int i = 0; i < 5; ++i) sts64f(lb + E_A5 + (((w - 4) * 5 + i) * 64 + lane) * 8, (f32x2){ac5[i].z, ac5[i].w});
    BARRIER();
    TS(12);
    float m[4], r[4];
    mr4(m, r);
    cs[0] += lds128f(lb + E_CS + (((w ^ 2) * 3 + 0) * 4 + q) * 16);
    cs[1] += lds128f(lb + E_CS + (((w ^ 2) * 3 + 1) * 4 + q) * 16);
    f32x2 u[5][4], csv[5];
#pragma unroll
    for (int i = 0; i < 4; ++i) {
      u[0][i] = (f32x2){acc[i][0].x, acc[i][0].y}; u[1][i] = (f32x2){acc[i][0].z, acc[i][0].w};
      u[2][i] = (f32x2){acc[i][1].x, acc[i][1].y}; u[3][i] = (f32x2){acc[i][1].z, acc[i][1].w};
      u[4][i] = (f32x2){ac5[i].x, ac5[i].y} + lds64f(lb + E_A5 + ((((w - 4) ^ 1) * 5 + i) * 64 + lane) * 8);
    }
    csv[0] = (f32x2){cs[0].x, cs[0].y}; csv[1] = (f32x2){cs[0].z, cs[0].w};
    csv[2] = (f32x2){cs[1].x, cs[1].y}; csv[3] = (f32x2){cs[1].z, cs[1].w};
    csv[4] = (f32x2){ac5[4].x, ac5[4].y} + lds64f(lb + E_A5 + ((((w - 4) ^ 1) * 5 + 4) * 64 + lane) * 8);
#pragma unroll
    for (int i = 0; i < 4; ++i) {
      f32x2 nm = {-m[i], -m[i]}, rr = {r[i], r[i]};
#pragma unroll
      for (int k = 0; k < 5; ++k) u[k][i] = cmulp(tb[k].co, pfma(csv[k], nm, u[k][i]) * rr);
    }
    f32x2 hh[5];
#pragma unroll
    for (int k = 0; k < 5; ++k) hh[k] = u[k][0];
#pragma unroll
    for (int i = 1; i < 4; ++i)
#pragma unroll
      for (int k = 0; k < 5; ++k) hh[k] = cfmap(tb[k].L, hh[k], u[k][i]);
#pragma unroll
    for (int k = 0; k < 5; ++k) hh[k] = cfmap(tb[k].L4, shr2<1>(hh[k]), hh[k]);
#pragma unroll
    for (int k = 0; k < 5; ++k) hh[k] = cfmap(tb[k].L8, shr2<2>(hh[k]), hh[k]);
#pragma unroll
    for (int k = 0; k < 5; ++k) hh[k] = cfmap(tb[k].L16, shr2<4>(hh[k]), hh[k]);
#pragma unroll
    for (int k = 0; k < 5; ++k) hh[k] = cfmap(tb[k].L32, shr2<8>(hh[k]), hh[k]);
    const int ksr = 5 * (g >> 1) + 2 * (g & 1) + a, ks5 = 5 * (g >> 1) + 4;
    f32x2 ag[5];
#pragma unroll
    for (int k = 0; k < 5; ++k) ag[k] = hh[k];
    if (h == 0) {
      if (c == 15) {
        unsigned ta = lb + T_AG + (a * 4 + q) * 48;
        sts128f(ta, (f32x4){ag[0].x, ag[0].y, ag[1].x, ag[1].y});
        sts128f(ta + 16, (f32x4){ag[2].x, ag[2].y, ag[3].x, ag[3].y});
        sts64f(ta + 32, ag[4]);
      }
      WAITL0();
      if (lane == 0) *(volatile AS3 unsigned*)(uintptr_t)(lb + T_FLAG + a * 4) = 1u;
    }
#pragma unroll
    for (int k = 0; k < 5; ++k) hh[k] = shr2<1>(hh[k]);
    __amdgpu_buffer_rsrc_t rs = mkrsrc(ws + WS_HF, 5242880);
    unsigned o5[4];
#pragma unroll
    for (int i = 0; i < 4; ++i) {
      u32x4 o;
#pragma unroll
      for (int k = 0; k < 5; ++k) {
        hh[k] = cfmap(tb[k].L, hh[k], u[k][i]);
        if (k < 4) o[k] = pk2(hh[k].x, hh[k].y); else o5[i] = pk2(hh[k].x, hh[k].y);
      }
      st128(o, rs, lane * 16 + ((rb * 40 + ksr) * 4 + i) * 1024);
    }
    swap32(o5[0], o5[2]); swap32(o5[1], o5[3]);
    swap16(o5[0], o5[1]); swap16(o5[2], o5[3]);
    st128((u32x4){o5[0], o5[1], o5[2], o5[3]}, rs, (c + 16 * (2 * (g & 1) + a)) * 16 + ((rb * 40 + ks5) * 4 + q) * 1024);
    if (h == 1) {
      while (*(volatile AS3 unsigned*)(uintptr_t)(lb + T_FLAG + a * 4) == 0u) __builtin_amdgcn_s_sleep(1);
      asm volatile("" ::: "memory");
      unsigned ta = lb + T_AG + (a * 4 + q) * 48;
      f32x4 l0 = lds128f(ta), l1 = lds128f(ta + 16);
      f32x2 l2 = lds64f(ta + 32);
      ag[0] = cfmap(tb[0].L64, (f32x2){l0.x, l0.y}, ag[0]); ag[1] = cfmap(tb[1].L64, (f32x2){l0.z, l0.w}, ag[1]);
      ag[2] = cfmap(tb[2].L64, (f32x2){l1.x, l1.y}, ag[2]); ag[3] = cfmap(tb[3].L64, (f32x2){l1.z, l1.w}, ag[3]);
      ag[4] = cfmap(tb[4].L64, l2, ag[4]);
    }
    if (c == 15) {
      char* base = ws + (h ? WS_AGG : WS_AGL) + rc * 2560;
      *(u32x4*)(base + (ksr * 16 + 4 * q) * 4) = (u32x4){pk2(ag[0].x, ag[0].y), pk2(ag[1].x, ag[1].y), pk2(ag[2].x, ag[2].y), pk2(ag[3].x, ag[3].y)};
      *(unsigned*)(base + (ks5 * 16 + 8 * (g & 1) + 4 * a + q) * 4) = pk2(ag[4].x, ag[4].y);
    }
    TS(13);
  }
}

constexpr unsigned M_RO = 0;
constexpr unsigned M_RS = 61440;
constexpr unsigned M_T1 = M_RS + 81920;
constexpr unsigned M_CTL = M_T1 + 15360;
constexpr unsigned K2_LDS = M_CTL + 64;

__global__ __launch_bounds__(512, 2) void k2(const u16* x, const u16* Dsk, char* ws, float* out) {
  __shared__ __attribute__((aligned(1024))) char smem[K2_LDS];
  const unsigned lb = (unsigned)(uintptr_t)(AS3 char*)smem;
  const int tid = threadIdx.x, lane = tid & 63;
  const int w = __builtin_amdgcn_readfirstlane(tid >> 6);
  const int c = lane & 15, q = lane >> 4;
  const int b = blockIdx.x, xcd = b & 7, jb = b >> 3;
  const int rb = (xcd >> 1) * 8 + (jb & 7), cg = (xcd & 1) * 4 + (jb >> 3), gh = xcd & 1;
  const int rc = rb >> 1, par = rb & 1;
  const int vo = lane * 16;
  TSDECL;
  TS(0);
  f32x4 acc[4][5];
  if (tid < 4) sts32f(lb + M_CTL + tid * 4, 0.f);
  BARRIER();

  if (w >= 4) {
    const int sw = w - 4;
    __builtin_amdgcn_s_setprio(SPRIO);
    __amdgpu_buffer_rsrc_t rA = mkrsrc(ws + WS_HF + (size_t)(rb * 40) * 4096, 40 * 4096);
    __amdgpu_buffer_rsrc_t rB = mkrsrc(ws + WS_CF + (size_t)(cg * 40) * 5120, 40 * 5120);
    auto kof = [&](int i) { return 5 * sw + (i >> 1) + 20 * ((i & 1) ^ gh); };
    u32x4 A[3][4], B[3][5];
    auto load = [&](int i) {
      int bf = i % 3;
#pragma unroll
      for (int n = 0; n < 5; ++n) B[bf][n] = __builtin_amdgcn_raw_buffer_load_b128(rB, vo + n * 1024, kof(i) * 5120, 0);
#pragma unroll
      for (int m = 0; m < 4; ++m) A[bf][m] = __builtin_amdgcn_raw_buffer_load_b128(rA, vo + m * 1024, kof(i) * 4096, 0);
    };
    const unsigned tl = lb + M_T1 + sw * 3840;
    {
      const int l40 = lane < 40 ? lane : 39;
      const int sl0 = kof(l40 >> 2) * 16 + 4 * (l40 & 3);
      __amdgpu_buffer_rsrc_t rG = mkrsrc(ws + WS_AGG, WS_ST - WS_AGG);
      f32x4 tv[2];
      u32x4 ag[15], al;
      tv[0] = __builtin_bit_cast(f32x4, __builtin_amdgcn_raw_buffer_load_b128(rG, sl0 * 8, WS_TAB - WS_AGG, 0));
      tv[1] = __builtin_bit_cast(f32x4, __builtin_amdgcn_raw_buffer_load_b128(rG, sl0 * 8 + 16, WS_TAB - WS_AGG, 0));
#pragma unroll
      for (int m = 0; m < 15; ++m)
        if (m < rc) ag[m] = __builtin_amdgcn_raw_buffer_load_b128(rG, sl0 * 4 + m * 2560, 0, 0);
      if (par) al = __builtin_amdgcn_raw_buffer_load_b128(rG, sl0 * 4, WS_AGL - WS_AGG + rc * 2560, 0);
      SB();
      load(0);
      load(1);
      SB();
      f32x2 P[4], gg[4];
#pragma unroll
      for (int k = 0; k < 4; ++k) { P[k] = cpow(tv[k >> 1][2 * (k & 1)], tv[k >> 1][2 * (k & 1) + 1], 128.f); gg[k] = (f32x2){0.f, 0.f}; }
      f32x2 L1[4], L64[4];
#pragma unroll
      for (int k = 0; k < 4; ++k) {
        L1[k] = cpow(tv[k >> 1][2 * (k & 1)], tv[k >> 1][2 * (k & 1) + 1], 1.f);
        L64[k] = cpow(tv[k >> 1][2 * (k & 1)], tv[k >> 1][2 * (k & 1) + 1], 64.f);
      }
      sts128f(tl + 2560 + l40 * 32, (f32x4){L1[0].x, L1[0].y, L1[1].x, L1[1].y});
      sts128f(tl + 2560 + l40 * 32 + 16, (f32x4){L1[2].x, L1[2].y, L1[3].x, L1[3].y});
#pragma unroll
      for (int m = 0; m < 15; ++m)
        if (m < rc) {
#pragma unroll
          for (int k = 0; k < 4; ++k) gg[k] = cfmap(P[k], gg[k], (f32x2){bflo(ag[m][k]), bfhi(ag[m][k])});
        }
      if (par) {
#pragma unroll
        for (int k = 0; k < 4; ++k) gg[k] = cfmap(L64[k], gg[k], (f32x2){bflo(al[k]), bfhi(al[k])});
      }
#pragma unroll
      for (int k = 0; k < 4; ++k)
        sts128f(tl + l40 * 64 + k * 16, (f32x4){tv[k >> 1][2 * (k & 1)], tv[k >> 1][2 * (k & 1) + 1], gg[k].x, gg[k].y});
    }
    TS(1);
#pragma unroll
    for (int m = 0; m < 4; ++m)
#pragma unroll
      for (int n = 0; n < 5; ++n) acc[m][n] = (f32x4){0.f, 0.f, 0.f, 0.f};
    const float nf = (float)(4 * c + 1);
    const unsigned tq = tl + q * 64, lq = tl + 2560 + q * 32;
    const unsigned tb = lb + M_RS + sw * 20480 + vo;
    u32x4 z[4];
    f32x4 tt[4], Lq[2];
    float zr[4], zi[4], e0, e1, e2, e3;
    auto rdt = [&](int i) {
#pragma unroll
      for (int d = 0; d < 4; ++d) tt[d] = lds128f(tq + i * 256 + d * 16);
    };
    auto rdl = [&](int i) { Lq[0] = lds128f(lq + i * 128); Lq[1] = lds128f(lq + i * 128 + 16); };
    auto opA = [&](int k) {
      const int m = k / 20 + 1, d = (k / 5) & 3, o = k % 5;
      const float Lx = Lq[d >> 1][2 * (d & 1)], Ly = Lq[d >> 1][2 * (d & 1) + 1];
      if (o == 0) e0 = vmul(Ly, zi[d]);
      else if (o == 1) e1 = vmul(Ly, zr[d]);
      else if (o == 2) zr[d] = vfms(Lx, zr[d], e0);
      else if (o == 3) zi[d] = vfma(Lx, zi[d], e1);
      else z[m][d] = pk2(zr[d], zi[d]);
    };
    auto opZ = [&](int k) {
      if (k >= 48) { z[0][k - 48] = pk2(zr[k - 48], zi[k - 48]); return; }
      const int d = k / 12, o = k % 12;
      if (o == 0) e0 = vmul(nf, tt[d].x);
      else if (o == 1) e1 = vmul(nf, tt[d].y);
      else if (o == 2) e0 = __builtin_amdgcn_exp2f(e0);
      else if (o == 3) e1 = __builtin_amdgcn_fractf(e1);
      else if (o == 4) e2 = __builtin_amdgcn_cosf(e1);
      else if (o == 5) e3 = __builtin_amdgcn_sinf(e1);
      else if (o == 6) e2 = vmul(e0, e2);
      else if (o == 7) e3 = vmul(e0, e3);
      else if (o == 8) e0 = vmul(e3, tt[d].w);
      else if (o == 9) e1 = vmul(e3, tt[d].z);
      else if (o == 10) zr[d] = vfms(e2, tt[d].z, e0);
      else zi[d] = vfma(e2, tt[d].w, e1);
    };
    rdt(0); rdl(0);
#pragma unroll
    for (int k = 0; k < 52; ++k) opZ(k);
    SB();
#pragma unroll
    for (int i = 0; i < 10; ++i) {
      if (i + 2 < 10) {
        if (SLP != SPRIO) __builtin_amdgcn_s_setprio(SLP);
        load(i + 2);
        SB();
        if (SLP != SPRIO) __builtin_amdgcn_s_setprio(SPRIO);
      }
      SB();
#pragma unroll
      for (int sl = 0; sl < 20; ++sl) {
        acc[sl / 5][sl % 5] = mfma(B[i % 3][sl % 5], A[i % 3][sl / 5], acc[sl / 5][sl % 5]);
        opA(3 * sl); opA(3 * sl + 1); opA(3 * sl + 2);
        if (sl == 12 && i < 9) rdt(i + 1);
        SB();
      }
#pragma unroll
      for (int sl = 0; sl < 20; ++sl) {
        acc[sl / 5][sl % 5] = mfma(B[i % 3][sl % 5], z[sl / 5], acc[sl / 5][sl % 5]);
        if (i < 9) {
          constexpr int st[21] = {0, 3, 5, 7, 10, 12, 15, 17, 19, 22, 24, 27, 29, 31, 34, 36, 39, 41, 43, 46, 52};
#pragma unroll
          for (int k = st[sl]; k < st[sl + 1]; ++k) opZ(k);
          if (sl == 12) rdl(i + 1);
        } else if (sl >= 2) {
          sts128f(tb + (sl - 2) * 1024, acc[(sl - 2) / 5][(sl - 2) % 5]);
        }
        SB();
      }
      TS(2 + i);
    }
    sts128f(tb + 18 * 1024, acc[3][3]);
    sts128f(tb + 19 * 1024, acc[3][4]);
    TS(14);
    BARRIER();
    TS(15);
    TSDUMP(33554432 + 1048576);
    return;
  }

  __builtin_amdgcn_s_setprio(OPRIO);
  __amdgpu_buffer_rsrc_t rA = mkrsrc(ws + WS_H1F + (size_t)rb * 48 * 4096, 48 * 4096);
  __amdgpu_buffer_rsrc_t rB = mkrsrc(ws + WS_W2F + (size_t)cg * 48 * 5120, 48 * 5120);
  u32x4 A[4][4], B[4][5];
  auto loadA = [&](int i) {
    int bf = i & 3, ks = w + 4 * ((i >> 1) + 6 * ((i & 1) ^ gh));
#pragma unroll
    for (int m = 0; m < 4; ++m) A[bf][m] = __builtin_amdgcn_raw_buffer_load_b128(rA, vo, ks * 4096 + ((w + m) & 3) * 1024, 0);
  };
  auto loadB = [&](int i) {
    int bf = i & 3, ks = w + 4 * ((i >> 1) + 6 * ((i & 1) ^ gh));
#pragma unroll
    for (int n = 0; n < 5; ++n) B[bf][n] = __builtin_amdgcn_raw_buffer_load_b128(rB, vo + n * 1024, ks * 5120, 0);
  };
  auto load = [&](int i) { loadA(i); loadB(i); };
  u32x2 xv[5], sk[5];
  f32x2 st;
  load(0);
  load(1);
  load(2);
  TS(1);
#pragma unroll
  for (int m = 0; m < 4; ++m)
#pragma unroll
    for (int n = 0; n < 5; ++n) acc[m][n] = (f32x4){0.f, 0.f, 0.f, 0.f};
  unsigned da[3];
#pragma unroll
  for (int i = 0; i < 12; ++i) {
    if (OTP != OPRIO && i == OTI) __builtin_amdgcn_s_setprio(OTP);
    if (i == 9) {
      int l2 = lane;
      asm volatile("" : "+v"(l2));
      const int trow = 64 * rb + 4 * (l2 & 15) + w;
      const int eo = trow * 640 + 80 * cg + 4 * (l2 >> 4);
      __amdgpu_buffer_rsrc_t rx = mkrsrc(x, 2621440), rd = mkrsrc(Dsk, 1280), rt = mkrsrc(ws + WS_ST, 16384);
      st = __builtin_bit_cast(f32x2, __builtin_amdgcn_raw_buffer_load_b64(rt, trow * 8, 0, 0));
#pragma unroll
      for (int n = 0; n < 5; ++n) {
        xv[n] = __builtin_amdgcn_raw_buffer_load_b64(rx, eo * 2 + n * 32, 0, 0);
        sk[n] = __builtin_amdgcn_raw_buffer_load_b64(rd, (l2 >> 4) * 8 + n * 32, 160 * cg, 0);
      }
      SB();
    }
    if (i + 3 < 12) {
      if (OLP != OPRIO && i >= OLI && (OTP == OPRIO || i < OTI)) __builtin_amdgcn_s_setprio(OLP);
      load(i + 3);
      SB();
      if (OLP != OPRIO && i >= OLI && (OTP == OPRIO || i < OTI)) __builtin_amdgcn_s_setprio(OPRIO);
    }
    SB();
    if (i < 11) {
#pragma unroll
      for (int m = 0; m < 4; ++m)
#pragma unroll
        for (int n = 0; n < 5; ++n) acc[m][n] = mfma(B[i & 3][n], A[i & 3][m], acc[m][n]);
    } else {
      int v2 = vo;
      asm volatile("" : "+v"(v2));
#pragma unroll
      for (int j = 1; j < 4; ++j) da[j - 1] = lb + M_RO + (((w + j) & 3) * 3 + (j - 1)) * 5120 + v2;
#pragma unroll
      for (int sl = 0; sl < 20; ++sl) {
        const int m = (sl / 5 + 1) & 3, n = sl % 5;
        acc[m][n] = mfma(B[i & 3][n], A[i & 3][m], acc[m][n]);
        if (sl >= 2 && sl < 17) sts128f(da[(sl - 2) / 5] + ((sl - 2) % 5) * 1024, acc[(sl - 2) / 5 + 1][(sl - 2) % 5]);
        SB();
      }
    }
    SB();
    TS(2 + i);
  }
  WAITL0();
  if (lane == 0) *(volatile AS3 unsigned*)(uintptr_t)(lb + M_CTL + w * 4) = 1u;
  {
#pragma unroll
    for (int n = 0; n < 5; ++n)
#pragma unroll
      for (int hf = 0; hf < 2; ++hf) {
        unsigned p = pk2a(vsub(bflo(xv[n][hf]), st.x), vsub(bfhi(xv[n][hf]), st.x));
        p = pk2a(vmul(bflo(p), st.y), vmul(bfhi(p), st.y));
        sk[n][hf] = pk2a(vmul(bflo(p), bflo(sk[n][hf])), vmul(bfhi(p), bfhi(sk[n][hf])));
      }
#pragma unroll
    for (int n = 0; n < 5; ++n) asm volatile("" : "+v"(sk[n]), "+v"(xv[n]));
  }
  for (;;) {
    u32x4 f = *(volatile AS3 u32x4*)(uintptr_t)(lb + M_CTL);
    if ((f.x & f.y & f.z & f.w) != 0u) break;
    __builtin_amdgcn_s_sleep(1);
  }
  asm volatile("" ::: "memory");
  TS(14);
  f32x4 xs[5], g2[5];
  {
    f32x4 po[5][3];
#pragma unroll
    for (int n = 0; n < 5; ++n)
#pragma unroll
      for (int j = 0; j < 3; ++j) po[n][j] = lds128f(lb + M_RO + ((w * 3 + j) * 5 + n) * 1024 + vo);
    SB();
#pragma unroll
    for (int n = 0; n < 5; ++n) {
      f32x4 o;
#pragma unroll
      for (int j = 0; j < 4; ++j) o[j] = vadd(vadd(acc[0][n][j], po[n][0][j]), vadd(po[n][1][j], po[n][2][j]));
#pragma unroll
      for (int hf = 0; hf < 2; ++hf) {
        unsigned p = pk2a(o[2 * hf], o[2 * hf + 1]);
        float v0 = bflo(p), v1 = bfhi(p);
        float e0 = __builtin_amdgcn_exp2f(vmul(v0, -1.44269504088896f)), e1 = __builtin_amdgcn_exp2f(vmul(v1, -1.44269504088896f));
        e0 = __builtin_amdgcn_rcpf(vadd(e0, 1.f)); e1 = __builtin_amdgcn_rcpf(vadd(e1, 1.f));
        p = pk2a(vmul(v0, e0), vmul(v1, e1));
        v0 = bflo(p); v1 = bfhi(p);
        xs[n][2 * hf] = vfma(v0, bflo(sk[n][hf]), bflo(xv[n][hf]));
        xs[n][2 * hf + 1] = vfma(v1, bfhi(sk[n][hf]), bfhi(xv[n][hf]));
        g2[n][2 * hf] = v0; g2[n][2 * hf + 1] = v1;
      }
    }
#pragma unroll
    for (int n = 0; n < 5; ++n) asm volatile("" : "+v"(xs[n]), "+v"(g2[n]));
  }
  __amdgpu_buffer_rsrc_t ro = mkrsrc(out, 5242880);
  int l2 = lane;
  asm volatile("" : "+v"(l2));
  const int eo2 = ((64 * rb + 4 * (l2 & 15) + w) * 640 + 80 * cg + 4 * (l2 >> 4)) * 4;
  TS(15);
  BARRIER_NW();
  TS(16);
  f32x4 ps[5][4];
#pragma unroll
  for (int n = 0; n < 5; ++n)
#pragma unroll
    for (int k = 0; k < 4; ++k) ps[n][k] = lds128f(lb + M_RS + (w * 5 + n) * 1024 + k * 20480 + vo);
  SB();
#pragma unroll
  for (int n = 0; n < 5; ++n) {
    f32x4 s = (ps[n][0] + ps[n][1]) + (ps[n][2] + ps[n][3]);
    f32x4 r = __builtin_elementwise_fma(s, g2[n], xs[n]);
    __builtin_amdgcn_raw_buffer_store_b128(__builtin_bit_cast(u32x4, r), ro, eo2 + n * 64, 0, 0);
    SB();
  }
}

extern "C" void kernel_launch(void* const* d_in, const int* in_sizes, int n_in, void* d_out, int out_size, void* d_ws,
                              size_t ws_size, hipStream_t stream) {
  const u16* x = (const u16*)d_in[0];
  k1<<<256, 512, 0, stream>>>(x, (const u16*)d_in[1], (const u16*)d_in[2], (const u16*)d_in[3], (const u16*)d_in[4],
                              (const u16*)d_in[5], (const u16*)d_in[6], (const u16*)d_in[7], (const u16*)d_in[10],
                              (const u16*)d_in[11], (char*)d_ws);
  k2<<<256, 512, 0, stream>>>(x, (const u16*)d_in[8], (char*)d_ws, (float*)d_out);
}
```

```cpp
#include <hip/hip_runtime.h>
#include <stdint.h>

typedef unsigned short u16;
typedef __attribute__((ext_vector_type(8))) __bf16 bf16x8;
typedef __attribute__((ext_vector_type(2))) __bf16 bf16x2;
typedef __attribute__((ext_vector_type(4))) float f32x4;
typedef __attribute__((ext_vector_type(2))) float f32x2;
typedef __attribute__((ext_vector_type(4))) unsigned u32x4;
typedef __attribute__((ext_vector_type(2))) unsigned u32x2;
typedef __attribute__((ext_vector_type(4))) short s16x4;
#define AS3 __attribute__((address_space(3)))
#define DEVI __device__ __forceinline__

constexpr size_t WS_H1F = 0;
constexpr size_t WS_HF = 6291456;
constexpr size_t WS_W2F = 11534336;
constexpr size_t WS_CF = 13500416;
constexpr size_t WS_AGG = 15138816;
constexpr size_t WS_AGL = 15220736;
constexpr size_t WS_TAB = 15302656;
constexpr size_t WS_ST = 15323136;

DEVI float bf2f(unsigned v) { return __uint_as_float(v << 16); }
DEVI float bflo(unsigned v) { return __uint_as_float(v << 16); }
DEVI float bfhi(unsigned v) { return __uint_as_float(v & 0xffff0000u); }
DEVI unsigned pk2(float a, float b) {
  bf16x2 r = {(__bf16)a, (__bf16)b};
  return __builtin_bit_cast(unsigned, r);
}
DEVI unsigned pk2a(float a, float b) { unsigned r; asm("v_cvt_pk_bf16_f32 %0, %1, %2" : "=v"(r) : "v"(a), "v"(b)); return r; }
DEVI float rbf(float a) { return bflo(pk2(a, 0.f)); }
template <int N> DEVI float shr(float v) {
  return __builtin_bit_cast(float, __builtin_amdgcn_update_dpp(0, __builtin_bit_cast(int, v), 0x110 + N, 0xf, 0xf, true));
}
template <int N> DEVI f32x2 shr2(f32x2 v) { f32x2 r; r.x = shr<N>(v.x); r.y = shr<N>(v.y); return r; }
DEVI f32x2 cpow(float a, float b, float n) {
  float mg = __builtin_amdgcn_exp2f(n * a);
  float rv = __builtin_amdgcn_fractf(n * b);
  f32x2 r;
  r.x = mg * __builtin_amdgcn_cosf(rv);
  r.y = mg * __builtin_amdgcn_sinf(rv);
  return r;
}
DEVI f32x2 cmul(f32x2 a, f32x2 b) { f32x2 r; r.x = a.x * b.x - a.y * b.y; r.y = a.x * b.y + a.y * b.x; return r; }
DEVI f32x2 pfma(f32x2 a, f32x2 b, f32x2 c) { return __builtin_elementwise_fma(a, b, c); }
DEVI f32x2 cfmap(f32x2 a, f32x2 b, f32x2 c) {
  f32x2 ax = {a.x, a.x}, ay = {-a.y, a.y}, bs = {b.y, b.x};
  return pfma(ay, bs, pfma(ax, b, c));
}
DEVI f32x2 cmulp(f32x2 a, f32x2 b) {
  f32x2 ax = {a.x, a.x}, ay = {-a.y, a.y}, bs = {b.y, b.x};
  return pfma(ay, bs, ax * b);
}
extern "C" __device__ __attribute__((const)) float mul_legacy_f32(float, float) __asm("llvm.amdgcn.fmul.legacy");
DEVI float vmul(float a, float b) { return mul_legacy_f32(a, b); }
DEVI float vfma(float a, float b, float c) { float r; asm("v_fma_f32 %0, %1, %2, %3" : "=v"(r) : "v"(a), "v"(b), "v"(c)); return r; }
DEVI float vadd(float a, float b) { float r; asm("v_add_f32 %0, %1, %2" : "=v"(r) : "v"(a), "v"(b)); return r; }
DEVI float vsub(float a, float b) { float r; asm("v_sub_f32 %0, %1, %2" : "=v"(r) : "v"(a), "v"(b)); return r; }
DEVI float vfms(float a, float b, float c) { float r; asm("v_fma_f32 %0, %1, %2, -%3" : "=v"(r) : "v"(a), "v"(b), "v"(c)); return r; }
DEVI float siluf(float a) { return a * __builtin_amdgcn_rcpf(1.f + __builtin_amdgcn_exp2f(-1.44269504088896f * a)); }
DEVI void swap16(unsigned& a, unsigned& b) { auto r = __builtin_amdgcn_permlane16_swap(a, b, false, false); a = r[0]; b = r[1]; }
DEVI void swap32(unsigned& a, unsigned& b) { auto r = __builtin_amdgcn_permlane32_swap(a, b, false, false); a = r[0]; b = r[1]; }
DEVI void swap16f(float& a, float& b) { unsigned x = __float_as_uint(a), y = __float_as_uint(b); swap16(x, y); a = __uint_as_float(x); b = __uint_as_float(y); }
DEVI void swap32f(float& a, float& b) { unsigned x = __float_as_uint(a), y = __float_as_uint(b); swap32(x, y); a = __uint_as_float(x); b = __uint_as_float(y); }

#define WAITV(n) asm volatile("s_waitcnt vmcnt(%0)" ::"n"(n) : "memory")
#define WAITL0() asm volatile("s_waitcnt lgkmcnt(0)" ::: "memory")
#define BARRIER() do { WAITL0(); __builtin_amdgcn_s_barrier(); asm volatile("" ::: "memory"); } while (0)
#define BARRIER_NW() do { asm volatile("" ::: "memory"); __builtin_amdgcn_s_barrier(); asm volatile("" ::: "memory"); } while (0)
#define SB() __builtin_amdgcn_sched_barrier(0)
DEVI void glds(const void* p, unsigned ldsaddr) {
  unsigned keep;
  asm volatile("s_mov_b32 %0, m0\n\ts_mov_b32 m0, %2\n\ts_nop 0\n\tglobal_load_lds_dwordx4 %1, off\n\ts_mov_b32 m0, %0"
               : "=&s"(keep) : "v"(p), "s"(ldsaddr) : "memory");
}

DEVI u32x4 lds128(unsigned off) { return *(const AS3 u32x4*)(uintptr_t)off; }
DEVI f32x4 lds128f(unsigned off) { return *(const AS3 f32x4*)(uintptr_t)off; }
DEVI f32x2 lds64f(unsigned off) { return *(const AS3 f32x2*)(uintptr_t)off; }
DEVI void sts128f(unsigned off, f32x4 v) { *(AS3 f32x4*)(uintptr_t)off = v; }
DEVI void sts64f(unsigned off, f32x2 v) { *(AS3 f32x2*)(uintptr_t)off = v; }
DEVI void sts32f(unsigned off, float v) { *(AS3 float*)(uintptr_t)off = v; }
DEVI u32x2 ldstr(unsigned off) {
  s16x4 r = __builtin_amdgcn_ds_read_tr16_b64_v4i16((AS3 s16x4*)(uintptr_t)off);
  return __builtin_bit_cast(u32x2, r);
}
DEVI u32x4 ldstr2(unsigned a, unsigned d) { u32x2 lo = ldstr(a), hi = ldstr(a + d); return (u32x4){lo.x, lo.y, hi.x, hi.y}; }
DEVI f32x4 mfma(u32x4 a, u32x4 b, f32x4 c) {
  return __builtin_amdgcn_mfma_f32_16x16x32_bf16(__builtin_bit_cast(bf16x8, a), __builtin_bit_cast(bf16x8, b), c, 0, 0, 0);
}
#define STAUX 16
#define SPRIO 2
#define OPRIO 0
#define OLP OPRIO
#define SLP SPRIO
#define OLI 0
#define OTP OPRIO
#define OTI 9
DEVI void st128(u32x4 v, __amdgpu_buffer_rsrc_t rs, int off) { __builtin_amdgcn_raw_buffer_store_b128(v, rs, off, 0, STAUX); }
DEVI __amdgpu_buffer_rsrc_t mkrsrc(const void* p, unsigned bytes) {
  return __builtin_amdgcn_make_buffer_rsrc((void*)p, 0, bytes, 0x00020000);
}
#define TSDECL
#define TS(k)
#define TSDUMP(base)

constexpr unsigned SLOT = 38912, OB = 16384, OW = 26624;
constexpr unsigned SPARE = 4 * SLOT;
constexpr unsigned T_TAB = SPARE;
constexpr unsigned T_AG = SPARE + 2560;
constexpr unsigned T_FLAG = SPARE + 3072;
constexpr unsigned K1_LDS = SPARE + 4096;
constexpr unsigned S_W2 = 2 * SLOT;
constexpr unsigned E_ST = 3 * SLOT;
constexpr unsigned E_CS = E_ST + 2048;
constexpr unsigned E_A5 = E_CS + 2048;

DEVI void stat(u32x4 f, float& s, float& q) {
#pragma unroll
  for (int r = 0; r < 4; ++r) {
    float lo = bflo(f[r]), hi = bfhi(f[r]);
    s += lo; q = fmaf(lo, lo, q);
    s += hi; q = fmaf(hi, hi, q);
  }
}
template <int NM, int NV> DEVI void ileave() {
#pragma unroll
  for (int i = 0; i < NM; ++i) {
    __builtin_amdgcn_sched_group_barrier(0x008, 1, 0);
    __builtin_amdgcn_sched_group_barrier(0x002, NV, 0);
  }
}
struct Tab { f32x2 L, L4, L8, L16, L32, L64, co; };
DEVI void rdtab(Tab& t, unsigned a) {
  f32x4 v0 = lds128f(a), v1 = lds128f(a + 16), v2 = lds128f(a + 32);
  f32x2 v3 = lds64f(a + 48);
  t.L = (f32x2){v0.x, v0.y}; t.L4 = (f32x2){v0.z, v0.w}; t.L8 = (f32x2){v1.x, v1.y}; t.L16 = (f32x2){v1.z, v1.w};
  t.L32 = (f32x2){v2.x, v2.y}; t.L64 = (f32x2){v2.z, v2.w}; t.co = v3;
}

DEVI void dma_s(unsigned voff, const char* sbase, unsigned ldsaddr, int t, int lim) {
  asm volatile("s_cmp_lt_i32 %3, %4\n\ts_cbranch_scc0 .Ldma%=\n\ts_mov_b32 m0, %2\n\ts_nop 0\n\tglobal_load_lds_dwordx4 %0, %1\n.Ldma%=:"
               ::"v"(voff), "s"(sbase), "s"(ldsaddr), "s"(t), "n"(lim) : "memory", "scc");
}
DEVI void dma_v(const char* p, unsigned ldsaddr, int t, int lim) {
  asm volatile("s_cmp_lt_i32 %2, %3\n\ts_cbranch_scc0 .Ldmb%=\n\ts_mov_b32 m0, %1\n\ts_nop 0\n\tglobal_load_lds_dwordx4 %0, off\n.Ldmb%=:"
               ::"v"(p), "s"(ldsaddr), "s"(t), "n"(lim) : "memory", "scc");
}
DEVI void dma_s0(unsigned voff, const char* sbase, unsigned ldsaddr) {
  asm volatile("s_mov_b32 m0, %2\n\ts_nop 0\n\tglobal_load_lds_dwordx4 %0, %1" ::"v"(voff), "s"(sbase), "s"(ldsaddr) : "memory");
}
DEVI void dma_v0(const char* p, unsigned ldsaddr) {
  asm volatile("s_mov_b32 m0, %1\n\ts_nop 0\n\tglobal_load_lds_dwordx4 %0, off" ::"v"(p), "s"(ldsaddr) : "memory");
}
#define WAITV2(t, lim, A, B) asm volatile("s_cmp_lt_i32 %0, %1\n\ts_cbranch_scc1 .Lwv%=\n\ts_waitcnt vmcnt(%3)\n.Lwv%=:\n\ts_waitcnt vmcnt(%2)" ::"s"(t), "n"(lim), "n"(A), "n"(B) : "memory", "scc")
#define LGKM0() __builtin_amdgcn_s_waitcnt(0xc07f)

__global__ __launch_bounds__(512, 2) void k1(const u16* x, const u16* Lre, const u16* Lim, const u16* Bre, const u16* Bim,
                                             const u16* Cre, const u16* Cim, const u16* ldt, const u16* w1, const u16* w2,
                                             char* ws) {
  __shared__ __attribute__((aligned(1024))) char smem[K1_LDS];
  const unsigned lb = (unsigned)(uintptr_t)(AS3 char*)smem;
  const int b = blockIdx.x, xcd = b & 7, jb = b >> 3;
  const int rc = (xcd >> 1) * 4 + (jb & 3), g = (xcd & 1) * 8 + (jb >> 2);
  const int tid = threadIdx.x, lane = tid & 63;
  const int w = __builtin_amdgcn_readfirstlane(tid >> 6);
  const int c = lane & 15, q = lane >> 4;
  const int l3 = lane >> 3, l7 = lane & 7;
  const int h = (w >> 1) & 1, a = w & 1;
  const int kk0 = h ^ a;
  TSDECL;
  TS(0);

  const u32x4 ONES = {0x3f803f80u, 0x3f803f80u, 0x3f803f80u, 0x3f803f80u};
  unsigned xo[2];
#pragma unroll
  for (int p = 0; p < 2; ++p) xo[p] = lb + 8192 * h + c * 128 + (((4 * (kk0 ^ p) + q) ^ (c & 7)) << 4);
  float sv[4] = {0.f, 0.f, 0.f, 0.f};
  float t0, t1;
  const int rb = 2 * rc + h;
  unsigned pr[3];
  auto prload = [&]() {
    int p = 40 * g + (lane < 40 ? lane : 0);
    const u16* a0 = Lre + p; const u16* a1 = Lim + p; const u16* a2 = ldt + p;
    asm volatile("global_load_ushort %0, %1, off" : "=v"(pr[0]) : "v"(a0) : "memory");
    asm volatile("global_load_ushort %0, %1, off" : "=v"(pr[1]) : "v"(a1) : "memory");
    asm volatile("global_load_ushort %0, %1, off" : "=v"(pr[2]) : "v"(a2) : "memory");
  };
  float lr, li, ea, eb;
  auto tab0 = [&]() {
    asm volatile("" : "+v"(pr[0]), "+v"(pr[1]), "+v"(pr[2]));
    lr = bf2f(pr[0]); li = bf2f(pr[1]);
    float dt = rbf(expf(bf2f(pr[2])));
    ea = dt * lr * 1.44269504088896f; eb = dt * li * 0.15915494309189535f;
  };
  auto tab1 = [&]() {
    const float n = (w == 0 || w > 5) ? 1.f : (float)(2 << w);
    f32x2 P = cpow(ea, eb, n);
    if (w == 6) {
      float nr = P.x - 1.f, ni = P.y, dd = 2.f / (lr * lr + li * li);
      P = (f32x2){(nr * lr + ni * li) * dd, (ni * lr - nr * li) * dd};
    }
    if (w == 7) {
      P = (f32x2){ea, eb};
      if (lane >= 62) sts32f(lb + T_FLAG + (lane - 62) * 4, 0.f);
    }
    if (lane < 40) sts64f(lb + T_TAB + lane * 64 + w * 8, P);
  };
  auto opS = [&](const u32x4& f, float& s, float& qq, int k) {
    const int r = k / 6, o = k % 6;
    if (o == 0) t0 = bflo(f[r]);
    else if (o == 1) t1 = bfhi(f[r]);
    else if (o == 2) s = vadd(s, t0);
    else if (o == 3) qq = vfma(t0, t0, qq);
    else if (o == 4) s = vadd(s, t1);
    else qq = vfma(t1, t1, qq);
  };

  auto mr4 = [&](float* m, float* r) {
#pragma unroll
    for (int i = 0; i < 4; ++i) {
      f32x4 v = lds128f(lb + E_ST + (64 * h + 4 * c + i) * 16);
      float mean = (v.x + v.z) * (1.f / 640.f);
      float var = fmaxf((v.y + v.w) * (1.f / 640.f) - mean * mean, 0.f);
      float den = rbf(__builtin_amdgcn_sqrtf(rbf(rbf(var) + 1e-5f)));
      m[i] = rbf(mean); r[i] = __builtin_amdgcn_rcpf(den);
    }
  };
  auto put_stats = [&](int i0) {
    swap32f(sv[0], sv[2]); swap32f(sv[1], sv[3]);
    float w0 = sv[0] + sv[2], w1v = sv[1] + sv[3];
    swap16f(w0, w1v);
    sts32f(lb + E_ST + ((64 * h + 4 * c + i0 + (q >> 1)) * 4 + 2 * a + (q & 1)) * 4, w0 + w1v);
  };

  if (w < 4) {
    unsigned vo[4];
#pragma unroll
    for (int v = 0; v < 4; ++v) {
      int rho = 8 * (w + 4 * v) + l3;
      vo[v] = (64 * (rho >> 6) + 4 * (rho & 15) + ((rho >> 4) & 3)) * 1280 + ((l7 ^ l3) << 4);
    }
    const char* sb0 = (const char*)x + rc * 163840;
    const char* sb1 = sb0;
    unsigned dst = lb + w * 1024, dstep = 4096;
    auto issue0 = [&](int st) {
#pragma unroll
      for (int v = 0; v < 4; ++v) dma_s0(vo[v], sb0 + st * 128, dst + st * SLOT + v * 4096);
    };
    issue0(0);
    prload();
    BARRIER_NW();
    issue0(1);
    BARRIER_NW();
    issue0(2);
    unsigned wo[2][3];
#pragma unroll
    for (int u = 0; u < 3; ++u) {
      int tl = a ? (u == 2 ? 3 : 4 + u) : u;
      wo[0][u] = lb + OW + (8 * q + (c >> 2)) * 192 + (c & 3) * 8 + ((tl ^ (q & 1)) << 5) + kk0 * 6144;
      wo[1][u] = wo[0][u] + 6144 - 2 * kk0 * 6144;
    }
    f32x4 acc[4][3], cs[3];
#pragma unroll
    for (int u = 0; u < 3; ++u) {
      cs[u] = (f32x4){0.f, 0.f, 0.f, 0.f};
#pragma unroll
      for (int i = 0; i < 4; ++i) acc[i][u] = cs[u];
    }
    u32x4 X[2][2][4], W[2][2][3];
    auto rd = [&](int r, int k) {
      const int p = k / 10, o = k % 10;
      if (o < 6) {
        u32x2 v = ldstr(wo[p][o >> 1] + (o & 1) * 768);
        W[r][p][o >> 1][2 * (o & 1)] = v.x; W[r][p][o >> 1][2 * (o & 1) + 1] = v.y;
      } else X[r][p][o - 6] = lds128(xo[p] + 2048 * (o - 6));
    };
    auto adv = [&](unsigned d) {
#pragma unroll
      for (int p = 0; p < 2; ++p) {
        xo[p] += d;
#pragma unroll
        for (int u = 0; u < 3; ++u) wo[p][u] += d;
      }
    };
    int unit = 2 * jb + (w & 1);
    if (w < 2) { if (unit >= 60) unit -= 4; } else { if (unit >= 50) unit -= 14; }
    const int u5 = unit / 5;
    const int uks = (w < 2 ? 12 : 10) * (xcd >> 1) + u5, udb = 5 * (xcd & 1) + (unit - 5 * u5);

    WAITV(8);
    BARRIER_NW();
    TS(1);
#pragma unroll
    for (int k = 0; k < 20; ++k) rd(0, k);
    adv(SLOT);
    SB();
    dma_s0(vo[0], sb0 + 384, dst + 3 * SLOT);
    tab0(); SB();
    dma_s0(vo[1], sb0 + 384, dst + 3 * SLOT + 4096);
    tab1(); SB();
    dma_s0(vo[2], sb0 + 384, dst + 3 * SLOT + 8192);
    dma_s0(vo[3], sb0 + 384, dst + 3 * SLOT + 12288);
    sb0 += 512; sb1 = sb0;
    LGKM0();
    WAITV(8);
    BARRIER_NW();
    TS(2);
    auto stage = [&](int r, int t) {
#pragma unroll
      for (int sl = 0; sl < 27; ++sl) {
        if (sl < 24) {
          const int p = sl / 12, i = (sl % 12) / 3, u = sl % 3;
          acc[i][u] = mfma(W[r][p][u], X[r][p][i], acc[i][u]);
        } else cs[sl - 24] = mfma(W[r][1][sl - 24], ONES, cs[sl - 24]);
        if (sl < 20) rd(r ^ 1, sl);
        if (sl % 6 == 2 && sl < 24) dma_s(vo[sl / 6], sl < 12 ? sb0 : sb1, dst + (sl / 6) * dstep, t, 7);
        if (sl < 12) { opS(X[r][0][0], sv[0], sv[1], 2 * sl); opS(X[r][0][0], sv[0], sv[1], 2 * sl + 1); }
        else if (sl < 24) { opS(X[r][1][1], sv[2], sv[3], 2 * sl - 24); opS(X[r][1][1], sv[2], sv[3], 2 * sl - 23); }
        SB();
      }
    };
    for (int t = 0; t < 10; t += 2) {
      if (t == 6) {
        dst = lb + S_W2 + w * 4096; dstep = 1024;
        if (w < 2) {
          sb0 = (const char*)w2 + uks * 40960 + udb * 128; sb1 = sb0;
#pragma unroll
          for (int v = 0; v < 4; ++v) {
            int r = 8 * v + l3;
            vo[v] = r * 1280 + ((l7 ^ (((r >> 1) & 3) << 1)) << 4);
          }
        } else {
          int G = uks / 5, kr = uks - 5 * G;
          int p0 = 80 * G + (kr < 4 ? 40 * (kr >> 1) + 16 * (kr & 1) : 32);
          sb0 = (const char*)Cre + udb * 81920 + p0 * 2; sb1 = (const char*)Cim + udb * 81920 + p0 * 2;
          vo[0] = lane * 1280; vo[1] = vo[0] + (kr < 4 ? 16 : 80); vo[2] = vo[0]; vo[3] = vo[1];
        }
      }
      stage(0, t);
      adv((t & 2) ? -3 * SLOT : SLOT);
      dst += SLOT; sb0 += 128; sb1 += 128;
      LGKM0();
      WAITV2(t, 8, 8, 0);
      BARRIER_NW();
      TS(3 + t);
      stage(1, t + 1);
      adv(SLOT);
      dst += (t & 2) ? -3 * SLOT : SLOT; sb0 += 128; sb1 += 128;
      LGKM0();
      WAITV2(t, 6, 8, 4);
      BARRIER_NW();
      TS(4 + t);
    }
    put_stats(0);
    if (c == 0) {
#pragma unroll
      for (int u = 0; u < 3; ++u) sts128f(lb + E_CS + ((w * 3 + u) * 4 + q) * 16, cs[u]);
    }
    BARRIER();
    TS(13);
    float m[4], r[4];
    mr4(m, r);
#pragma unroll
    for (int u = 0; u < 3; ++u) cs[u] += lds128f(lb + E_CS + (((w ^ 2) * 3 + u) * 4 + q) * 16);
    if (g == 0 && a == 0 && q == 0) {
      f32x4* st = (f32x4*)(ws + WS_ST + (64 * rb + 4 * c) * 8);
      st[0] = (f32x4){m[0], r[0], m[1], r[1]};
      st[1] = (f32x4){m[2], r[2], m[3], r[3]};
    }
    __amdgpu_buffer_rsrc_t rs = mkrsrc(ws + WS_H1F, 6291456);
    unsigned o[4][6];
#pragma unroll
    for (int i = 0; i < 4; ++i) {
      f32x2 nm = {-m[i], -m[i]}, rr = {r[i], r[i]}, re = {-1.44269504088896f * r[i], -1.44269504088896f * r[i]};
#pragma unroll
      for (int u = 0; u < 3; ++u)
#pragma unroll
        for (int hf = 0; hf < 2; ++hf) {
          f32x2 v = pfma((f32x2){cs[u][2 * hf], cs[u][2 * hf + 1]}, nm, (f32x2){acc[i][u][2 * hf], acc[i][u][2 * hf + 1]});
          f32x2 e = v * re;
          e.x = __builtin_amdgcn_exp2f(e.x); e.y = __builtin_amdgcn_exp2f(e.y);
          e = e + (f32x2){1.f, 1.f};
          e.x = __builtin_amdgcn_rcpf(e.x); e.y = __builtin_amdgcn_rcpf(e.y);
          v = (v * rr) * e;
          o[i][2 * u + hf] = pk2(v.x, v.y);
        }
      st128((u32x4){o[i][0], o[i][1], o[i][2], o[i][3]}, rs, lane * 16 + ((rb * 48 + 3 * g + 2 * a) * 4 + i) * 1024);
      if (i & 1) {
        swap16(o[i - 1][4], o[i][4]); swap16(o[i - 1][5], o[i][5]);
        st128((u32x4){o[i - 1][4], o[i - 1][5], o[i][4], o[i][5]}, rs,
              (c + 16 * (2 * a + (q >> 1))) * 16 + ((rb * 48 + 3 * g + 1) * 4 + (i - 1) + (q & 1)) * 1024);
      }
    }
    TS(14);
    SB();
    if (w < 2) {
      const bool nat = (uks % 3) == 1;
      int r0 = (nat ? 8 * q : 4 * q) + (c >> 2);
      int dr = nat ? 4 : 16;
      unsigned a0 = lb + S_W2 + w * 4096 + r0 * 128 + (c & 3) * 8;
      int f0 = (r0 >> 1) & 3, f1 = ((r0 + dr) >> 1) & 3;
#pragma unroll
      for (int j = 0; j < 4; ++j) {
        u32x2 lo = ldstr(a0 + ((j ^ f0) << 5));
        u32x2 hi = ldstr(a0 + dr * 128 + ((j ^ f1) << 5));
        int Dt = 4 * udb + j, cg = Dt / 5, nt = Dt - 5 * cg;
        *(u32x4*)(ws + WS_W2F + (size_t)(((cg * 48 + uks) * 5 + nt) * 1024) + lane * 16) = (u32x4){lo.x, lo.y, hi.x, hi.y};
      }
      if (rc == 0 && w == 0 && lane < 40) {
        int sg = lane < 32 ? (5 * (g >> 1) + 2 * (g & 1) + (lane >> 4)) * 16 + 4 * ((lane >> 1) & 3) + 2 * ((lane >> 3) & 1) + (lane & 1)
                           : (5 * (g >> 1) + 4) * 16 + 8 * (g & 1) + 4 * (lane & 1) + ((lane - 32) >> 1);
        *(f32x2*)(ws + WS_TAB + sg * 8) = lds64f(lb + T_TAB + lane * 64 + 56);
      }
    } else {
      u32x4 sj[4];
#pragma unroll
      for (int v = 0; v < 4; ++v) sj[v] = lds128(lb + S_W2 + w * 4096 + v * 1024 + lane * 16);
      unsigned P[2][4][2];
#pragma unroll
      for (int ga = 0; ga < 2; ++ga)
#pragma unroll
        for (int j = 0; j < 4; ++j) {
          unsigned im = sj[2 + ga][j] ^ 0x80008000u;
          P[ga][j][0] = __builtin_amdgcn_perm(im, sj[ga][j], 0x05040100u);
          P[ga][j][1] = __builtin_amdgcn_perm(im, sj[ga][j], 0x07060302u);
        }
      int Dt = 4 * udb + q, cg = Dt / 5, nt = Dt - 5 * cg;
      char* dd = ws + WS_CF + (size_t)(((cg * 40 + uks) * 5 + nt) * 1024) + c * 16;
      if (uks % 5 < 4) {
#pragma unroll
        for (int j = 0; j < 4; ++j) *(u32x4*)(dd + j * 256) = (u32x4){P[0][j][0], P[0][j][1], P[1][j][0], P[1][j][1]};
      } else {
#pragma unroll
        for (int j = 0; j < 4; ++j)
          *(u32x4*)(dd + j * 256) = (u32x4){P[j >> 1][0][j & 1], P[j >> 1][1][j & 1], P[j >> 1][2][j & 1], P[j >> 1][3][j & 1]};
      }
    }
    TS(15);
  } else {
    const int wb = w - 4;
    const char* bp[3];
    unsigned vo[3];
    const int e2 = wb < 2 ? 8 + wb : 6 + wb;
#pragma unroll
    for (int v = 0; v < 3; ++v) {
      int n = 8 * (v < 2 ? wb + 4 * v : e2) + l3;
      bp[v] = (const char*)((n & 1) ? Bim : Bre) + (40 * g + (n >> 1)) * 1280 + ((l7 ^ l3) << 4);
      int idx = 64 * (wb + 4 * v) + lane, k = idx / 12, uu = idx - 12 * k;
      vo[v] = k * 3072 + (((uu >> 1) ^ ((k >> 3) & 1)) << 5) + ((uu & 1) << 4);
    }
    const char* sb = (const char*)w1 + 192 * g;
    unsigned dst = lb + OB + wb * 1024;
    const unsigned d2 = (e2 - wb) * 1024;
    auto piece0 = [&](int st, int j) {
      const int v = j >> 1;
      if (j & 1) dma_s0(vo[v], sb + st * 196608, dst + st * SLOT + (OW - OB) + v * 4096);
      else { dma_v0(bp[v], dst + st * SLOT + (v < 2 ? v * 4096 : d2)); bp[v] += 128; }
    };
    auto issue0 = [&](int st) {
#pragma unroll
      for (int j = 0; j < 6; ++j) piece0(st, j);
    };
    issue0(0);
    prload();
    BARRIER_NW();
    issue0(1);
    BARRIER_NW();
    issue0(2);
    unsigned bo[2];
#pragma unroll
    for (int p = 0; p < 2; ++p) bo[p] = xo[p] - 8192 * h + OB + 4096 * a;
    const int r5 = c ^ (2 * a);
    unsigned f5 = lb + OB + 8192 + r5 * 128 + (((4 * kk0 + q) ^ (r5 & 7)) << 4);
    f32x4 acc[4][2], ac5[5], cs[2];
#pragma unroll
    for (int i = 0; i < 4; ++i) { acc[i][0] = (f32x4){0.f, 0.f, 0.f, 0.f}; acc[i][1] = acc[i][0]; ac5[i] = acc[i][0]; }
    ac5[4] = ac5[0]; cs[0] = ac5[0]; cs[1] = ac5[0];
    u32x4 X[2][2][4], W[2][2][2], W5[2];
    auto rd = [&](int r, int k) {
      if (k == 6) { W5[r] = lds128(f5); return; }
      const int p = k / 7, o = k % 7;
      if (o < 2) W[r][p][o] = lds128(bo[p] + 2048 * o);
      else X[r][p][o - 2] = lds128(xo[p] + 2048 * (o - 2));
    };
    auto adv = [&](unsigned d) { xo[0] += d; xo[1] += d; bo[0] += d; bo[1] += d; f5 += d; };
    WAITV(12);
    BARRIER_NW();
    TS(1);
#pragma unroll
    for (int k = 0; k < 13; ++k) rd(0, k);
    adv(SLOT);
    SB();
    piece0(3, 0); piece0(3, 1);
    tab0(); SB();
    piece0(3, 2); piece0(3, 3);
    tab1(); SB();
    piece0(3, 4); piece0(3, 5);
    sb += 4 * 196608;
    LGKM0();
    WAITV(12);
    BARRIER_NW();
    TS(2);
    auto stage = [&](int r, int t) {
#pragma unroll
      for (int sl = 0; sl < 23; ++sl) {
        if (sl < 8) acc[sl >> 1][sl & 1] = mfma(W[r][0][sl & 1], X[r][0][sl >> 1], acc[sl >> 1][sl & 1]);
        else if (sl < 12) ac5[sl - 8] = mfma(W5[r], X[r][0][sl - 8], ac5[sl - 8]);
        else if (sl == 12) ac5[4] = mfma(W5[r], ONES, ac5[4]);
        else if (sl < 21) acc[(sl - 13) >> 1][(sl - 13) & 1] = mfma(W[r][1][(sl - 13) & 1], X[r][1][(sl - 13) >> 1], acc[(sl - 13) >> 1][(sl - 13) & 1]);
        else cs[sl - 21] = mfma(W[r][1][sl - 21], ONES, cs[sl - 21]);
        if (sl < 13) rd(r ^ 1, sl);
        if (sl % 4 == 1 && sl < 23) {
          const int j = sl / 4, v = j >> 1;
          if (j & 1) dma_s(vo[v], sb, dst + (OW - OB) + v * 4096, t, 6);
          else { dma_v(bp[v], dst + (v < 2 ? v * 4096 : d2), t, 6); bp[v] += 128; }
        }
        constexpr int st[24] = {0, 2, 4, 6, 8, 10, 12, 14, 16, 18, 20, 22, 24, 26, 29, 31, 34, 36, 39, 41, 44, 46, 48, 48};
#pragma unroll
        for (int k = st[sl]; k < st[sl + 1]; ++k) {
          if (k < 24) opS(X[r][0][2], sv[0], sv[1], k); else opS(X[r][1][3], sv[2], sv[3], k - 24);
        }
        SB();
      }
    };
    for (int t = 0; t < 10; t += 2) {
      stage(0, t);
      adv((t & 2) ? -3 * SLOT : SLOT);
      dst += SLOT; sb += 196608;
      LGKM0();
      WAITV2(t, 6, 12, 6);
      BARRIER_NW();
      TS(3 + t);
      stage(1, t + 1);
      adv(SLOT);
      dst += (t & 2) ? -3 * SLOT : SLOT; sb += 196608;
      LGKM0();
      WAITV2(t, 6, 12, 0);
      BARRIER_NW();
      TS(4 + t);
    }
    Tab tb[5];
#pragma unroll
    for (int k = 0; k < 4; ++k) rdtab(tb[k], lb + T_TAB + (16 * a + 8 * (k >> 1) + 2 * q + (k & 1)) * 64);
    rdtab(tb[4], lb + T_TAB + (32 + 2 * q + a) * 64);
    put_stats(2);
    if (c == 0) {
      sts128f(lb + E_CS + ((w * 3 + 0) * 4 + q) * 16, cs[0]);
      sts128f(lb + E_CS + ((w * 3 + 1) * 4 + q) * 16, cs[1]);
    }
#pragma unroll
    for (int i = 0; i < 5; ++i) sts64f(lb + E_A5 + (((w - 4) * 5 + i) * 64 + lane) * 8, (f32x2){ac5[i].z, ac5[i].w});
    BARRIER();
    TS(13);
    float m[4], r[4];
    mr4(m, r);
    cs[0] += lds128f(lb + E_CS + (((w ^ 2) * 3 + 0) * 4 + q) * 16);
    cs[1] += lds128f(lb + E_CS + (((w ^ 2) * 3 + 1) * 4 + q) * 16);
    f32x2 u[5][4], csv[5];
#pragma unroll
    for (int i = 0; i < 4; ++i) {
      u[0][i] = (f32x2){acc[i][0].x, acc[i][0].y}; u[1][i] = (f32x2){acc[i][0].z, acc[i][0].w};
      u[2][i] = (f32x2){acc[i][1].x, acc[i][1].y}; u[3][i] = (f32x2){acc[i][1].z, acc[i][1].w};
      u[4][i] = (f32x2){ac5[i].x, ac5[i].y} + lds64f(lb + E_A5 + ((((w - 4) ^ 1) * 5 + i) * 64 + lane) * 8);
    }
    csv[0] = (f32x2){cs[0].x, cs[0].y}; csv[1] = (f32x2){cs[0].z, cs[0].w};
    csv[2] = (f32x2){cs[1].x, cs[1].y}; csv[3] = (f32x2){cs[1].z, cs[1].w};
    csv[4] = (f32x2){ac5[4].x, ac5[4].y} + lds64f(lb + E_A5 + ((((w - 4) ^ 1) * 5 + 4) * 64 + lane) * 8);
#pragma unroll
    for (int i = 0; i < 4; ++i) {
      f32x2 nm = {-m[i], -m[i]}, rr = {r[i], r[i]};
#pragma unroll
      for (int k = 0; k < 5; ++k) u[k][i] = cmulp(tb[k].co, pfma(csv[k], nm, u[k][i]) * rr);
    }
    f32x2 hh[5];
#pragma unroll
    for (int k = 0; k < 5; ++k) hh[k] = u[k][0];
#pragma unroll
    for (int i = 1; i < 4; ++i)
#pragma unroll
      for (int k = 0; k < 5; ++k) hh[k] = cfmap(tb[k].L, hh[k], u[k][i]);
#pragma unroll
    for (int k = 0; k < 5; ++k) hh[k] = cfmap(tb[k].L4, shr2<1>(hh[k]), hh[k]);
#pragma unroll
    for (int k = 0; k < 5; ++k) hh[k] = cfmap(tb[k].L8, shr2<2>(hh[k]), hh[k]);
#pragma unroll
    for (int k = 0; k < 5; ++k) hh[k] = cfmap(tb[k].L16, shr2<4>(hh[k]), hh[k]);
#pragma unroll
    for (int k = 0; k < 5; ++k) hh[k] = cfmap(tb[k].L32, shr2<8>(hh[k]), hh[k]);
    const int ksr = 5 * (g >> 1) + 2 * (g & 1) + a, ks5 = 5 * (g >> 1) + 4;
    f32x2 ag[5];
#pragma unroll
    for (int k = 0; k < 5; ++k) ag[k] = hh[k];
    if (h == 0) {
      if (c == 15) {
        unsigned ta = lb + T_AG + (a * 4 + q) * 48;
        sts128f(ta, (f32x4){ag[0].x, ag[0].y, ag[1].x, ag[1].y});
        sts128f(ta + 16, (f32x4){ag[2].x, ag[2].y, ag[3].x, ag[3].y});
        sts64f(ta + 32, ag[4]);
      }
      WAITL0();
      if (lane == 0) *(volatile AS3 unsigned*)(uintptr_t)(lb + T_FLAG + a * 4) = 1u;
    }
#pragma unroll
    for (int k = 0; k < 5; ++k) hh[k] = shr2<1>(hh[k]);
    __amdgpu_buffer_rsrc_t rs = mkrsrc(ws + WS_HF, 5242880);
    unsigned o5[4];
#pragma unroll
    for (int i = 0; i < 4; ++i) {
      u32x4 o;
#pragma unroll
      for (int k = 0; k < 5; ++k) {
        hh[k] = cfmap(tb[k].L, hh[k], u[k][i]);
        if (k < 4) o[k] = pk2(hh[k].x, hh[k].y); else o5[i] = pk2(hh[k].x, hh[k].y);
      }
      st128(o, rs, lane * 16 + ((rb * 40 + ksr) * 4 + i) * 1024);
    }
    swap32(o5[0], o5[2]); swap32(o5[1], o5[3]);
    swap16(o5[0], o5[1]); swap16(o5[2], o5[3]);
    st128((u32x4){o5[0], o5[1], o5[2], o5[3]}, rs, (c + 16 * (2 * (g & 1) + a)) * 16 + ((rb * 40 + ks5) * 4 + q) * 1024);
    if (h == 1) {
      while (*(volatile AS3 unsigned*)(uintptr_t)(lb + T_FLAG + a * 4) == 0u) __builtin_amdgcn_s_sleep(1);
      asm volatile("" ::: "memory");
      unsigned ta = lb + T_AG + (a * 4 + q) * 48;
      f32x4 l0 = lds128f(ta), l1 = lds128f(ta + 16);
      f32x2 l2 = lds64f(ta + 32);
      ag[0] = cfmap(tb[0].L64, (f32x2){l0.x, l0.y}, ag[0]); ag[1] = cfmap(tb[1].L64, (f32x2){l0.z, l0.w}, ag[1]);
      ag[2] = cfmap(tb[2].L64, (f32x2){l1.x, l1.y}, ag[2]); ag[3] = cfmap(tb[3].L64, (f32x2){l1.z, l1.w}, ag[3]);
      ag[4] = cfmap(tb[4].L64, l2, ag[4]);
    }
    if (c == 15) {
      char* base = ws + (h ? WS_AGG : WS_AGL) + rc * 2560;
      *(u32x4*)(base + (ksr * 16 + 4 * q) * 4) = (u32x4){pk2(ag[0].x, ag[0].y), pk2(ag[1].x, ag[1].y), pk2(ag[2].x, ag[2].y), pk2(ag[3].x, ag[3].y)};
      *(unsigned*)(base + (ks5 * 16 + 8 * (g & 1) + 4 * a + q) * 4) = pk2(ag[4].x, ag[4].y);
    }
    TS(15);
  }
}

constexpr unsigned M_RO = 0;
constexpr unsigned M_RS = 61440;
constexpr unsigned M_T1 = M_RS + 81920;
constexpr unsigned M_CTL = M_T1 + 15360;
constexpr unsigned K2_LDS = M_CTL + 64;

__global__ __launch_bounds__(512, 2) void k2(const u16* x, const u16* Dsk, char* ws, float* out) {
  __shared__ __attribute__((aligned(1024))) char smem[K2_LDS];
  const unsigned lb = (unsigned)(uintptr_t)(AS3 char*)smem;
  const int tid = threadIdx.x, lane = tid & 63;
  const int w = __builtin_amdgcn_readfirstlane(tid >> 6);
  const int c = lane & 15, q = lane >> 4;
  const int b = blockIdx.x, xcd = b & 7, jb = b >> 3;
  const int rb = (xcd >> 1) * 8 + (jb & 7), cg = (xcd & 1) * 4 + (jb >> 3), gh = xcd & 1;
  const int rc = rb >> 1, par = rb & 1;
  const int vo = lane * 16;
  TSDECL;
  TS(0);
  f32x4 acc[4][5];
  if (tid < 4) sts32f(lb + M_CTL + tid * 4, 0.f);
  BARRIER();

  if (w >= 4) {
    const int sw = w - 4;
    __builtin_amdgcn_s_setprio(SPRIO);
    __amdgpu_buffer_rsrc_t rA = mkrsrc(ws + WS_HF + (size_t)(rb * 40) * 4096, 40 * 4096);
    __amdgpu_buffer_rsrc_t rB = mkrsrc(ws + WS_CF + (size_t)(cg * 40) * 5120, 40 * 5120);
    auto kof = [&](int i) { return 5 * sw + (i >> 1) + 20 * ((i & 1) ^ gh); };
    u32x4 A[3][4], B[3][5];
    auto load = [&](int i) {
      int bf = i % 3;
#pragma unroll
      for (int n = 0; n < 5; ++n) B[bf][n] = __builtin_amdgcn_raw_buffer_load_b128(rB, vo + n * 1024, kof(i) * 5120, 0);
#pragma unroll
      for (int m = 0; m < 4; ++m) A[bf][m] = __builtin_amdgcn_raw_buffer_load_b128(rA, vo + m * 1024, kof(i) * 4096, 0);
    };
    const unsigned tl = lb + M_T1 + sw * 3840;
    {
      const int l40 = lane < 40 ? lane : 39;
      const int sl0 = kof(l40 >> 2) * 16 + 4 * (l40 & 3);
      __amdgpu_buffer_rsrc_t rG = mkrsrc(ws + WS_AGG, WS_ST - WS_AGG);
      f32x4 tv[2];
      u32x4 ag[15], al;
      tv[0] = __builtin_bit_cast(f32x4, __builtin_amdgcn_raw_buffer_load_b128(rG, sl0 * 8, WS_TAB - WS_AGG, 0));
      tv[1] = __builtin_bit_cast(f32x4, __builtin_amdgcn_raw_buffer_load_b128(rG, sl0 * 8 + 16, WS_TAB - WS_AGG, 0));
#pragma unroll
      for (int m = 0; m < 15; ++m)
        if (m < rc) ag[m] = __builtin_amdgcn_raw_buffer_load_b128(rG, sl0 * 4 + m * 2560, 0, 0);
      if (par) al = __builtin_amdgcn_raw_buffer_load_b128(rG, sl0 * 4, WS_AGL - WS_AGG + rc * 2560, 0);
      SB();
      load(0);
      load(1);
      SB();
      f32x2 P[4], gg[4];
#pragma unroll
      for (int k = 0; k < 4; ++k) { P[k] = cpow(tv[k >> 1][2 * (k & 1)], tv[k >> 1][2 * (k & 1) + 1], 128.f); gg[k] = (f32x2){0.f, 0.f}; }
      f32x2 L1[4], L64[4];
#pragma unroll
      for (int k = 0; k < 4; ++k) {
        L1[k] = cpow(tv[k >> 1][2 * (k & 1)], tv[k >> 1][2 * (k & 1) + 1], 1.f);
        L64[k] = cpow(tv[k >> 1][2 * (k & 1)], tv[k >> 1][2 * (k & 1) + 1], 64.f);
      }
      sts128f(tl + 2560 + l40 * 32, (f32x4){L1[0].x, L1[0].y, L1[1].x, L1[1].y});
      sts128f(tl + 2560 + l40 * 32 + 16, (f32x4){L1[2].x, L1[2].y, L1[3].x, L1[3].y});
#pragma unroll
      for (int m = 0; m < 15; ++m)
        if (m < rc) {
#pragma unroll
          for (int k = 0; k < 4; ++k) gg[k] = cfmap(P[k], gg[k], (f32x2){bflo(ag[m][k]), bfhi(ag[m][k])});
        }
      if (par) {
#pragma unroll
        for (int k = 0; k < 4; ++k) gg[k] = cfmap(L64[k], gg[k], (f32x2){bflo(al[k]), bfhi(al[k])});
      }
#pragma unroll
      for (int k = 0; k < 4; ++k)
        sts128f(tl + l40 * 64 + k * 16, (f32x4){tv[k >> 1][2 * (k & 1)], tv[k >> 1][2 * (k & 1) + 1], gg[k].x, gg[k].y});
    }
    TS(1);
#pragma unroll
    for (int m = 0; m < 4; ++m)
#pragma unroll
      for (int n = 0; n < 5; ++n) acc[m][n] = (f32x4){0.f, 0.f, 0.f, 0.f};
    const float nf = (float)(4 * c + 1);
    const unsigned tq = tl + q * 64, lq = tl + 2560 + q * 32;
    const unsigned tb = lb + M_RS + sw * 20480 + vo;
    u32x4 z[4];
    f32x4 tt[4], Lq[2];
    float zr[4], zi[4], e0, e1, e2, e3;
    auto rdt = [&](int i) {
#pragma unroll
      for (int d = 0; d < 4; ++d) tt[d] = lds128f(tq + i * 256 + d * 16);
    };
    auto rdl = [&](int i) { Lq[0] = lds128f(lq + i * 128); Lq[1] = lds128f(lq + i * 128 + 16); };
    auto opA = [&](int k) {
      const int m = k / 20 + 1, d = (k / 5) & 3, o = k % 5;
      const float Lx = Lq[d >> 1][2 * (d & 1)], Ly = Lq[d >> 1][2 * (d & 1) + 1];
      if (o == 0) e0 = vmul(Ly, zi[d]);
      else if (o == 1) e1 = vmul(Ly, zr[d]);
      else if (o == 2) zr[d] = vfms(Lx, zr[d], e0);
      else if (o == 3) zi[d] = vfma(Lx, zi[d], e1);
      else z[m][d] = pk2(zr[d], zi[d]);
    };
    auto opZ = [&](int k) {
      if (k >= 48) { z[0][k - 48] = pk2(zr[k - 48], zi[k - 48]); return; }
      const int d = k / 12, o = k % 12;
      if (o == 0) e0 = vmul(nf, tt[d].x);
      else if (o == 1) e1 = vmul(nf, tt[d].y);
      else if (o == 2) e0 = __builtin_amdgcn_exp2f(e0);
      else if (o == 3) e1 = __builtin_amdgcn_fractf(e1);
      else if (o == 4) e2 = __builtin_amdgcn_cosf(e1);
      else if (o == 5) e3 = __builtin_amdgcn_sinf(e1);
      else if (o == 6) e2 = vmul(e0, e2);
      else if (o == 7) e3 = vmul(e0, e3);
      else if (o == 8) e0 = vmul(e3, tt[d].w);
      else if (o == 9) e1 = vmul(e3, tt[d].z);
      else if (o == 10) zr[d] = vfms(e2, tt[d].z, e0);
      else zi[d] = vfma(e2, tt[d].w, e1);
    };
    rdt(0); rdl(0);
#pragma unroll
    for (int k = 0; k < 52; ++k) opZ(k);
    SB();
#pragma unroll
    for (int i = 0; i < 10; ++i) {
      if (i + 2 < 10) {
        if (SLP != SPRIO) __builtin_amdgcn_s_setprio(SLP);
        load(i + 2);
        SB();
        if (SLP != SPRIO) __builtin_amdgcn_s_setprio(SPRIO);
      }
      SB();
#pragma unroll
      for (int sl = 0; sl < 20; ++sl) {
        acc[sl / 5][sl % 5] = mfma(B[i % 3][sl % 5], A[i % 3][sl / 5], acc[sl / 5][sl % 5]);
        opA(3 * sl); opA(3 * sl + 1); opA(3 * sl + 2);
        if (sl == 12 && i < 9) rdt(i + 1);
        SB();
      }
#pragma unroll
      for (int sl = 0; sl < 20; ++sl) {
        acc[sl / 5][sl % 5] = mfma(B[i % 3][sl % 5], z[sl / 5], acc[sl / 5][sl % 5]);
        if (i < 9) {
          constexpr int st[21] = {0, 3, 5, 7, 10, 12, 15, 17, 19, 22, 24, 27, 29, 31, 34, 36, 39, 41, 43, 46, 52};
#pragma unroll
          for (int k = st[sl]; k < st[sl + 1]; ++k) opZ(k);
          if (sl == 12) rdl(i + 1);
        } else if (sl >= 2) {
          sts128f(tb + (sl - 2) * 1024, acc[(sl - 2) / 5][(sl - 2) % 5]);
        }
        SB();
      }
      TS(2 + i);
    }
    sts128f(tb + 18 * 1024, acc[3][3]);
    sts128f(tb + 19 * 1024, acc[3][4]);
    TS(14);
    BARRIER();
    TS(15);
    TSDUMP(33554432 + 1048576);
    return;
  }

  __builtin_amdgcn_s_setprio(OPRIO);
  __amdgpu_buffer_rsrc_t rA = mkrsrc(ws + WS_H1F + (size_t)rb * 48 * 4096, 48 * 4096);
  __amdgpu_buffer_rsrc_t rB = mkrsrc(ws + WS_W2F + (size_t)cg * 48 * 5120, 48 * 5120);
  u32x4 A[4][4], B[4][5];
  auto loadA = [&](int i) {
    int bf = i & 3, ks = w + 4 * ((i >> 1) + 6 * ((i & 1) ^ gh));
#pragma unroll
    for (int m = 0; m < 4; ++m) A[bf][m] = __builtin_amdgcn_raw_buffer_load_b128(rA, vo, ks * 4096 + ((w + m) & 3) * 1024, 0);
  };
  auto loadB = [&](int i) {
    int bf = i & 3, ks = w + 4 * ((i >> 1) + 6 * ((i & 1) ^ gh));
#pragma unroll
    for (int n = 0; n < 5; ++n) B[bf][n] = __builtin_amdgcn_raw_buffer_load_b128(rB, vo + n * 1024, ks * 5120, 0);
  };
  auto load = [&](int i) { loadA(i); loadB(i); };
  u32x2 xv[5], sk[5];
  f32x2 st;
  load(0);
  load(1);
  load(2);
  TS(1);
#pragma unroll
  for (int m = 0; m < 4; ++m)
#pragma unroll
    for (int n = 0; n < 5; ++n) acc[m][n] = (f32x4){0.f, 0.f, 0.f, 0.f};
  unsigned da[3];
#pragma unroll
  for (int i = 0; i < 12; ++i) {
    if (OTP != OPRIO && i == OTI) __builtin_amdgcn_s_setprio(OTP);
    if (i == 9) {
      int l2 = lane;
      asm volatile("" : "+v"(l2));
      const int trow = 64 * rb + 4 * (l2 & 15) + w;
      const int eo = trow * 640 + 80 * cg + 4 * (l2 >> 4);
      __amdgpu_buffer_rsrc_t rx = mkrsrc(x, 2621440), rd = mkrsrc(Dsk, 1280), rt = mkrsrc(ws + WS_ST, 16384);
      st = __builtin_bit_cast(f32x2, __builtin_amdgcn_raw_buffer_load_b64(rt, trow * 8, 0, 0));
#pragma unroll
      for (int n = 0; n < 5; ++n) {
        xv[n] = __builtin_amdgcn_raw_buffer_load_b64(rx, eo * 2 + n * 32, 0, 0);
        sk[n] = __builtin_amdgcn_raw_buffer_load_b64(rd, (l2 >> 4) * 8 + n * 32, 160 * cg, 0);
      }
      SB();
    }
    if (i + 3 < 12) {
      if (OLP != OPRIO && i >= OLI && (OTP == OPRIO || i < OTI)) __builtin_amdgcn_s_setprio(OLP);
      load(i + 3);
      SB();
      if (OLP != OPRIO && i >= OLI && (OTP == OPRIO || i < OTI)) __builtin_amdgcn_s_setprio(OPRIO);
    }
    SB();
    if (i < 11) {
#pragma unroll
      for (int m = 0; m < 4; ++m)
#pragma unroll
        for (int n = 0; n < 5; ++n) acc[m][n] = mfma(B[i & 3][n], A[i & 3][m], acc[m][n]);
    } else {
      int v2 = vo;
      asm volatile("" : "+v"(v2));
#pragma unroll
      for (int j = 1; j < 4; ++j) da[j - 1] = lb + M_RO + (((w + j) & 3) * 3 + (j - 1)) * 5120 + v2;
#pragma unroll
      for (int sl = 0; sl < 20; ++sl) {
        const int m = (sl / 5 + 1) & 3, n = sl % 5;
        acc[m][n] = mfma(B[i & 3][n], A[i & 3][m], acc[m][n]);
        if (sl >= 2 && sl < 17) sts128f(da[(sl - 2) / 5] + ((sl - 2) % 5) * 1024, acc[(sl - 2) / 5 + 1][(sl - 2) % 5]);
        SB();
      }
    }
    SB();
    TS(2 + i);
  }
  WAITL0();
  if (lane == 0) *(volatile AS3 unsigned*)(uintptr_t)(lb + M_CTL + w * 4) = 1u;
  {
#pragma unroll
    for (int n = 0; n < 5; ++n)
#pragma unroll
      for (int hf = 0; hf < 2; ++hf) {
        unsigned p = pk2a(vsub(bflo(xv[n][hf]), st.x), vsub(bfhi(xv[n][hf]), st.x));
        p = pk2a(vmul(bflo(p), st.y), vmul(bfhi(p), st.y));
        sk[n][hf] = pk2a(vmul(bflo(p), bflo(sk[n][hf])), vmul(bfhi(p), bfhi(sk[n][hf])));
      }
#pragma unroll
    for (int n = 0; n < 5; ++n) asm volatile("" : "+v"(sk[n]), "+v"(xv[n]));
  }
  for (;;) {
    u32x4 f = *(volatile AS3 u32x4*)(uintptr_t)(lb + M_CTL);
    if ((f.x & f.y & f.z & f.w) != 0u) break;
    __builtin_amdgcn_s_sleep(1);
  }
  asm volatile("" ::: "memory");
  TS(14);
  f32x4 xs[5], g2[5];
  {
    f32x4 po[5][3];
#pragma unroll
    for (int n = 0; n < 5; ++n)
#pragma unroll
      for (int j = 0; j < 3; ++j) po[n][j] = lds128f(lb + M_RO + ((w * 3 + j) * 5 + n) * 1024 + vo);
    SB();
#pragma unroll
    for (int n = 0; n < 5; ++n) {
      f32x4 o;
#pragma unroll
      for (int j = 0; j < 4; ++j) o[j] = vadd(vadd(acc[0][n][j], po[n][0][j]), vadd(po[n][1][j], po[n][2][j]));
#pragma unroll
      for (int hf = 0; hf < 2; ++hf) {
        unsigned p = pk2a(o[2 * hf], o[2 * hf + 1]);
        float v0 = bflo(p), v1 = bfhi(p);
        float e0 = __builtin_amdgcn_exp2f(vmul(v0, -1.44269504088896f)), e1 = __builtin_amdgcn_exp2f(vmul(v1, -1.44269504088896f));
        e0 = __builtin_amdgcn_rcpf(vadd(e0, 1.f)); e1 = __builtin_amdgcn_rcpf(vadd(e1, 1.f));
        p = pk2a(vmul(v0, e0), vmul(v1, e1));
        v0 = bflo(p); v1 = bfhi(p);
        xs[n][2 * hf] = vfma(v0, bflo(sk[n][hf]), bflo(xv[n][hf]));
        xs[n][2 * hf + 1] = vfma(v1, bfhi(sk[n][hf]), bfhi(xv[n][hf]));
        g2[n][2 * hf] = v0; g2[n][2 * hf + 1] = v1;
      }
    }
#pragma unroll
    for (int n = 0; n < 5; ++n) asm volatile("" : "+v"(xs[n]), "+v"(g2[n]));
  }
  __amdgpu_buffer_rsrc_t ro = mkrsrc(out, 5242880);
  int l2 = lane;
  asm volatile("" : "+v"(l2));
  const int eo2 = ((64 * rb + 4 * (l2 & 15) + w) * 640 + 80 * cg + 4 * (l2 >> 4)) * 4;
  TS(15);
  BARRIER_NW();
  TS(16);
  f32x4 ps[5][4];
#pragma unroll
  for (int n = 0; n < 5; ++n)
#pragma unroll
    for (int k = 0; k < 4; ++k) ps[n][k] = lds128f(lb + M_RS + (w * 5 + n) * 1024 + k * 20480 + vo);
  SB();
#pragma unroll
  for (int n = 0; n < 5; ++n) {
    f32x4 s = (ps[n][0] + ps[n][1]) + (ps[n][2] + ps[n][3]);
    f32x4 r = __builtin_elementwise_fma(s, g2[n], xs[n]);
    __builtin_amdgcn_raw_buffer_store_b128(__builtin_bit_cast(u32x4, r), ro, eo2 + n * 64, 0, 0);
    SB();
  }
}

extern "C" void kernel_launch(void* const* d_in, const int* in_sizes, int n_in, void* d_out, int out_size, void* d_ws,
                              size_t ws_size, hipStream_t stream) {
  const u16* x = (const u16*)d_in[0];
  k1<<<256, 512, 0, stream>>>(x, (const u16*)d_in[1], (const u16*)d_in[2], (const u16*)d_in[3], (const u16*)d_in[4],
                              (const u16*)d_in[5], (const u16*)d_in[6], (const u16*)d_in[7], (const u16*)d_in[10],
                              (const u16*)d_in[11], (char*)d_ws);
  k2<<<256, 512, 0, stream>>>(x, (const u16*)d_in[8], (char*)d_ws, (float*)d_out);
}
```

```cpp
#include <hip/hip_runtime.h>
#include <stdint.h>

typedef unsigned short u16;
typedef __attribute__((ext_vector_type(8))) __bf16 bf16x8;
typedef __attribute__((ext_vector_type(2))) __bf16 bf16x2;
typedef __attribute__((ext_vector_type(4))) float f32x4;
typedef __attribute__((ext_vector_type(2))) float f32x2;
typedef __attribute__((ext_vector_type(4))) unsigned u32x4;
typedef __attribute__((ext_vector_type(2))) unsigned u32x2;
typedef __attribute__((ext_vector_type(4))) short s16x4;
#define AS3 __attribute__((address_space(3)))
#define DEVI __device__ __forceinline__

constexpr size_t WS_H1F = 0;
constexpr size_t WS_HF = 6291456;
constexpr size_t WS_W2F = 11534336;
constexpr size_t WS_CF = 13500416;
constexpr size_t WS_AGG = 15138816;
constexpr size_t WS_AGL = 15220736;
constexpr size_t WS_TAB = 15302656;
constexpr size_t WS_ST = 15323136;

DEVI float bf2f(unsigned v) { return __uint_as_float(v << 16); }
DEVI float bflo(unsigned v) { return __uint_as_float(v << 16); }
DEVI float bfhi(unsigned v) { return __uint_as_float(v & 0xffff0000u); }
DEVI unsigned pk2(float a, float b) {
  bf16x2 r = {(__bf16)a, (__bf16)b};
  return __builtin_bit_cast(unsigned, r);
}
DEVI unsigned pk2a(float a, float b) { unsigned r; asm("v_cvt_pk_bf16_f32 %0, %1, %2" : "=v"(r) : "v"(a), "v"(b)); return r; }
DEVI float rbf(float a) { return bflo(pk2(a, 0.f)); }
template <int N> DEVI float shr(float v) {
  return __builtin_bit_cast(float, __builtin_amdgcn_update_dpp(0, __builtin_bit_cast(int, v), 0x110 + N, 0xf, 0xf, true));
}
template <int N> DEVI f32x2 shr2(f32x2 v) { f32x2 r; r.x = shr<N>(v.x); r.y = shr<N>(v.y); return r; }
DEVI f32x2 cpow(float a, float b, float n) {
  float mg = __builtin_amdgcn_exp2f(n * a);
  float rv = __builtin_amdgcn_fractf(n * b);
  f32x2 r;
  r.x = mg * __builtin_amdgcn_cosf(rv);
  r.y = mg * __builtin_amdgcn_sinf(rv);
  return r;
}
DEVI f32x2 cmul(f32x2 a, f32x2 b) { f32x2 r; r.x = a.x * b.x - a.y * b.y; r.y = a.x * b.y + a.y * b.x; return r; }
DEVI f32x2 pfma(f32x2 a, f32x2 b, f32x2 c) { return __builtin_elementwise_fma(a, b, c); }
DEVI f32x2 cfmap(f32x2 a, f32x2 b, f32x2 c) {
  f32x2 ax = {a.x, a.x}, ay = {-a.y, a.y}, bs = {b.y, b.x};
  return pfma(ay, bs, pfma(ax, b, c));
}
DEVI f32x2 cmulp(f32x2 a, f32x2 b) {
  f32x2 ax = {a.x, a.x}, ay = {-a.y, a.y}, bs = {b.y, b.x};
  return pfma(ay, bs, ax * b);
}
extern "C" __device__ __attribute__((const)) float mul_legacy_f32(float, float) __asm("llvm.amdgcn.fmul.legacy");
DEVI float vmul(float a, float b) { return mul_legacy_f32(a, b); }
DEVI float vfma(float a, float b, float c) { float r; asm("v_fma_f32 %0, %1, %2, %3" : "=v"(r) : "v"(a), "v"(b), "v"(c)); return r; }
DEVI float vadd(float a, float b) { float r; asm("v_add_f32 %0, %1, %2" : "=v"(r) : "v"(a), "v"(b)); return r; }
DEVI float vsub(float a, float b) { float r; asm("v_sub_f32 %0, %1, %2" : "=v"(r) : "v"(a), "v"(b)); return r; }
DEVI float vfms(float a, float b, float c) { float r; asm("v_fma_f32 %0, %1, %2, -%3" : "=v"(r) : "v"(a), "v"(b), "v"(c)); return r; }
DEVI float siluf(float a) { return a * __builtin_amdgcn_rcpf(1.f + __builtin_amdgcn_exp2f(-1.44269504088896f * a)); }
DEVI void swap16(unsigned& a, unsigned& b) { auto r = __builtin_amdgcn_permlane16_swap(a, b, false, false); a = r[0]; b = r[1]; }
DEVI void swap32(unsigned& a, unsigned& b) { auto r = __builtin_amdgcn_permlane32_swap(a, b, false, false); a = r[0]; b = r[1]; }
DEVI void swap16f(float& a, float& b) { unsigned x = __float_as_uint(a), y = __float_as_uint(b); swap16(x, y); a = __uint_as_float(x); b = __uint_as_float(y); }
DEVI void swap32f(float& a, float& b) { unsigned x = __float_as_uint(a), y = __float_as_uint(b); swap32(x, y); a = __uint_as_float(x); b = __uint_as_float(y); }

#define WAITV(n) asm volatile("s_waitcnt vmcnt(%0)" ::"n"(n) : "memory")
#define WAITL0() asm volatile("s_waitcnt lgkmcnt(0)" ::: "memory")
#define BARRIER() do { WAITL0(); __builtin_amdgcn_s_barrier(); asm volatile("" ::: "memory"); } while (0)
#define BARRIER_NW() do { asm volatile("" ::: "memory"); __builtin_amdgcn_s_barrier(); asm volatile("" ::: "memory"); } while (0)
#define SB() __builtin_amdgcn_sched_barrier(0)
DEVI void glds(const void* p, unsigned ldsaddr) {
  unsigned keep;
  asm volatile("s_mov_b32 %0, m0\n\ts_mov_b32 m0, %2\n\ts_nop 0\n\tglobal_load_lds_dwordx4 %1, off\n\ts_mov_b32 m0, %0"
               : "=&s"(keep) : "v"(p), "s"(ldsaddr) : "memory");
}

DEVI u32x4 lds128(unsigned off) { return *(const AS3 u32x4*)(uintptr_t)off; }
DEVI f32x4 lds128f(unsigned off) { return *(const AS3 f32x4*)(uintptr_t)off; }
DEVI f32x2 lds64f(unsigned off) { return *(const AS3 f32x2*)(uintptr_t)off; }
DEVI void sts128f(unsigned off, f32x4 v) { *(AS3 f32x4*)(uintptr_t)off = v; }
DEVI void sts64f(unsigned off, f32x2 v) { *(AS3 f32x2*)(uintptr_t)off = v; }
DEVI void sts32f(unsigned off, float v) { *(AS3 float*)(uintptr_t)off = v; }
DEVI u32x2 ldstr(unsigned off) {
  s16x4 r = __builtin_amdgcn_ds_read_tr16_b64_v4i16((AS3 s16x4*)(uintptr_t)off);
  return __builtin_bit_cast(u32x2, r);
}
DEVI u32x4 ldstr2(unsigned a, unsigned d) { u32x2 lo = ldstr(a), hi = ldstr(a + d); return (u32x4){lo.x, lo.y, hi.x, hi.y}; }
DEVI f32x4 mfma(u32x4 a, u32x4 b, f32x4 c) {
  return __builtin_amdgcn_mfma_f32_16x16x32_bf16(__builtin_bit_cast(bf16x8, a), __builtin_bit_cast(bf16x8, b), c, 0, 0, 0);
}
#define STAUX 16
#define SPRIO 2
#define OPRIO 0
#define OLP OPRIO
#define SLP SPRIO
#define OLI 0
#define OTP OPRIO
#define OTI 9
DEVI void st128(u32x4 v, __amdgpu_buffer_rsrc_t rs, int off) { __builtin_amdgcn_raw_buffer_store_b128(v, rs, off, 0, STAUX); }
DEVI __amdgpu_buffer_rsrc_t mkrsrc(const void* p, unsigned bytes) {
  return __builtin_amdgcn_make_buffer_rsrc((void*)p, 0, bytes, 0x00020000);
}
#define TSDECL
#define TS(k)
#define TSDUMP(base)

constexpr unsigned SLOT = 38912, OB = 16384, OW = 26624;
constexpr unsigned SPARE = 4 * SLOT;
constexpr unsigned T_TAB = SPARE;
constexpr unsigned T_AG = SPARE + 2560;
constexpr unsigned T_FLAG = SPARE + 3072;
constexpr unsigned T_PF = SPARE + 4096;
constexpr unsigned K1_LDS = SPARE + 5120;
constexpr unsigned S_W2 = 2 * SLOT;
constexpr unsigned E_ST = 3 * SLOT;
constexpr unsigned E_CS = E_ST + 2048;
constexpr unsigned E_A5 = E_CS + 2048;

DEVI void stat(u32x4 f, float& s, float& q) {
#pragma unroll
  for (int r = 0; r < 4; ++r) {
    float lo = bflo(f[r]), hi = bfhi(f[r]);
    s += lo; q = fmaf(lo, lo, q);
    s += hi; q = fmaf(hi, hi, q);
  }
}
template <int NM, int NV> DEVI void ileave() {
#pragma unroll
  for (int i = 0; i < NM; ++i) {
    __builtin_amdgcn_sched_group_barrier(0x008, 1, 0);
    __builtin_amdgcn_sched_group_barrier(0x002, NV, 0);
  }
}
struct Tab { f32x2 L, L4, L8, L16, L32, L64, co; };
DEVI void rdtab(Tab& t, unsigned a) {
  f32x4 v0 = lds128f(a), v1 = lds128f(a + 16), v2 = lds128f(a + 32);
  f32x2 v3 = lds64f(a + 48);
  t.L = (f32x2){v0.x, v0.y}; t.L4 = (f32x2){v0.z, v0.w}; t.L8 = (f32x2){v1.x, v1.y}; t.L16 = (f32x2){v1.z, v1.w};
  t.L32 = (f32x2){v2.x, v2.y}; t.L64 = (f32x2){v2.z, v2.w}; t.co = v3;
}

DEVI void dma_s(unsigned voff, const char* sbase, unsigned ldsaddr, int t, int lim) {
  asm volatile("s_cmp_lt_i32 %3, %4\n\ts_cbranch_scc0 .Ldma%=\n\ts_mov_b32 m0, %2\n\ts_nop 0\n\tglobal_load_lds_dwordx4 %0, %1\n.Ldma%=:"
               ::"v"(voff), "s"(sbase), "s"(ldsaddr), "s"(t), "n"(lim) : "memory", "scc");
}
DEVI void dma_v(const char* p, unsigned ldsaddr, int t, int lim) {
  asm volatile("s_cmp_lt_i32 %2, %3\n\ts_cbranch_scc0 .Ldmb%=\n\ts_mov_b32 m0, %1\n\ts_nop 0\n\tglobal_load_lds_dwordx4 %0, off\n.Ldmb%=:"
               ::"v"(p), "s"(ldsaddr), "s"(t), "n"(lim) : "memory", "scc");
}
DEVI void dma_s0(unsigned voff, const char* sbase, unsigned ldsaddr) {
  asm volatile("s_mov_b32 m0, %2\n\ts_nop 0\n\tglobal_load_lds_dwordx4 %0, %1" ::"v"(voff), "s"(sbase), "s"(ldsaddr) : "memory");
}
DEVI void dma_v0(const char* p, unsigned ldsaddr) {
  asm volatile("s_mov_b32 m0, %1\n\ts_nop 0\n\tglobal_load_lds_dwordx4 %0, off" ::"v"(p), "s"(ldsaddr) : "memory");
}
DEVI const char* code_base(unsigned& len, unsigned& off2) {
  uint64_t pc;
  unsigned d;
  asm volatile("s_getpc_b64 %0\n.Lpc%=:\n\ts_mov_b32 %1, .Lpc%=-k1\n\ts_mov_b32 %2, kend-k1\n\ts_mov_b32 %3, k2-k1"
               : "=s"(pc), "=s"(d), "=s"(len), "=s"(off2));
  return (const char*)(pc - d);
}
#define WAITV2(t, lim, A, B) asm volatile("s_cmp_lt_i32 %0, %1\n\ts_cbranch_scc1 .Lwv%=\n\ts_waitcnt vmcnt(%3)\n.Lwv%=:\n\ts_waitcnt vmcnt(%2)" ::"s"(t), "n"(lim), "n"(A), "n"(B) : "memory", "scc")
#define LGKM0() __builtin_amdgcn_s_waitcnt(0xc07f)

extern "C" __global__ __launch_bounds__(512, 2) void k1(const u16* x, const u16* Lre, const u16* Lim, const u16* Bre, const u16* Bim,
                                             const u16* Cre, const u16* Cim, const u16* ldt, const u16* w1, const u16* w2,
                                             char* ws) {
  __shared__ __attribute__((aligned(1024))) char smem[K1_LDS];
  const unsigned lb = (unsigned)(uintptr_t)(AS3 char*)smem;
  const int b = blockIdx.x, xcd = b & 7, jb = b >> 3;
  const int rc = (xcd >> 1) * 4 + (jb & 3), g = (xcd & 1) * 8 + (jb >> 2);
  const int tid = threadIdx.x, lane = tid & 63;
  const int w = __builtin_amdgcn_readfirstlane(tid >> 6);
  const int c = lane & 15, q = lane >> 4;
  const int l3 = lane >> 3, l7 = lane & 7;
  const int h = (w >> 1) & 1, a = w & 1;
  const int kk0 = h ^ a;
  TSDECL;
  TS(0);

  const u32x4 ONES = {0x3f803f80u, 0x3f803f80u, 0x3f803f80u, 0x3f803f80u};
  unsigned xo[2];
#pragma unroll
  for (int p = 0; p < 2; ++p) xo[p] = lb + 8192 * h + c * 128 + (((4 * (kk0 ^ p) + q) ^ (c & 7)) << 4);
  float sv[4] = {0.f, 0.f, 0.f, 0.f};
  float t0, t1;
  const int rb = 2 * rc + h;
  unsigned pr[3];
  auto prload = [&]() {
    int p = 40 * g + (lane < 40 ? lane : 0);
    const u16* a0 = Lre + p; const u16* a1 = Lim + p; const u16* a2 = ldt + p;
    asm volatile("global_load_ushort %0, %1, off" : "=v"(pr[0]) : "v"(a0) : "memory");
    asm volatile("global_load_ushort %0, %1, off" : "=v"(pr[1]) : "v"(a1) : "memory");
    asm volatile("global_load_ushort %0, %1, off" : "=v"(pr[2]) : "v"(a2) : "memory");
  };
  float lr, li, ea, eb;
  auto tab0 = [&]() {
    asm volatile("" : "+v"(pr[0]), "+v"(pr[1]), "+v"(pr[2]));
    lr = bf2f(pr[0]); li = bf2f(pr[1]);
    float dt = rbf(expf(bf2f(pr[2])));
    ea = dt * lr * 1.44269504088896f; eb = dt * li * 0.15915494309189535f;
  };
  auto tab1 = [&]() {
    const float n = (w == 0 || w > 5) ? 1.f : (float)(2 << w);
    f32x2 P = cpow(ea, eb, n);
    if (w == 6) {
      float nr = P.x - 1.f, ni = P.y, dd = 2.f / (lr * lr + li * li);
      P = (f32x2){(nr * lr + ni * li) * dd, (ni * lr - nr * li) * dd};
    }
    if (w == 7) {
      P = (f32x2){ea, eb};
      if (lane >= 62) sts32f(lb + T_FLAG + (lane - 62) * 4, 0.f);
    }
    if (lane < 40) sts64f(lb + T_TAB + lane * 64 + w * 8, P);
  };
  auto opS = [&](const u32x4& f, float& s, float& qq, int k) {
    const int r = k / 6, o = k % 6;
    if (o == 0) t0 = bflo(f[r]);
    else if (o == 1) t1 = bfhi(f[r]);
    else if (o == 2) s = vadd(s, t0);
    else if (o == 3) qq = vfma(t0, t0, qq);
    else if (o == 4) s = vadd(s, t1);
    else qq = vfma(t1, t1, qq);
  };

  auto mr4 = [&](float* m, float* r) {
#pragma unroll
    for (int i = 0; i < 4; ++i) {
      f32x4 v = lds128f(lb + E_ST + (64 * h + 4 * c + i) * 16);
      float mean = (v.x + v.z) * (1.f / 640.f);
      float var = fmaxf((v.y + v.w) * (1.f / 640.f) - mean * mean, 0.f);
      float den = rbf(__builtin_amdgcn_sqrtf(rbf(rbf(var) + 1e-5f)));
      m[i] = rbf(mean); r[i] = __builtin_amdgcn_rcpf(den);
    }
  };
  auto put_stats = [&](int i0) {
    swap32f(sv[0], sv[2]); swap32f(sv[1], sv[3]);
    float w0 = sv[0] + sv[2], w1v = sv[1] + sv[3];
    swap16f(w0, w1v);
    sts32f(lb + E_ST + ((64 * h + 4 * c + i0 + (q >> 1)) * 4 + 2 * a + (q & 1)) * 4, w0 + w1v);
  };

  if (w < 4) {
    unsigned vo[4];
#pragma unroll
    for (int v = 0; v < 4; ++v) {
      int rho = 8 * (w + 4 * v) + l3;
      vo[v] = (64 * (rho >> 6) + 4 * (rho & 15) + ((rho >> 4) & 3)) * 1280 + ((l7 ^ l3) << 4);
    }
    const char* sb0 = (const char*)x + rc * 163840;
    const char* sb1 = sb0;
    unsigned dst = lb + w * 1024, dstep = 4096;
    auto issue0 = [&](int st) {
#pragma unroll
      for (int v = 0; v < 4; ++v) dma_s0(vo[v], sb0 + st * 128, dst + st * SLOT + v * 4096);
    };
    issue0(0);
    prload();
    BARRIER_NW();
    issue0(1);
    BARRIER_NW();
    issue0(2);
    {
      unsigned len, off2;
      const char* cb = code_base(len, off2);
      int lm = ((int)len - 1024) & ~15;
      lm = lm > 0 ? lm : 0;
      int po = (4 * jb + w) * 1024;
      po = (po < lm ? po : lm) + lane * 16;
      dma_s0(po, cb, lb + T_PF);
    }
    unsigned wo[2][3];
#pragma unroll
    for (int u = 0; u < 3; ++u) {
      int tl = a ? (u == 2 ? 3 : 4 + u) : u;
      wo[0][u] = lb + OW + (8 * q + (c >> 2)) * 192 + (c & 3) * 8 + ((tl ^ (q & 1)) << 5) + kk0 * 6144;
      wo[1][u] = wo[0][u] + 6144 - 2 * kk0 * 6144;
    }
    f32x4 acc[4][3], cs[3];
#pragma unroll
    for (int u = 0; u < 3; ++u) {
      cs[u] = (f32x4){0.f, 0.f, 0.f, 0.f};
#pragma unroll
      for (int i = 0; i < 4; ++i) acc[i][u] = cs[u];
    }
    u32x4 X[2][2][4], W[2][2][3];
    auto rd = [&](int r, int k) {
      const int p = k / 10, o = k % 10;
      if (o < 6) {
        u32x2 v = ldstr(wo[p][o >> 1] + (o & 1) * 768);
        W[r][p][o >> 1][2 * (o & 1)] = v.x; W[r][p][o >> 1][2 * (o & 1) + 1] = v.y;
      } else X[r][p][o - 6] = lds128(xo[p] + 2048 * (o - 6));
    };
    auto adv = [&](unsigned d) {
#pragma unroll
      for (int p = 0; p < 2; ++p) {
        xo[p] += d;
#pragma unroll
        for (int u = 0; u < 3; ++u) wo[p][u] += d;
      }
    };
    int unit = 2 * jb + (w & 1);
    if (w < 2) { if (unit >= 60) unit -= 4; } else { if (unit >= 50) unit -= 14; }
    const int u5 = unit / 5;
    const int uks = (w < 2 ? 12 : 10) * (xcd >> 1) + u5, udb = 5 * (xcd & 1) + (unit - 5 * u5);

    WAITV(9);
    BARRIER_NW();
    TS(1);
#pragma unroll
    for (int k = 0; k < 20; ++k) rd(0, k);
    adv(SLOT);
    SB();
    dma_s0(vo[0], sb0 + 384, dst + 3 * SLOT);
    tab0(); SB();
    dma_s0(vo[1], sb0 + 384, dst + 3 * SLOT + 4096);
    tab1(); SB();
    dma_s0(vo[2], sb0 + 384, dst + 3 * SLOT + 8192);
    dma_s0(vo[3], sb0 + 384, dst + 3 * SLOT + 12288);
    sb0 += 512; sb1 = sb0;
    LGKM0();
    WAITV(9);
    BARRIER_NW();
    TS(2);
    auto stage = [&](int r, int t) {
#pragma unroll
      for (int sl = 0; sl < 27; ++sl) {
        if (sl < 24) {
          const int p = sl / 12, i = (sl % 12) / 3, u = sl % 3;
          acc[i][u] = mfma(W[r][p][u], X[r][p][i], acc[i][u]);
        } else cs[sl - 24] = mfma(W[r][1][sl - 24], ONES, cs[sl - 24]);
        if (sl < 20) rd(r ^ 1, sl);
        if (sl % 6 == 2 && sl < 24) dma_s(vo[sl / 6], sl < 12 ? sb0 : sb1, dst + (sl / 6) * dstep, t, 7);
        if (sl < 12) { opS(X[r][0][0], sv[0], sv[1], 2 * sl); opS(X[r][0][0], sv[0], sv[1], 2 * sl + 1); }
        else if (sl < 24) { opS(X[r][1][1], sv[2], sv[3], 2 * sl - 24); opS(X[r][1][1], sv[2], sv[3], 2 * sl - 23); }
        SB();
      }
    };
    for (int t = 0; t < 10; t += 2) {
      if (t == 6) {
        dst = lb + S_W2 + w * 4096; dstep = 1024;
        if (w < 2) {
          sb0 = (const char*)w2 + uks * 40960 + udb * 128; sb1 = sb0;
#pragma unroll
          for (int v = 0; v < 4; ++v) {
            int r = 8 * v + l3;
            vo[v] = r * 1280 + ((l7 ^ (((r >> 1) & 3) << 1)) << 4);
          }
        } else {
          int G = uks / 5, kr = uks - 5 * G;
          int p0 = 80 * G + (kr < 4 ? 40 * (kr >> 1) + 16 * (kr & 1) : 32);
          sb0 = (const char*)Cre + udb * 81920 + p0 * 2; sb1 = (const char*)Cim + udb * 81920 + p0 * 2;
          vo[0] = lane * 1280; vo[1] = vo[0] + (kr < 4 ? 16 : 80); vo[2] = vo[0]; vo[3] = vo[1];
        }
      }
      stage(0, t);
      adv((t & 2) ? -3 * SLOT : SLOT);
      dst += SLOT; sb0 += 128; sb1 += 128;
      LGKM0();
      WAITV2(t, 8, 8, 0);
      BARRIER_NW();
      TS(3 + t);
      stage(1, t + 1);
      adv(SLOT);
      dst += (t & 2) ? -3 * SLOT : SLOT; sb0 += 128; sb1 += 128;
      LGKM0();
      WAITV2(t, 6, 8, 4);
      BARRIER_NW();
      TS(4 + t);
    }
    put_stats(0);
    if (c == 0) {
#pragma unroll
      for (int u = 0; u < 3; ++u) sts128f(lb + E_CS + ((w * 3 + u) * 4 + q) * 16, cs[u]);
    }
    BARRIER();
    TS(13);
    float m[4], r[4];
    mr4(m, r);
#pragma unroll
    for (int u = 0; u < 3; ++u) cs[u] += lds128f(lb + E_CS + (((w ^ 2) * 3 + u) * 4 + q) * 16);
    if (g == 0 && a == 0 && q == 0) {
      f32x4* st = (f32x4*)(ws + WS_ST + (64 * rb + 4 * c) * 8);
      st[0] = (f32x4){m[0], r[0], m[1], r[1]};
      st[1] = (f32x4){m[2], r[2], m[3], r[3]};
    }
    __amdgpu_buffer_rsrc_t rs = mkrsrc(ws + WS_H1F, 6291456);
    unsigned o[4][6];
#pragma unroll
    for (int i = 0; i < 4; ++i) {
      f32x2 nm = {-m[i], -m[i]}, rr = {r[i], r[i]}, re = {-1.44269504088896f * r[i], -1.44269504088896f * r[i]};
#pragma unroll
      for (int u = 0; u < 3; ++u)
#pragma unroll
        for (int hf = 0; hf < 2; ++hf) {
          f32x2 v = pfma((f32x2){cs[u][2 * hf], cs[u][2 * hf + 1]}, nm, (f32x2){acc[i][u][2 * hf], acc[i][u][2 * hf + 1]});
          f32x2 e = v * re;
          e.x = __builtin_amdgcn_exp2f(e.x); e.y = __builtin_amdgcn_exp2f(e.y);
          e = e + (f32x2){1.f, 1.f};
          e.x = __builtin_amdgcn_rcpf(e.x); e.y = __builtin_amdgcn_rcpf(e.y);
          v = (v * rr) * e;
          o[i][2 * u + hf] = pk2(v.x, v.y);
        }
      st128((u32x4){o[i][0], o[i][1], o[i][2], o[i][3]}, rs, lane * 16 + ((rb * 48 + 3 * g + 2 * a) * 4 + i) * 1024);
      if (i & 1) {
        swap16(o[i - 1][4], o[i][4]); swap16(o[i - 1][5], o[i][5]);
        st128((u32x4){o[i - 1][4], o[i - 1][5], o[i][4], o[i][5]}, rs,
              (c + 16 * (2 * a + (q >> 1))) * 16 + ((rb * 48 + 3 * g + 1) * 4 + (i - 1) + (q & 1)) * 1024);
      }
    }
    TS(14);
    SB();
    if (w < 2) {
      const bool nat = (uks % 3) == 1;
      int r0 = (nat ? 8 * q : 4 * q) + (c >> 2);
      int dr = nat ? 4 : 16;
      unsigned a0 = lb + S_W2 + w * 4096 + r0 * 128 + (c & 3) * 8;
      int f0 = (r0 >> 1) & 3, f1 = ((r0 + dr) >> 1) & 3;
#pragma unroll
      for (int j = 0; j < 4; ++j) {
        u32x2 lo = ldstr(a0 + ((j ^ f0) << 5));
        u32x2 hi = ldstr(a0 + dr * 128 + ((j ^ f1) << 5));
        int Dt = 4 * udb + j, cg = Dt / 5, nt = Dt - 5 * cg;
        *(u32x4*)(ws + WS_W2F + (size_t)(((cg * 48 + uks) * 5 + nt) * 1024) + lane * 16) = (u32x4){lo.x, lo.y, hi.x, hi.y};
      }
      if (rc == 0 && w == 0 && lane < 40) {
        int sg = lane < 32 ? (5 * (g >> 1) + 2 * (g & 1) + (lane >> 4)) * 16 + 4 * ((lane >> 1) & 3) + 2 * ((lane >> 3) & 1) + (lane & 1)
                           : (5 * (g >> 1) + 4) * 16 + 8 * (g & 1) + 4 * (lane & 1) + ((lane - 32) >> 1);
        *(f32x2*)(ws + WS_TAB + sg * 8) = lds64f(lb + T_TAB + lane * 64 + 56);
      }
    } else {
      u32x4 sj[4];
#pragma unroll
      for (int v = 0; v < 4; ++v) sj[v] = lds128(lb + S_W2 + w * 4096 + v * 1024 + lane * 16);
      unsigned P[2][4][2];
#pragma unroll
      for (int ga = 0; ga < 2; ++ga)
#pragma unroll
        for (int j = 0; j < 4; ++j) {
          unsigned im = sj[2 + ga][j] ^ 0x80008000u;
          P[ga][j][0] = __builtin_amdgcn_perm(im, sj[ga][j], 0x05040100u);
          P[ga][j][1] = __builtin_amdgcn_perm(im, sj[ga][j], 0x07060302u);
        }
      int Dt = 4 * udb + q, cg = Dt / 5, nt = Dt - 5 * cg;
      char* dd = ws + WS_CF + (size_t)(((cg * 40 + uks) * 5 + nt) * 1024) + c * 16;
      if (uks % 5 < 4) {
#pragma unroll
        for (int j = 0; j < 4; ++j) *(u32x4*)(dd + j * 256) = (u32x4){P[0][j][0], P[0][j][1], P[1][j][0], P[1][j][1]};
      } else {
#pragma unroll
        for (int j = 0; j < 4; ++j)
          *(u32x4*)(dd + j * 256) = (u32x4){P[j >> 1][0][j & 1], P[j >> 1][1][j & 1], P[j >> 1][2][j & 1], P[j >> 1][3][j & 1]};
      }
    }
    TS(15);
  } else {
    const int wb = w - 4;
    const char* bp[3];
    unsigned vo[3];
    const int e2 = wb < 2 ? 8 + wb : 6 + wb;
#pragma unroll
    for (int v = 0; v < 3; ++v) {
      int n = 8 * (v < 2 ? wb + 4 * v : e2) + l3;
      bp[v] = (const char*)((n & 1) ? Bim : Bre) + (40 * g + (n >> 1)) * 1280 + ((l7 ^ l3) << 4);
      int idx = 64 * (wb + 4 * v) + lane, k = idx / 12, uu = idx - 12 * k;
      vo[v] = k * 3072 + (((uu >> 1) ^ ((k >> 3) & 1)) << 5) + ((uu & 1) << 4);
    }
    const char* sb = (const char*)w1 + 192 * g;
    unsigned dst = lb + OB + wb * 1024;
    const unsigned d2 = (e2 - wb) * 1024;
    auto piece0 = [&](int st, int j) {
      const int v = j >> 1;
      if (j & 1) dma_s0(vo[v], sb + st * 196608, dst + st * SLOT + (OW - OB) + v * 4096);
      else { dma_v0(bp[v], dst + st * SLOT + (v < 2 ? v * 4096 : d2)); bp[v] += 128; }
    };
    auto issue0 = [&](int st) {
#pragma unroll
      for (int j = 0; j < 6; ++j) piece0(st, j);
    };
    issue0(0);
    prload();
    BARRIER_NW();
    issue0(1);
    BARRIER_NW();
    issue0(2);
    unsigned bo[2];
#pragma unroll
    for (int p = 0; p < 2; ++p) bo[p] = xo[p] - 8192 * h + OB + 4096 * a;
    const int r5 = c ^ (2 * a);
    unsigned f5 = lb + OB + 8192 + r5 * 128 + (((4 * kk0 + q) ^ (r5 & 7)) << 4);
    f32x4 acc[4][2], ac5[5], cs[2];
#pragma unroll
    for (int i = 0; i < 4; ++i) { acc[i][0] = (f32x4){0.f, 0.f, 0.f, 0.f}; acc[i][1] = acc[i][0]; ac5[i] = acc[i][0]; }
    ac5[4] = ac5[0]; cs[0] = ac5[0]; cs[1] = ac5[0];
    u32x4 X[2][2][4], W[2][2][2], W5[2];
    auto rd = [&](int r, int k) {
      if (k == 6) { W5[r] = lds128(f5); return; }
      const int p = k / 7, o = k % 7;
      if (o < 2) W[r][p][o] = lds128(bo[p] + 2048 * o);
      else X[r][p][o - 2] = lds128(xo[p] + 2048 * (o - 2));
    };
    auto adv = [&](unsigned d) { xo[0] += d; xo[1] += d; bo[0] += d; bo[1] += d; f5 += d; };
    WAITV(12);
    BARRIER_NW();
    TS(1);
#pragma unroll
    for (int k = 0; k < 13; ++k) rd(0, k);
    adv(SLOT);
    SB();
    piece0(3, 0); piece0(3, 1);
    tab0(); SB();
    piece0(3, 2); piece0(3, 3);
    tab1(); SB();
    piece0(3, 4); piece0(3, 5);
    sb += 4 * 196608;
    LGKM0();
    WAITV(12);
    BARRIER_NW();
    TS(2);
    auto stage = [&](int r, int t) {
#pragma unroll
      for (int sl = 0; sl < 23; ++sl) {
        if (sl < 8) acc[sl >> 1][sl & 1] = mfma(W[r][0][sl & 1], X[r][0][sl >> 1], acc[sl >> 1][sl & 1]);
        else if (sl < 12) ac5[sl - 8] = mfma(W5[r], X[r][0][sl - 8], ac5[sl - 8]);
        else if (sl == 12) ac5[4] = mfma(W5[r], ONES, ac5[4]);
        else if (sl < 21) acc[(sl - 13) >> 1][(sl - 13) & 1] = mfma(W[r][1][(sl - 13) & 1], X[r][1][(sl - 13) >> 1], acc[(sl - 13) >> 1][(sl - 13) & 1]);
        else cs[sl - 21] = mfma(W[r][1][sl - 21], ONES, cs[sl - 21]);
        if (sl < 13) rd(r ^ 1, sl);
        if (sl % 4 == 1 && sl < 23) {
          const int j = sl / 4, v = j >> 1;
          if (j & 1) dma_s(vo[v], sb, dst + (OW - OB) + v * 4096, t, 6);
          else { dma_v(bp[v], dst + (v < 2 ? v * 4096 : d2), t, 6); bp[v] += 128; }
        }
        constexpr int st[24] = {0, 2, 4, 6, 8, 10, 12, 14, 16, 18, 20, 22, 24, 26, 29, 31, 34, 36, 39, 41, 44, 46, 48, 48};
#pragma unroll
        for (int k = st[sl]; k < st[sl + 1]; ++k) {
          if (k < 24) opS(X[r][0][2], sv[0], sv[1], k); else opS(X[r][1][3], sv[2], sv[3], k - 24);
        }
        SB();
      }
    };
    for (int t = 0; t < 10; t += 2) {
      stage(0, t);
      adv((t & 2) ? -3 * SLOT : SLOT);
      dst += SLOT; sb += 196608;
      LGKM0();
      WAITV2(t, 6, 12, 6);
      BARRIER_NW();
      TS(3 + t);
      stage(1, t + 1);
      adv(SLOT);
      dst += (t & 2) ? -3 * SLOT : SLOT; sb += 196608;
      LGKM0();
      WAITV2(t, 6, 12, 0);
      BARRIER_NW();
      TS(4 + t);
    }
    Tab tb[5];
#pragma unroll
    for (int k = 0; k < 4; ++k) rdtab(tb[k], lb + T_TAB + (16 * a + 8 * (k >> 1) + 2 * q + (k & 1)) * 64);
    rdtab(tb[4], lb + T_TAB + (32 + 2 * q + a) * 64);
    put_stats(2);
    if (c == 0) {
      sts128f(lb + E_CS + ((w * 3 + 0) * 4 + q) * 16, cs[0]);
      sts128f(lb + E_CS + ((w * 3 + 1) * 4 + q) * 16, cs[1]);
    }
#pragma unroll
    for (int i = 0; i < 5; ++i) sts64f(lb + E_A5 + (((w - 4) * 5 + i) * 64 + lane) * 8, (f32x2){ac5[i].z, ac5[i].w});
    BARRIER();
    TS(13);
    float m[4], r[4];
    mr4(m, r);
    cs[0] += lds128f(lb + E_CS + (((w ^ 2) * 3 + 0) * 4 + q) * 16);
    cs[1] += lds128f(lb + E_CS + (((w ^ 2) * 3 + 1) * 4 + q) * 16);
    f32x2 u[5][4], csv[5];
#pragma unroll
    for (int i = 0; i < 4; ++i) {
      u[0][i] = (f32x2){acc[i][0].x, acc[i][0].y}; u[1][i] = (f32x2){acc[i][0].z, acc[i][0].w};
      u[2][i] = (f32x2){acc[i][1].x, acc[i][1].y}; u[3][i] = (f32x2){acc[i][1].z, acc[i][1].w};
      u[4][i] = (f32x2){ac5[i].x, ac5[i].y} + lds64f(lb + E_A5 + ((((w - 4) ^ 1) * 5 + i) * 64 + lane) * 8);
    }
    csv[0] = (f32x2){cs[0].x, cs[0].y}; csv[1] = (f32x2){cs[0].z, cs[0].w};
    csv[2] = (f32x2){cs[1].x, cs[1].y}; csv[3] = (f32x2){cs[1].z, cs[1].w};
    csv[4] = (f32x2){ac5[4].x, ac5[4].y} + lds64f(lb + E_A5 + ((((w - 4) ^ 1) * 5 + 4) * 64 + lane) * 8);
#pragma unroll
    for (int i = 0; i < 4; ++i) {
      f32x2 nm = {-m[i], -m[i]}, rr = {r[i], r[i]};
#pragma unroll
      for (int k = 0; k < 5; ++k) u[k][i] = cmulp(tb[k].co, pfma(csv[k], nm, u[k][i]) * rr);
    }
    f32x2 hh[5];
#pragma unroll
    for (int k = 0; k < 5; ++k) hh[k] = u[k][0];
#pragma unroll
    for (int i = 1; i < 4; ++i)
#pragma unroll
      for (int k = 0; k < 5; ++k) hh[k] = cfmap(tb[k].L, hh[k], u[k][i]);
#pragma unroll
    for (int k = 0; k < 5; ++k) hh[k] = cfmap(tb[k].L4, shr2<1>(hh[k]), hh[k]);
#pragma unroll
    for (int k = 0; k < 5; ++k) hh[k] = cfmap(tb[k].L8, shr2<2>(hh[k]), hh[k]);
#pragma unroll
    for (int k = 0; k < 5; ++k) hh[k] = cfmap(tb[k].L16, shr2<4>(hh[k]), hh[k]);
#pragma unroll
    for (int k = 0; k < 5; ++k) hh[k] = cfmap(tb[k].L32, shr2<8>(hh[k]), hh[k]);
    const int ksr = 5 * (g >> 1) + 2 * (g & 1) + a, ks5 = 5 * (g >> 1) + 4;
    f32x2 ag[5];
#pragma unroll
    for (int k = 0; k < 5; ++k) ag[k] = hh[k];
    if (h == 0) {
      if (c == 15) {
        unsigned ta = lb + T_AG + (a * 4 + q) * 48;
        sts128f(ta, (f32x4){ag[0].x, ag[0].y, ag[1].x, ag[1].y});
        sts128f(ta + 16, (f32x4){ag[2].x, ag[2].y, ag[3].x, ag[3].y});
        sts64f(ta + 32, ag[4]);
      }
      WAITL0();
      if (lane == 0) *(volatile AS3 unsigned*)(uintptr_t)(lb + T_FLAG + a * 4) = 1u;
    }
#pragma unroll
    for (int k = 0; k < 5; ++k) hh[k] = shr2<1>(hh[k]);
    __amdgpu_buffer_rsrc_t rs = mkrsrc(ws + WS_HF, 5242880);
    unsigned o5[4];
#pragma unroll
    for (int i = 0; i < 4; ++i) {
      u32x4 o;
#pragma unroll
      for (int k = 0; k < 5; ++k) {
        hh[k] = cfmap(tb[k].L, hh[k], u[k][i]);
        if (k < 4) o[k] = pk2(hh[k].x, hh[k].y); else o5[i] = pk2(hh[k].x, hh[k].y);
      }
      st128(o, rs, lane * 16 + ((rb * 40 + ksr) * 4 + i) * 1024);
    }
    swap32(o5[0], o5[2]); swap32(o5[1], o5[3]);
    swap16(o5[0], o5[1]); swap16(o5[2], o5[3]);
    st128((u32x4){o5[0], o5[1], o5[2], o5[3]}, rs, (c + 16 * (2 * (g & 1) + a)) * 16 + ((rb * 40 + ks5) * 4 + q) * 1024);
    if (h == 1) {
      while (*(volatile AS3 unsigned*)(uintptr_t)(lb + T_FLAG + a * 4) == 0u) __builtin_amdgcn_s_sleep(1);
      asm volatile("" ::: "memory");
      unsigned ta = lb + T_AG + (a * 4 + q) * 48;
      f32x4 l0 = lds128f(ta), l1 = lds128f(ta + 16);
      f32x2 l2 = lds64f(ta + 32);
      ag[0] = cfmap(tb[0].L64, (f32x2){l0.x, l0.y}, ag[0]); ag[1] = cfmap(tb[1].L64, (f32x2){l0.z, l0.w}, ag[1]);
      ag[2] = cfmap(tb[2].L64, (f32x2){l1.x, l1.y}, ag[2]); ag[3] = cfmap(tb[3].L64, (f32x2){l1.z, l1.w}, ag[3]);
      ag[4] = cfmap(tb[4].L64, l2, ag[4]);
    }
    if (c == 15) {
      char* base = ws + (h ? WS_AGG : WS_AGL) + rc * 2560;
      *(u32x4*)(base + (ksr * 16 + 4 * q) * 4) = (u32x4){pk2(ag[0].x, ag[0].y), pk2(ag[1].x, ag[1].y), pk2(ag[2].x, ag[2].y), pk2(ag[3].x, ag[3].y)};
      *(unsigned*)(base + (ks5 * 16 + 8 * (g & 1) + 4 * a + q) * 4) = pk2(ag[4].x, ag[4].y);
    }
    TS(15);
  }
}

constexpr unsigned M_RO = 0;
constexpr unsigned M_RS = 61440;
constexpr unsigned M_T1 = M_RS + 81920;
constexpr unsigned M_CTL = M_T1 + 15360;
constexpr unsigned K2_LDS = M_CTL + 64;

extern "C" __global__ __launch_bounds__(512, 2) void k2(const u16* x, const u16* Dsk, char* ws, float* out) {
  __shared__ __attribute__((aligned(1024))) char smem[K2_LDS];
  const unsigned lb = (unsigned)(uintptr_t)(AS3 char*)smem;
  const int tid = threadIdx.x, lane = tid & 63;
  const int w = __builtin_amdgcn_readfirstlane(tid >> 6);
  const int c = lane & 15, q = lane >> 4;
  const int b = blockIdx.x, xcd = b & 7, jb = b >> 3;
  const int rb = (xcd >> 1) * 8 + (jb & 7), cg = (xcd & 1) * 4 + (jb >> 3), gh = xcd & 1;
  const int rc = rb >> 1, par = rb & 1;
  const int vo = lane * 16;
  TSDECL;
  TS(0);
  f32x4 acc[4][5];
  if (tid < 4) sts32f(lb + M_CTL + tid * 4, 0.f);
  BARRIER();

  if (w >= 4) {
    const int sw = w - 4;
    __builtin_amdgcn_s_setprio(SPRIO);
    __amdgpu_buffer_rsrc_t rA = mkrsrc(ws + WS_HF + (size_t)(rb * 40) * 4096, 40 * 4096);
    __amdgpu_buffer_rsrc_t rB = mkrsrc(ws + WS_CF + (size_t)(cg * 40) * 5120, 40 * 5120);
    auto kof = [&](int i) { return 5 * sw + (i >> 1) + 20 * ((i & 1) ^ gh); };
    u32x4 A[3][4], B[3][5];
    auto load = [&](int i) {
      int bf = i % 3;
#pragma unroll
      for (int n = 0; n < 5; ++n) B[bf][n] = __builtin_amdgcn_raw_buffer_load_b128(rB, vo + n * 1024, kof(i) * 5120, 0);
#pragma unroll
      for (int m = 0; m < 4; ++m) A[bf][m] = __builtin_amdgcn_raw_buffer_load_b128(rA, vo + m * 1024, kof(i) * 4096, 0);
    };
    const unsigned tl = lb + M_T1 + sw * 3840;
    {
      const int l40 = lane < 40 ? lane : 39;
      const int sl0 = kof(l40 >> 2) * 16 + 4 * (l40 & 3);
      __amdgpu_buffer_rsrc_t rG = mkrsrc(ws + WS_AGG, WS_ST - WS_AGG);
      f32x4 tv[2];
      u32x4 ag[15], al;
      tv[0] = __builtin_bit_cast(f32x4, __builtin_amdgcn_raw_buffer_load_b128(rG, sl0 * 8, WS_TAB - WS_AGG, 0));
      tv[1] = __builtin_bit_cast(f32x4, __builtin_amdgcn_raw_buffer_load_b128(rG, sl0 * 8 + 16, WS_TAB - WS_AGG, 0));
#pragma unroll
      for (int m = 0; m < 15; ++m)
        if (m < rc) ag[m] = __builtin_amdgcn_raw_buffer_load_b128(rG, sl0 * 4 + m * 2560, 0, 0);
      if (par) al = __builtin_amdgcn_raw_buffer_load_b128(rG, sl0 * 4, WS_AGL - WS_AGG + rc * 2560, 0);
      SB();
      load(0);
      load(1);
      SB();
      f32x2 P[4], gg[4];
#pragma unroll
      for (int k = 0; k < 4; ++k) { P[k] = cpow(tv[k >> 1][2 * (k & 1)], tv[k >> 1][2 * (k & 1) + 1], 128.f); gg[k] = (f32x2){0.f, 0.f}; }
      f32x2 L1[4], L64[4];
#pragma unroll
      for (int k = 0; k < 4; ++k) {
        L1[k] = cpow(tv[k >> 1][2 * (k & 1)], tv[k >> 1][2 * (k & 1) + 1], 1.f);
        L64[k] = cpow(tv[k >> 1][2 * (k & 1)], tv[k >> 1][2 * (k & 1) + 1], 64.f);
      }
      sts128f(tl + 2560 + l40 * 32, (f32x4){L1[0].x, L1[0].y, L1[1].x, L1[1].y});
      sts128f(tl + 2560 + l40 * 32 + 16, (f32x4){L1[2].x, L1[2].y, L1[3].x, L1[3].y});
#pragma unroll
      for (int m = 0; m < 15; ++m)
        if (m < rc) {
#pragma unroll
          for (int k = 0; k < 4; ++k) gg[k] = cfmap(P[k], gg[k], (f32x2){bflo(ag[m][k]), bfhi(ag[m][k])});
        }
      if (par) {
#pragma unroll
        for (int k = 0; k < 4; ++k) gg[k] = cfmap(L64[k], gg[k], (f32x2){bflo(al[k]), bfhi(al[k])});
      }
#pragma unroll
      for (int k = 0; k < 4; ++k)
        sts128f(tl + l40 * 64 + k * 16, (f32x4){tv[k >> 1][2 * (k & 1)], tv[k >> 1][2 * (k & 1) + 1], gg[k].x, gg[k].y});
    }
    TS(1);
#pragma unroll
    for (int m = 0; m < 4; ++m)
#pragma unroll
      for (int n = 0; n < 5; ++n) acc[m][n] = (f32x4){0.f, 0.f, 0.f, 0.f};
    const float nf = (float)(4 * c + 1);
    const unsigned tq = tl + q * 64, lq = tl + 2560 + q * 32;
    const unsigned tb = lb + M_RS + sw * 20480 + vo;
    u32x4 z[4];
    f32x4 tt[4], Lq[2];
    float zr[4], zi[4], e0, e1, e2, e3;
    auto rdt = [&](int i) {
#pragma unroll
      for (int d = 0; d < 4; ++d) tt[d] = lds128f(tq + i * 256 + d * 16);
    };
    auto rdl = [&](int i) { Lq[0] = lds128f(lq + i * 128); Lq[1] = lds128f(lq + i * 128 + 16); };
    auto opA = [&](int k) {
      const int m = k / 20 + 1, d = (k / 5) & 3, o = k % 5;
      const float Lx = Lq[d >> 1][2 * (d & 1)], Ly = Lq[d >> 1][2 * (d & 1) + 1];
      if (o == 0) e0 = vmul(Ly, zi[d]);
      else if (o == 1) e1 = vmul(Ly, zr[d]);
      else if (o == 2) zr[d] = vfms(Lx, zr[d], e0);
      else if (o == 3) zi[d] = vfma(Lx, zi[d], e1);
      else z[m][d] = pk2(zr[d], zi[d]);
    };
    auto opZ = [&](int k) {
      if (k >= 48) { z[0][k - 48] = pk2(zr[k - 48], zi[k - 48]); return; }
      const int d = k / 12, o = k % 12;
      if (o == 0) e0 = vmul(nf, tt[d].x);
      else if (o == 1) e1 = vmul(nf, tt[d].y);
      else if (o == 2) e0 = __builtin_amdgcn_exp2f(e0);
      else if (o == 3) e1 = __builtin_amdgcn_fractf(e1);
      else if (o == 4) e2 = __builtin_amdgcn_cosf(e1);
      else if (o == 5) e3 = __builtin_amdgcn_sinf(e1);
      else if (o == 6) e2 = vmul(e0, e2);
      else if (o == 7) e3 = vmul(e0, e3);
      else if (o == 8) e0 = vmul(e3, tt[d].w);
      else if (o == 9) e1 = vmul(e3, tt[d].z);
      else if (o == 10) zr[d] = vfms(e2, tt[d].z, e0);
      else zi[d] = vfma(e2, tt[d].w, e1);
    };
    rdt(0); rdl(0);
#pragma unroll
    for (int k = 0; k < 52; ++k) opZ(k);
    SB();
#pragma unroll
    for (int i = 0; i < 10; ++i) {
      if (i + 2 < 10) {
        if (SLP != SPRIO) __builtin_amdgcn_s_setprio(SLP);
        load(i + 2);
        SB();
        if (SLP != SPRIO) __builtin_amdgcn_s_setprio(SPRIO);
      }
      SB();
#pragma unroll
      for (int sl = 0; sl < 20; ++sl) {
        acc[sl / 5][sl % 5] = mfma(B[i % 3][sl % 5], A[i % 3][sl / 5], acc[sl / 5][sl % 5]);
        opA(3 * sl); opA(3 * sl + 1); opA(3 * sl + 2);
        if (sl == 12 && i < 9) rdt(i + 1);
        SB();
      }
#pragma unroll
      for (int sl = 0; sl < 20; ++sl) {
        acc[sl / 5][sl % 5] = mfma(B[i % 3][sl % 5], z[sl / 5], acc[sl / 5][sl % 5]);
        if (i < 9) {
          constexpr int st[21] = {0, 3, 5, 7, 10, 12, 15, 17, 19, 22, 24, 27, 29, 31, 34, 36, 39, 41, 43, 46, 52};
#pragma unroll
          for (int k = st[sl]; k < st[sl + 1]; ++k) opZ(k);
          if (sl == 12) rdl(i + 1);
        } else if (sl >= 2) {
          sts128f(tb + (sl - 2) * 1024, acc[(sl - 2) / 5][(sl - 2) % 5]);
        }
        SB();
      }
      TS(2 + i);
    }
    sts128f(tb + 18 * 1024, acc[3][3]);
    sts128f(tb + 19 * 1024, acc[3][4]);
    TS(14);
    BARRIER();
    TS(15);
    TSDUMP(33554432 + 1048576);
    return;
  }

  __builtin_amdgcn_s_setprio(OPRIO);
  __amdgpu_buffer_rsrc_t rA = mkrsrc(ws + WS_H1F + (size_t)rb * 48 * 4096, 48 * 4096);
  __amdgpu_buffer_rsrc_t rB = mkrsrc(ws + WS_W2F + (size_t)cg * 48 * 5120, 48 * 5120);
  u32x4 A[4][4], B[4][5];
  auto loadA = [&](int i) {
    int bf = i & 3, ks = w + 4 * ((i >> 1) + 6 * ((i & 1) ^ gh));
#pragma unroll
    for (int m = 0; m < 4; ++m) A[bf][m] = __builtin_amdgcn_raw_buffer_load_b128(rA, vo, ks * 4096 + ((w + m) & 3) * 1024, 0);
  };
  auto loadB = [&](int i) {
    int bf = i & 3, ks = w + 4 * ((i >> 1) + 6 * ((i & 1) ^ gh));
#pragma unroll
    for (int n = 0; n < 5; ++n) B[bf][n] = __builtin_amdgcn_raw_buffer_load_b128(rB, vo + n * 1024, ks * 5120, 0);
  };
  auto load = [&](int i) { loadA(i); loadB(i); };
  u32x2 xv[5], sk[5];
  f32x2 st;
  load(0);
  load(1);
  load(2);
  TS(1);
#pragma unroll
  for (int m = 0; m < 4; ++m)
#pragma unroll
    for (int n = 0; n < 5; ++n) acc[m][n] = (f32x4){0.f, 0.f, 0.f, 0.f};
  unsigned da[3];
#pragma unroll
  for (int i = 0; i < 12; ++i) {
    if (OTP != OPRIO && i == OTI) __builtin_amdgcn_s_setprio(OTP);
    if (i == 9) {
      int l2 = lane;
      asm volatile("" : "+v"(l2));
      const int trow = 64 * rb + 4 * (l2 & 15) + w;
      const int eo = trow * 640 + 80 * cg + 4 * (l2 >> 4);
      __amdgpu_buffer_rsrc_t rx = mkrsrc(x, 2621440), rd = mkrsrc(Dsk, 1280), rt = mkrsrc(ws + WS_ST, 16384);
      st = __builtin_bit_cast(f32x2, __builtin_amdgcn_raw_buffer_load_b64(rt, trow * 8, 0, 0));
#pragma unroll
      for (int n = 0; n < 5; ++n) {
        xv[n] = __builtin_amdgcn_raw_buffer_load_b64(rx, eo * 2 + n * 32, 0, 0);
        sk[n] = __builtin_amdgcn_raw_buffer_load_b64(rd, (l2 >> 4) * 8 + n * 32, 160 * cg, 0);
      }
      SB();
    }
    if (i + 3 < 12) {
      if (OLP != OPRIO && i >= OLI && (OTP == OPRIO || i < OTI)) __builtin_amdgcn_s_setprio(OLP);
      load(i + 3);
      SB();
      if (OLP != OPRIO && i >= OLI && (OTP == OPRIO || i < OTI)) __builtin_amdgcn_s_setprio(OPRIO);
    }
    SB();
    if (i < 11) {
#pragma unroll
      for (int m = 0; m < 4; ++m)
#pragma unroll
        for (int n = 0; n < 5; ++n) acc[m][n] = mfma(B[i & 3][n], A[i & 3][m], acc[m][n]);
    } else {
      int v2 = vo;
      asm volatile("" : "+v"(v2));
#pragma unroll
      for (int j = 1; j < 4; ++j) da[j - 1] = lb + M_RO + (((w + j) & 3) * 3 + (j - 1)) * 5120 + v2;
#pragma unroll
      for (int sl = 0; sl < 20; ++sl) {
        const int m = (sl / 5 + 1) & 3, n = sl % 5;
        acc[m][n] = mfma(B[i & 3][n], A[i & 3][m], acc[m][n]);
        if (sl >= 2 && sl < 17) sts128f(da[(sl - 2) / 5] + ((sl - 2) % 5) * 1024, acc[(sl - 2) / 5 + 1][(sl - 2) % 5]);
        SB();
      }
    }
    SB();
    TS(2 + i);
  }
  WAITL0();
  if (lane == 0) *(volatile AS3 unsigned*)(uintptr_t)(lb + M_CTL + w * 4) = 1u;
  {
#pragma unroll
    for (int n = 0; n < 5; ++n)
#pragma unroll
      for (int hf = 0; hf < 2; ++hf) {
        unsigned p = pk2a(vsub(bflo(xv[n][hf]), st.x), vsub(bfhi(xv[n][hf]), st.x));
        p = pk2a(vmul(bflo(p), st.y), vmul(bfhi(p), st.y));
        sk[n][hf] = pk2a(vmul(bflo(p), bflo(sk[n][hf])), vmul(bfhi(p), bfhi(sk[n][hf])));
      }
#pragma unroll
    for (int n = 0; n < 5; ++n) asm volatile("" : "+v"(sk[n]), "+v"(xv[n]));
  }
  for (;;) {
    u32x4 f = *(volatile AS3 u32x4*)(uintptr_t)(lb + M_CTL);
    if ((f.x & f.y & f.z & f.w) != 0u) break;
    __builtin_amdgcn_s_sleep(1);
  }
  asm volatile("" ::: "memory");
  TS(14);
  f32x4 xs[5], g2[5];
  {
    f32x4 po[5][3];
#pragma unroll
    for (int n = 0; n < 5; ++n)
#pragma unroll
      for (int j = 0; j < 3; ++j) po[n][j] = lds128f(lb + M_RO + ((w * 3 + j) * 5 + n) * 1024 + vo);
    SB();
#pragma unroll
    for (int n = 0; n < 5; ++n) {
      f32x4 o;
#pragma unroll
      for (int j = 0; j < 4; ++j) o[j] = vadd(vadd(acc[0][n][j], po[n][0][j]), vadd(po[n][1][j], po[n][2][j]));
#pragma unroll
      for (int hf = 0; hf < 2; ++hf) {
        unsigned p = pk2a(o[2 * hf], o[2 * hf + 1]);
        float v0 = bflo(p), v1 = bfhi(p);
        float e0 = __builtin_amdgcn_exp2f(vmul(v0, -1.44269504088896f)), e1 = __builtin_amdgcn_exp2f(vmul(v1, -1.44269504088896f));
        e0 = __builtin_amdgcn_rcpf(vadd(e0, 1.f)); e1 = __builtin_amdgcn_rcpf(vadd(e1, 1.f));
        p = pk2a(vmul(v0, e0), vmul(v1, e1));
        v0 = bflo(p); v1 = bfhi(p);
        xs[n][2 * hf] = vfma(v0, bflo(sk[n][hf]), bflo(xv[n][hf]));
        xs[n][2 * hf + 1] = vfma(v1, bfhi(sk[n][hf]), bfhi(xv[n][hf]));
        g2[n][2 * hf] = v0; g2[n][2 * hf + 1] = v1;
      }
    }
#pragma unroll
    for (int n = 0; n < 5; ++n) asm volatile("" : "+v"(xs[n]), "+v"(g2[n]));
  }
  __amdgpu_buffer_rsrc_t ro = mkrsrc(out, 5242880);
  int l2 = lane;
  asm volatile("" : "+v"(l2));
  const int eo2 = ((64 * rb + 4 * (l2 & 15) + w) * 640 + 80 * cg + 4 * (l2 >> 4)) * 4;
  TS(15);
  BARRIER_NW();
  TS(16);
  f32x4 ps[5][4];
#pragma unroll
  for (int n = 0; n < 5; ++n)
#pragma unroll
    for (int k = 0; k < 4; ++k) ps[n][k] = lds128f(lb + M_RS + (w * 5 + n) * 1024 + k * 20480 + vo);
  SB();
#pragma unroll
  for (int n = 0; n < 5; ++n) {
    f32x4 s = (ps[n][0] + ps[n][1]) + (ps[n][2] + ps[n][3]);
    f32x4 r = __builtin_elementwise_fma(s, g2[n], xs[n]);
    __builtin_amdgcn_raw_buffer_store_b128(__builtin_bit_cast(u32x4, r), ro, eo2 + n * 64, 0, 0);
    SB();
  }
}

extern "C" __device__ __attribute__((used, noinline)) void kend() {}

extern "C" void kernel_launch(void* const* d_in, const int* in_sizes, int n_in, void* d_out, int out_size, void* d_ws,
                              size_t ws_size, hipStream_t stream) {
  const u16* x = (const u16*)d_in[0];
  k1<<<256, 512, 0, stream>>>(x, (const u16*)d_in[1], (const u16*)d_in[2], (const u16*)d_in[3], (const u16*)d_in[4],
                              (const u16*)d_in[5], (const u16*)d_in[6], (const u16*)d_in[7], (const u16*)d_in[10],
                              (const u16*)d_in[11], (char*)d_ws);
  k2<<<256, 512, 0, stream>>>(x, (const u16*)d_in[8], (char*)d_ws, (float*)d_out);
}
```

```cpp
#include <hip/hip_runtime.h>
#include <stdint.h>

typedef unsigned short u16;
typedef __attribute__((ext_vector_type(8))) __bf16 bf16x8;
typedef __attribute__((ext_vector_type(2))) __bf16 bf16x2;
typedef __attribute__((ext_vector_type(4))) float f32x4;
typedef __attribute__((ext_vector_type(2))) float f32x2;
typedef __attribute__((ext_vector_type(4))) unsigned u32x4;
typedef __attribute__((ext_vector_type(2))) unsigned u32x2;
typedef __attribute__((ext_vector_type(4))) short s16x4;
#define AS3 __attribute__((address_space(3)))
#define DEVI __device__ __forceinline__

constexpr size_t WS_H1F = 0;
constexpr size_t WS_HF = 6291456;
constexpr size_t WS_W2F = 11534336;
constexpr size_t WS_CF = 13500416;
constexpr size_t WS_AGG = 15138816;
constexpr size_t WS_AGL = 15220736;
constexpr size_t WS_TAB = 15302656;
constexpr size_t WS_T2 = 15339520;
constexpr size_t WS_T3 = 15360000;
constexpr size_t WS_ST = 15323136;

DEVI float bf2f(unsigned v) { return __uint_as_float(v << 16); }
DEVI float bflo(unsigned v) { return __uint_as_float(v << 16); }
DEVI float bfhi(unsigned v) { return __uint_as_float(v & 0xffff0000u); }
DEVI unsigned pk2(float a, float b) {
  bf16x2 r = {(__bf16)a, (__bf16)b};
  return __builtin_bit_cast(unsigned, r);
}
DEVI unsigned pk2a(float a, float b) { unsigned r; asm("v_cvt_pk_bf16_f32 %0, %1, %2" : "=v"(r) : "v"(a), "v"(b)); return r; }
DEVI float rbf(float a) { return bflo(pk2(a, 0.f)); }
template <int N> DEVI float shr(float v) {
  return __builtin_bit_cast(float, __builtin_amdgcn_update_dpp(0, __builtin_bit_cast(int, v), 0x110 + N, 0xf, 0xf, true));
}
template <int N> DEVI f32x2 shr2(f32x2 v) { f32x2 r; r.x = shr<N>(v.x); r.y = shr<N>(v.y); return r; }
DEVI f32x2 cpow(float a, float b, float n) {
  float mg = __builtin_amdgcn_exp2f(n * a);
  float rv = __builtin_amdgcn_fractf(n * b);
  f32x2 r;
  r.x = mg * __builtin_amdgcn_cosf(rv);
  r.y = mg * __builtin_amdgcn_sinf(rv);
  return r;
}
DEVI f32x2 cmul(f32x2 a, f32x2 b) { f32x2 r; r.x = a.x * b.x - a.y * b.y; r.y = a.x * b.y + a.y * b.x; return r; }
DEVI f32x2 pfma(f32x2 a, f32x2 b, f32x2 c) { return __builtin_elementwise_fma(a, b, c); }
DEVI f32x2 cfmap(f32x2 a, f32x2 b, f32x2 c) {
  f32x2 ax = {a.x, a.x}, ay = {-a.y, a.y}, bs = {b.y, b.x};
  return pfma(ay, bs, pfma(ax, b, c));
}
DEVI f32x2 cmulp(f32x2 a, f32x2 b) {
  f32x2 ax = {a.x, a.x}, ay = {-a.y, a.y}, bs = {b.y, b.x};
  return pfma(ay, bs, ax * b);
}
extern "C" __device__ __attribute__((const)) float mul_legacy_f32(float, float) __asm("llvm.amdgcn.fmul.legacy");
DEVI float vmul(float a, float b) { return mul_legacy_f32(a, b); }
DEVI float vfma(float a, float b, float c) { float r; asm("v_fma_f32 %0, %1, %2, %3" : "=v"(r) : "v"(a), "v"(b), "v"(c)); return r; }
DEVI float vadd(float a, float b) { float r; asm("v_add_f32 %0, %1, %2" : "=v"(r) : "v"(a), "v"(b)); return r; }
DEVI float vsub(float a, float b) { float r; asm("v_sub_f32 %0, %1, %2" : "=v"(r) : "v"(a), "v"(b)); return r; }
DEVI float vfms(float a, float b, float c) { float r; asm("v_fma_f32 %0, %1, %2, -%3" : "=v"(r) : "v"(a), "v"(b), "v"(c)); return r; }
DEVI float vfnm(float a, float b, float c) { float r; asm("v_fma_f32 %0, -%1, %2, %3" : "=v"(r) : "v"(a), "v"(b), "v"(c)); return r; }
DEVI f32x2 cfmas(f32x2 a, f32x2 b, f32x2 c) {
  f32x2 r;
  r.x = vfnm(a.y, b.y, vfma(a.x, b.x, c.x));
  r.y = vfma(a.y, b.x, vfma(a.x, b.y, c.y));
  return r;
}
DEVI f32x2 cmuls(f32x2 a, f32x2 b) {
  f32x2 r;
  r.x = vfnm(a.y, b.y, vmul(a.x, b.x));
  r.y = vfma(a.y, b.x, vmul(a.x, b.y));
  return r;
}
DEVI f32x2 cpows(float a, float b, float n) {
  float mg = __builtin_amdgcn_exp2f(vmul(n, a));
  float rv = __builtin_amdgcn_fractf(vmul(n, b));
  f32x2 r;
  r.x = vmul(mg, __builtin_amdgcn_cosf(rv));
  r.y = vmul(mg, __builtin_amdgcn_sinf(rv));
  return r;
}
DEVI float siluf(float a) { return a * __builtin_amdgcn_rcpf(1.f + __builtin_amdgcn_exp2f(-1.44269504088896f * a)); }
DEVI void swap16(unsigned& a, unsigned& b) { auto r = __builtin_amdgcn_permlane16_swap(a, b, false, false); a = r[0]; b = r[1]; }
DEVI void swap32(unsigned& a, unsigned& b) { auto r = __builtin_amdgcn_permlane32_swap(a, b, false, false); a = r[0]; b = r[1]; }
DEVI void swap16f(float& a, float& b) { unsigned x = __float_as_uint(a), y = __float_as_uint(b); swap16(x, y); a = __uint_as_float(x); b = __uint_as_float(y); }
DEVI void swap32f(float& a, float& b) { unsigned x = __float_as_uint(a), y = __float_as_uint(b); swap32(x, y); a = __uint_as_float(x); b = __uint_as_float(y); }

template <int N> struct IC { static constexpr int v = N; };
template <int A, int B, class F> __device__ __forceinline__ void sfor(F&& f) { if constexpr (A < B) { f(IC<A>{}); sfor<A + 1, B>(f); } }
#define WAITV(n) asm volatile("s_waitcnt vmcnt(%0)" ::"n"(n) : "memory")
#define WAITL0() asm volatile("s_waitcnt lgkmcnt(0)" ::: "memory")
#define BARRIER() do { WAITL0(); __builtin_amdgcn_s_barrier(); asm volatile("" ::: "memory"); } while (0)
#define BARRIER_NW() do { asm volatile("" ::: "memory"); __builtin_amdgcn_s_barrier(); asm volatile("" ::: "memory"); } while (0)
#define SB() __builtin_amdgcn_sched_barrier(0)
DEVI void glds(const void* p, unsigned ldsaddr) {
  unsigned keep;
  asm volatile("s_mov_b32 %0, m0\n\ts_mov_b32 m0, %2\n\ts_nop 0\n\tglobal_load_lds_dwordx4 %1, off\n\ts_mov_b32 m0, %0"
               : "=&s"(keep) : "v"(p), "s"(ldsaddr) : "memory");
}

DEVI u32x4 lds128(unsigned off) { return *(const AS3 u32x4*)(uintptr_t)off; }
DEVI f32x4 lds128f(unsigned off) { return *(const AS3 f32x4*)(uintptr_t)off; }
DEVI f32x2 lds64f(unsigned off) { return *(const AS3 f32x2*)(uintptr_t)off; }
DEVI void sts128f(unsigned off, f32x4 v) { *(AS3 f32x4*)(uintptr_t)off = v; }
DEVI void sts64f(unsigned off, f32x2 v) { *(AS3 f32x2*)(uintptr_t)off = v; }
DEVI void sts32f(unsigned off, float v) { *(AS3 float*)(uintptr_t)off = v; }
DEVI u32x2 ldstr(unsigned off) {
  s16x4 r = __builtin_amdgcn_ds_read_tr16_b64_v4i16((AS3 s16x4*)(uintptr_t)off);
  return __builtin_bit_cast(u32x2, r);
}
DEVI u32x4 ldstr2(unsigned a, unsigned d) { u32x2 lo = ldstr(a), hi = ldstr(a + d); return (u32x4){lo.x, lo.y, hi.x, hi.y}; }
DEVI f32x4 mfma(u32x4 a, u32x4 b, f32x4 c) {
  return __builtin_amdgcn_mfma_f32_16x16x32_bf16(__builtin_bit_cast(bf16x8, a), __builtin_bit_cast(bf16x8, b), c, 0, 0, 0);
}
#define STAUX 16
#define PRO 2
#define SPRIO 2
#define OPRIO 0
#define OLP OPRIO
#define SLP SPRIO
#define OLI 0
#define OTP 3
#define OTI 10
DEVI void st128(u32x4 v, __amdgpu_buffer_rsrc_t rs, int off) { __builtin_amdgcn_raw_buffer_store_b128(v, rs, off, 0, STAUX); }
DEVI __amdgpu_buffer_rsrc_t mkrsrc(const void* p, unsigned bytes) {
  return __builtin_amdgcn_make_buffer_rsrc((void*)p, 0, bytes, 0x00020000);
}
#define TSDECL
#define TS(k)
#define TSDUMP(base)

constexpr unsigned SLOT = 38912, OB = 16384, OW = 26624;
constexpr unsigned SPARE = 4 * SLOT;
constexpr unsigned T_TAB = SPARE;
constexpr unsigned T_AG = SPARE + 2560;
constexpr unsigned T_FLAG = SPARE + 3072;
constexpr unsigned T_PF = SPARE + 4096;
constexpr unsigned K1_LDS = SPARE + 5120;
constexpr unsigned S_W2 = 2 * SLOT;
constexpr unsigned E_ST = 3 * SLOT;
constexpr unsigned E_CS = E_ST + 2048;
constexpr unsigned E_A5 = E_CS + 2048;

DEVI void stat(u32x4 f, float& s, float& q) {
#pragma unroll
  for (int r = 0; r < 4; ++r) {
    float lo = bflo(f[r]), hi = bfhi(f[r]);
    s += lo; q = fmaf(lo, lo, q);
    s += hi; q = fmaf(hi, hi, q);
  }
}
template <int NM, int NV> DEVI void ileave() {
#pragma unroll
  for (int i = 0; i < NM; ++i) {
    __builtin_amdgcn_sched_group_barrier(0x008, 1, 0);
    __builtin_amdgcn_sched_group_barrier(0x002, NV, 0);
  }
}
struct Tab { f32x2 L, L4, L8, L16, L32, L64, co; };
DEVI void rdtab(Tab& t, unsigned a) {
  f32x4 v0 = lds128f(a), v1 = lds128f(a + 16), v2 = lds128f(a + 32);
  f32x2 v3 = lds64f(a + 48);
  t.L = (f32x2){v0.x, v0.y}; t.L4 = (f32x2){v0.z, v0.w}; t.L8 = (f32x2){v1.x, v1.y}; t.L16 = (f32x2){v1.z, v1.w};
  t.L32 = (f32x2){v2.x, v2.y}; t.L64 = (f32x2){v2.z, v2.w}; t.co = v3;
}

DEVI void dma_s(unsigned voff, const char* sbase, unsigned ldsaddr, int t, int lim) {
  asm volatile("s_cmp_lt_i32 %3, %4\n\ts_cbranch_scc0 .Ldma%=\n\ts_mov_b32 m0, %2\n\ts_nop 0\n\tglobal_load_lds_dwordx4 %0, %1\n.Ldma%=:"
               ::"v"(voff), "s"(sbase), "s"(ldsaddr), "s"(t), "n"(lim) : "memory", "scc");
}
DEVI void dma_v(const char* p, unsigned ldsaddr, int t, int lim) {
  asm volatile("s_cmp_lt_i32 %2, %3\n\ts_cbranch_scc0 .Ldmb%=\n\ts_mov_b32 m0, %1\n\ts_nop 0\n\tglobal_load_lds_dwordx4 %0, off\n.Ldmb%=:"
               ::"v"(p), "s"(ldsaddr), "s"(t), "n"(lim) : "memory", "scc");
}
DEVI void dma_s0(unsigned voff, const char* sbase, unsigned ldsaddr) {
  asm volatile("s_mov_b32 m0, %2\n\ts_nop 0\n\tglobal_load_lds_dwordx4 %0, %1" ::"v"(voff), "s"(sbase), "s"(ldsaddr) : "memory");
}
DEVI void dma_v0(const char* p, unsigned ldsaddr) {
  asm volatile("s_mov_b32 m0, %1\n\ts_nop 0\n\tglobal_load_lds_dwordx4 %0, off" ::"v"(p), "s"(ldsaddr) : "memory");
}
DEVI const char* code_base(unsigned& len, unsigned& off2) {
  uint64_t pc;
  unsigned d;
  asm volatile("s_getpc_b64 %0\n.Lpc%=:\n\ts_mov_b32 %1, .Lpc%=-k1\n\ts_mov_b32 %2, kend-k1\n\ts_mov_b32 %3, k2-k1"
               : "=s"(pc), "=s"(d), "=s"(len), "=s"(off2));
  return (const char*)(pc - d);
}
#define WAITV2(t, lim, A, B) asm volatile("s_cmp_lt_i32 %0, %1\n\ts_cbranch_scc1 .Lwv%=\n\ts_waitcnt vmcnt(%3)\n.Lwv%=:\n\ts_waitcnt vmcnt(%2)" ::"s"(t), "n"(lim), "n"(A), "n"(B) : "memory", "scc")
#define LGKM0() __builtin_amdgcn_s_waitcnt(0xc07f)

extern "C" __global__ __launch_bounds__(512, 2) void k1(const u16* x, const u16* Lre, const u16* Lim, const u16* Bre, const u16* Bim,
                                             const u16* Cre, const u16* Cim, const u16* ldt, const u16* w1, const u16* w2,
                                             char* ws) {
  __shared__ __attribute__((aligned(1024))) char smem[K1_LDS];
  const unsigned lb = (unsigned)(uintptr_t)(AS3 char*)smem;
  const int b = blockIdx.x, xcd = b & 7, jb = b >> 3;
  const int rc = (xcd >> 1) * 4 + (jb & 3), g = (xcd & 1) * 8 + (jb >> 2);
  const int tid = threadIdx.x, lane = tid & 63;
  const int w = __builtin_amdgcn_readfirstlane(tid >> 6);
  const int c = lane & 15, q = lane >> 4;
  const int l3 = lane >> 3, l7 = lane & 7;
  const int h = (w >> 1) & 1, a = w & 1;
  const int kk0 = h ^ a;
  TSDECL;
  TS(0);

  const u32x4 ONES = {0x3f803f80u, 0x3f803f80u, 0x3f803f80u, 0x3f803f80u};
  unsigned xo[2];
#pragma unroll
  for (int p = 0; p < 2; ++p) xo[p] = lb + 8192 * h + c * 128 + (((4 * (kk0 ^ p) + q) ^ (c & 7)) << 4);
  float sv[4] = {0.f, 0.f, 0.f, 0.f};
  float t0, t1;
  const int rb = 2 * rc + h;
  unsigned pr[3];
  auto prload = [&]() {
    int p = 40 * g + (lane < 40 ? lane : 0);
    const u16* a0 = Lre + p; const u16* a1 = Lim + p; const u16* a2 = ldt + p;
    asm volatile("global_load_ushort %0, %1, off" : "=v"(pr[0]) : "v"(a0) : "memory");
    asm volatile("global_load_ushort %0, %1, off" : "=v"(pr[1]) : "v"(a1) : "memory");
    asm volatile("global_load_ushort %0, %1, off" : "=v"(pr[2]) : "v"(a2) : "memory");
  };
  float lr, li, ea, eb;
  auto tab0 = [&]() {
    asm volatile("" : "+v"(pr[0]), "+v"(pr[1]), "+v"(pr[2]));
    lr = bf2f(pr[0]); li = bf2f(pr[1]);
    float dt = rbf(expf(bf2f(pr[2])));
    ea = dt * lr * 1.44269504088896f; eb = dt * li * 0.15915494309189535f;
  };
  auto tab1 = [&]() {
    const float n = (w == 0 || w > 5) ? 1.f : (float)(2 << w);
    f32x2 P = cpow(ea, eb, n);
    if (w == 6) {
      float nr = P.x - 1.f, ni = P.y, dd = 2.f / (lr * lr + li * li);
      P = (f32x2){(nr * lr + ni * li) * dd, (ni * lr - nr * li) * dd};
    }
    if (w == 7) {
      P = (f32x2){ea, eb};
      if (lane >= 62) sts32f(lb + T_FLAG + (lane - 62) * 4, 0.f);
    }
    if (lane < 40) sts64f(lb + T_TAB + lane * 64 + w * 8, P);
  };
  auto opS = [&](const u32x4& f, float& s, float& qq, int k) {
    const int r = k / 6, o = k % 6;
    if (o == 0) t0 = bflo(f[r]);
    else if (o == 1) t1 = bfhi(f[r]);
    else if (o == 2) s = vadd(s, t0);
    else if (o == 3) qq = vfma(t0, t0, qq);
    else if (o == 4) s = vadd(s, t1);
    else qq = vfma(t1, t1, qq);
  };

  auto mr4 = [&](float* m, float* r) {
#pragma unroll
    for (int i = 0; i < 4; ++i) {
      f32x4 v = lds128f(lb + E_ST + (64 * h + 4 * c + i) * 16);
      float mean = (v.x + v.z) * (1.f / 640.f);
      float var = fmaxf((v.y + v.w) * (1.f / 640.f) - mean * mean, 0.f);
      float den = rbf(__builtin_amdgcn_sqrtf(rbf(rbf(var) + 1e-5f)));
      m[i] = rbf(mean); r[i] = __builtin_amdgcn_rcpf(den);
    }
  };
  auto put_stats = [&](int i0) {
    swap32f(sv[0], sv[2]); swap32f(sv[1], sv[3]);
    float w0 = sv[0] + sv[2], w1v = sv[1] + sv[3];
    swap16f(w0, w1v);
    sts32f(lb + E_ST + ((64 * h + 4 * c + i0 + (q >> 1)) * 4 + 2 * a + (q & 1)) * 4, w0 + w1v);
  };

  if (w < 4) {
    unsigned vo[4];
#pragma unroll
    for (int v = 0; v < 4; ++v) {
      int rho = 8 * (w + 4 * v) + l3;
      vo[v] = (64 * (rho >> 6) + 4 * (rho & 15) + ((rho >> 4) & 3)) * 1280 + ((l7 ^ l3) << 4);
    }
    const char* sb0 = (const char*)x + rc * 163840;
    const char* sb1 = sb0;
    unsigned dst = lb + w * 1024, dstep = 4096;
    auto issue0 = [&](int st) {
#pragma unroll
      for (int v = 0; v < 4; ++v) dma_s0(vo[v], sb0 + st * 128, dst + st * SLOT + v * 4096);
    };
    issue0(0);
    prload();
    BARRIER_NW();
    issue0(1);
    BARRIER_NW();
    issue0(2);
    {
      unsigned len, off2;
      const char* cb = code_base(len, off2);
      int lm = ((int)len - 1024) & ~15;
      lm = lm > 0 ? lm : 0;
      int po = (4 * jb + w) * 1024;
      po = (po < lm ? po : lm) + lane * 16;
      dma_s0(po, cb, lb + T_PF);
    }
    unsigned wo[2][3];
#pragma unroll
    for (int u = 0; u < 3; ++u) {
      int tl = a ? (u == 2 ? 3 : 4 + u) : u;
      wo[0][u] = lb + OW + (8 * q + (c >> 2)) * 192 + (c & 3) * 8 + ((tl ^ (q & 1)) << 5) + kk0 * 6144;
      wo[1][u] = wo[0][u] + 6144 - 2 * kk0 * 6144;
    }
    f32x4 acc[4][3], cs[3];
#pragma unroll
    for (int u = 0; u < 3; ++u) {
      cs[u] = (f32x4){0.f, 0.f, 0.f, 0.f};
#pragma unroll
      for (int i = 0; i < 4; ++i) acc[i][u] = cs[u];
    }
    u32x4 X[2][2][4], W[2][2][3];
    auto rd = [&](int r, int k) {
      const int p = k / 10, o = k % 10;
      if (o < 6) {
        u32x2 v = ldstr(wo[p][o >> 1] + (o & 1) * 768);
        W[r][p][o >> 1][2 * (o & 1)] = v.x; W[r][p][o >> 1][2 * (o & 1) + 1] = v.y;
      } else X[r][p][o - 6] = lds128(xo[p] + 2048 * (o - 6));
    };
    auto adv = [&](unsigned d) {
#pragma unroll
      for (int p = 0; p < 2; ++p) {
        xo[p] += d;
#pragma unroll
        for (int u = 0; u < 3; ++u) wo[p][u] += d;
      }
    };
    int unit = 2 * jb + (w & 1);
    if (w < 2) { if (unit >= 60) unit -= 4; } else { if (unit >= 50) unit -= 14; }
    const int u5 = unit / 5;
    const int uks = (w < 2 ? 12 : 10) * (xcd >> 1) + u5, udb = 5 * (xcd & 1) + (unit - 5 * u5);

    WAITV(9);
    BARRIER_NW();
    TS(1);
#pragma unroll
    for (int k = 0; k < 20; ++k) rd(0, k);
    adv(SLOT);
    SB();
    dma_s0(vo[0], sb0 + 384, dst + 3 * SLOT);
    tab0(); SB();
    dma_s0(vo[1], sb0 + 384, dst + 3 * SLOT + 4096);
    tab1(); SB();
    dma_s0(vo[2], sb0 + 384, dst + 3 * SLOT + 8192);
    dma_s0(vo[3], sb0 + 384, dst + 3 * SLOT + 12288);
    sb0 += 512; sb1 = sb0;
    LGKM0();
    WAITV(9);
    BARRIER_NW();
    TS(2);
    auto stage = [&](int r, int t) {
#pragma unroll
      for (int sl = 0; sl < 27; ++sl) {
        if (sl < 24) {
          const int p = sl / 12, i = (sl % 12) / 3, u = sl % 3;
          acc[i][u] = mfma(W[r][p][u], X[r][p][i], acc[i][u]);
        } else cs[sl - 24] = mfma(W[r][1][sl - 24], ONES, cs[sl - 24]);
        if (sl < 20) rd(r ^ 1, sl);
        if (sl % 6 == 2 && sl < 24) dma_s(vo[sl / 6], sl < 12 ? sb0 : sb1, dst + (sl / 6) * dstep, t, 7);
        if (sl < 12) { opS(X[r][0][0], sv[0], sv[1], 2 * sl); opS(X[r][0][0], sv[0], sv[1], 2 * sl + 1); }
        else if (sl < 24) { opS(X[r][1][1], sv[2], sv[3], 2 * sl - 24); opS(X[r][1][1], sv[2], sv[3], 2 * sl - 23); }
        SB();
      }
    };
    for (int t = 0; t < 10; t += 2) {
      if (t == 6) {
        dst = lb + S_W2 + w * 4096; dstep = 1024;
        if (w < 2) {
          sb0 = (const char*)w2 + uks * 40960 + udb * 128; sb1 = sb0;
#pragma unroll
          for (int v = 0; v < 4; ++v) {
            int r = 8 * v + l3;
            vo[v] = r * 1280 + ((l7 ^ (((r >> 1) & 3) << 1)) << 4);
          }
        } else {
          int G = uks / 5, kr = uks - 5 * G;
          int p0 = 80 * G + (kr < 4 ? 40 * (kr >> 1) + 16 * (kr & 1) : 32);
          sb0 = (const char*)Cre + udb * 81920 + p0 * 2; sb1 = (const char*)Cim + udb * 81920 + p0 * 2;
          vo[0] = lane * 1280; vo[1] = vo[0] + (kr < 4 ? 16 : 80); vo[2] = vo[0]; vo[3] = vo[1];
        }
      }
      stage(0, t);
      adv((t & 2) ? -3 * SLOT : SLOT);
      dst += SLOT; sb0 += 128; sb1 += 128;
      LGKM0();
      WAITV2(t, 8, 8, 0);
      BARRIER_NW();
      TS(3 + t);
      stage(1, t + 1);
      adv(SLOT);
      dst += (t & 2) ? -3 * SLOT : SLOT; sb0 += 128; sb1 += 128;
      LGKM0();
      WAITV2(t, 6, 8, 4);
      BARRIER_NW();
      TS(4 + t);
    }
    put_stats(0);
    if (c == 0) {
#pragma unroll
      for (int u = 0; u < 3; ++u) sts128f(lb + E_CS + ((w * 3 + u) * 4 + q) * 16, cs[u]);
    }
    BARRIER();
    TS(13);
    float m[4], r[4];
    mr4(m, r);
#pragma unroll
    for (int u = 0; u < 3; ++u) cs[u] += lds128f(lb + E_CS + (((w ^ 2) * 3 + u) * 4 + q) * 16);
    if (g == 0 && a == 0 && q == 0) {
      f32x4* st = (f32x4*)(ws + WS_ST + (64 * rb + 4 * c) * 8);
      st[0] = (f32x4){m[0], r[0], m[1], r[1]};
      st[1] = (f32x4){m[2], r[2], m[3], r[3]};
    }
    __amdgpu_buffer_rsrc_t rs = mkrsrc(ws + WS_H1F, 6291456);
    unsigned o[4][6];
#pragma unroll
    for (int i = 0; i < 4; ++i) {
      f32x2 nm = {-m[i], -m[i]}, rr = {r[i], r[i]}, re = {-1.44269504088896f * r[i], -1.44269504088896f * r[i]};
#pragma unroll
      for (int u = 0; u < 3; ++u)
#pragma unroll
        for (int hf = 0; hf < 2; ++hf) {
          f32x2 v = pfma((f32x2){cs[u][2 * hf], cs[u][2 * hf + 1]}, nm, (f32x2){acc[i][u][2 * hf], acc[i][u][2 * hf + 1]});
          f32x2 e = v * re;
          e.x = __builtin_amdgcn_exp2f(e.x); e.y = __builtin_amdgcn_exp2f(e.y);
          e = e + (f32x2){1.f, 1.f};
          e.x = __builtin_amdgcn_rcpf(e.x); e.y = __builtin_amdgcn_rcpf(e.y);
          v = (v * rr) * e;
          o[i][2 * u + hf] = pk2(v.x, v.y);
        }
      st128((u32x4){o[i][0], o[i][1], o[i][2], o[i][3]}, rs, lane * 16 + ((rb * 48 + 3 * g + 2 * a) * 4 + i) * 1024);
      if (i & 1) {
        swap16(o[i - 1][4], o[i][4]); swap16(o[i - 1][5], o[i][5]);
        st128((u32x4){o[i - 1][4], o[i - 1][5], o[i][4], o[i][5]}, rs,
              (c + 16 * (2 * a + (q >> 1))) * 16 + ((rb * 48 + 3 * g + 1) * 4 + (i - 1) + (q & 1)) * 1024);
      }
    }
    TS(14);
    SB();
    if (w < 2) {
      const bool nat = (uks % 3) == 1;
      int r0 = (nat ? 8 * q : 4 * q) + (c >> 2);
      int dr = nat ? 4 : 16;
      unsigned a0 = lb + S_W2 + w * 4096 + r0 * 128 + (c & 3) * 8;
      int f0 = (r0 >> 1) & 3, f1 = ((r0 + dr) >> 1) & 3;
#pragma unroll
      for (int j = 0; j < 4; ++j) {
        u32x2 lo = ldstr(a0 + ((j ^ f0) << 5));
        u32x2 hi = ldstr(a0 + dr * 128 + ((j ^ f1) << 5));
        int Dt = 4 * udb + j, cg = Dt / 5, nt = Dt - 5 * cg;
        *(u32x4*)(ws + WS_W2F + (size_t)(((cg * 48 + uks) * 5 + nt) * 1024) + lane * 16) = (u32x4){lo.x, lo.y, hi.x, hi.y};
      }
      if (rc < 7 && w == 0 && lane < 40) {
        int sg = lane < 32 ? (5 * (g >> 1) + 2 * (g & 1) + (lane >> 4)) * 16 + 4 * ((lane >> 1) & 3) + 2 * ((lane >> 3) & 1) + (lane & 1)
                           : (5 * (g >> 1) + 4) * 16 + 8 * (g & 1) + 4 * (lane & 1) + ((lane - 32) >> 1);
        const f32x2 e = lds64f(lb + T_TAB + lane * 64 + 56);
        const float n = (float)(rc < 4 ? 4 * rc + 1 : (rc == 4 ? 16 : 64 * (rc - 4)));
        char* dp = rc < 4 ? ws + WS_T2 + rc * 32 + (sg >> 2) * 128 : ws + WS_T3 + (rc - 4) * 32 + (sg >> 2) * 96;
        *(f32x2*)(dp + (sg & 3) * 8) = cpow(e.x, e.y, n);
      }
    } else {
      u32x4 sj[4];
#pragma unroll
      for (int v = 0; v < 4; ++v) sj[v] = lds128(lb + S_W2 + w * 4096 + v * 1024 + lane * 16);
      unsigned P[2][4][2];
#pragma unroll
      for (int ga = 0; ga < 2; ++ga)
#pragma unroll
        for (int j = 0; j < 4; ++j) {
          unsigned im = sj[2 + ga][j] ^ 0x80008000u;
          P[ga][j][0] = __builtin_amdgcn_perm(im, sj[ga][j], 0x05040100u);
          P[ga][j][1] = __builtin_amdgcn_perm(im, sj[ga][j], 0x07060302u);
        }
      int Dt = 4 * udb + q, cg = Dt / 5, nt = Dt - 5 * cg;
      char* dd = ws + WS_CF + (size_t)(((cg * 40 + uks) * 5 + nt) * 1024) + c * 16;
      if (uks % 5 < 4) {
#pragma unroll
        for (int j = 0; j < 4; ++j) *(u32x4*)(dd + j * 256) = (u32x4){P[0][j][0], P[0][j][1], P[1][j][0], P[1][j][1]};
      } else {
#pragma unroll
        for (int j = 0; j < 4; ++j)
          *(u32x4*)(dd + j * 256) = (u32x4){P[j >> 1][0][j & 1], P[j >> 1][1][j & 1], P[j >> 1][2][j & 1], P[j >> 1][3][j & 1]};
      }
    }
    TS(15);
  } else {
    const int wb = w - 4;
    const char* bp[3];
    unsigned vo[3];
    const int e2 = wb < 2 ? 8 + wb : 6 + wb;
#pragma unroll
    for (int v = 0; v < 3; ++v) {
      int n = 8 * (v < 2 ? wb + 4 * v : e2) + l3;
      bp[v] = (const char*)((n & 1) ? Bim : Bre) + (40 * g + (n >> 1)) * 1280 + ((l7 ^ l3) << 4);
      int idx = 64 * (wb + 4 * v) + lane, k = idx / 12, uu = idx - 12 * k;
      vo[v] = k * 3072 + (((uu >> 1) ^ ((k >> 3) & 1)) << 5) + ((uu & 1) << 4);
    }
    const char* sb = (const char*)w1 + 192 * g;
    unsigned dst = lb + OB + wb * 1024;
    const unsigned d2 = (e2 - wb) * 1024;
    auto piece0 = [&](int st, int j) {
      const int v = j >> 1;
      if (j & 1) dma_s0(vo[v], sb + st * 196608, dst + st * SLOT + (OW - OB) + v * 4096);
      else { dma_v0(bp[v], dst + st * SLOT + (v < 2 ? v * 4096 : d2)); bp[v] += 128; }
    };
    auto issue0 = [&](int st) {
#pragma unroll
      for (int j = 0; j < 6; ++j) piece0(st, j);
    };
    issue0(0);
    prload();
    BARRIER_NW();
    issue0(1);
    BARRIER_NW();
    issue0(2);
    unsigned bo[2];
#pragma unroll
    for (int p = 0; p < 2; ++p) bo[p] = xo[p] - 8192 * h + OB + 4096 * a;
    const int r5 = c ^ (2 * a);
    unsigned f5 = lb + OB + 8192 + r5 * 128 + (((4 * kk0 + q) ^ (r5 & 7)) << 4);
    f32x4 acc[4][2], ac5[5], cs[2];
#pragma unroll
    for (int i = 0; i < 4; ++i) { acc[i][0] = (f32x4){0.f, 0.f, 0.f, 0.f}; acc[i][1] = acc[i][0]; ac5[i] = acc[i][0]; }
    ac5[4] = ac5[0]; cs[0] = ac5[0]; cs[1] = ac5[0];
    u32x4 X[2][2][4], W[2][2][2], W5[2];
    auto rd = [&](int r, int k) {
      if (k == 6) { W5[r] = lds128(f5); return; }
      const int p = k / 7, o = k % 7;
      if (o < 2) W[r][p][o] = lds128(bo[p] + 2048 * o);
      else X[r][p][o - 2] = lds128(xo[p] + 2048 * (o - 2));
    };
    auto adv = [&](unsigned d) { xo[0] += d; xo[1] += d; bo[0] += d; bo[1] += d; f5 += d; };
    WAITV(12);
    BARRIER_NW();
    TS(1);
#pragma unroll
    for (int k = 0; k < 13; ++k) rd(0, k);
    adv(SLOT);
    SB();
    piece0(3, 0); piece0(3, 1);
    tab0(); SB();
    piece0(3, 2); piece0(3, 3);
    tab1(); SB();
    piece0(3, 4); piece0(3, 5);
    sb += 4 * 196608;
    LGKM0();
    WAITV(12);
    BARRIER_NW();
    TS(2);
    auto stage = [&](int r, int t) {
#pragma unroll
      for (int sl = 0; sl < 23; ++sl) {
        if (sl < 8) acc[sl >> 1][sl & 1] = mfma(W[r][0][sl & 1], X[r][0][sl >> 1], acc[sl >> 1][sl & 1]);
        else if (sl < 12) ac5[sl - 8] = mfma(W5[r], X[r][0][sl - 8], ac5[sl - 8]);
        else if (sl == 12) ac5[4] = mfma(W5[r], ONES, ac5[4]);
        else if (sl < 21) acc[(sl - 13) >> 1][(sl - 13) & 1] = mfma(W[r][1][(sl - 13) & 1], X[r][1][(sl - 13) >> 1], acc[(sl - 13) >> 1][(sl - 13) & 1]);
        else cs[sl - 21] = mfma(W[r][1][sl - 21], ONES, cs[sl - 21]);
        if (sl < 13) rd(r ^ 1, sl);
        if (sl % 4 == 1 && sl < 23) {
          const int j = sl / 4, v = j >> 1;
          if (j & 1) dma_s(vo[v], sb, dst + (OW - OB) + v * 4096, t, 6);
          else { dma_v(bp[v], dst + (v < 2 ? v * 4096 : d2), t, 6); bp[v] += 128; }
        }
        constexpr int st[24] = {0, 2, 4, 6, 8, 10, 12, 14, 16, 18, 20, 22, 24, 26, 29, 31, 34, 36, 39, 41, 44, 46, 48, 48};
#pragma unroll
        for (int k = st[sl]; k < st[sl + 1]; ++k) {
          if (k < 24) opS(X[r][0][2], sv[0], sv[1], k); else opS(X[r][1][3], sv[2], sv[3], k - 24);
        }
        SB();
      }
    };
    for (int t = 0; t < 10; t += 2) {
      stage(0, t);
      adv((t & 2) ? -3 * SLOT : SLOT);
      dst += SLOT; sb += 196608;
      LGKM0();
      WAITV2(t, 6, 12, 6);
      BARRIER_NW();
      TS(3 + t);
      stage(1, t + 1);
      adv(SLOT);
      dst += (t & 2) ? -3 * SLOT : SLOT; sb += 196608;
      LGKM0();
      WAITV2(t, 6, 12, 0);
      BARRIER_NW();
      TS(4 + t);
    }
    Tab tb[5];
#pragma unroll
    for (int k = 0; k < 4; ++k) rdtab(tb[k], lb + T_TAB + (16 * a + 8 * (k >> 1) + 2 * q + (k & 1)) * 64);
    rdtab(tb[4], lb + T_TAB + (32 + 2 * q + a) * 64);
    put_stats(2);
    if (c == 0) {
      sts128f(lb + E_CS + ((w * 3 + 0) * 4 + q) * 16, cs[0]);
      sts128f(lb + E_CS + ((w * 3 + 1) * 4 + q) * 16, cs[1]);
    }
#pragma unroll
    for (int i = 0; i < 5; ++i) sts64f(lb + E_A5 + (((w - 4) * 5 + i) * 64 + lane) * 8, (f32x2){ac5[i].z, ac5[i].w});
    BARRIER();
    TS(13);
    float m[4], r[4];
    mr4(m, r);
    cs[0] += lds128f(lb + E_CS + (((w ^ 2) * 3 + 0) * 4 + q) * 16);
    cs[1] += lds128f(lb + E_CS + (((w ^ 2) * 3 + 1) * 4 + q) * 16);
    f32x2 u[5][4], csv[5];
#pragma unroll
    for (int i = 0; i < 4; ++i) {
      u[0][i] = (f32x2){acc[i][0].x, acc[i][0].y}; u[1][i] = (f32x2){acc[i][0].z, acc[i][0].w};
      u[2][i] = (f32x2){acc[i][1].x, acc[i][1].y}; u[3][i] = (f32x2){acc[i][1].z, acc[i][1].w};
      u[4][i] = (f32x2){ac5[i].x, ac5[i].y} + lds64f(lb + E_A5 + ((((w - 4) ^ 1) * 5 + i) * 64 + lane) * 8);
    }
    csv[0] = (f32x2){cs[0].x, cs[0].y}; csv[1] = (f32x2){cs[0].z, cs[0].w};
    csv[2] = (f32x2){cs[1].x, cs[1].y}; csv[3] = (f32x2){cs[1].z, cs[1].w};
    csv[4] = (f32x2){ac5[4].x, ac5[4].y} + lds64f(lb + E_A5 + ((((w - 4) ^ 1) * 5 + 4) * 64 + lane) * 8);
#pragma unroll
    for (int i = 0; i < 4; ++i) {
      f32x2 nm = {-m[i], -m[i]}, rr = {r[i], r[i]};
#pragma unroll
      for (int k = 0; k < 5; ++k) u[k][i] = cmulp(tb[k].co, pfma(csv[k], nm, u[k][i]) * rr);
    }
    f32x2 hh[5];
#pragma unroll
    for (int k = 0; k < 5; ++k) hh[k] = u[k][0];
#pragma unroll
    for (int i = 1; i < 4; ++i)
#pragma unroll
      for (int k = 0; k < 5; ++k) hh[k] = cfmap(tb[k].L, hh[k], u[k][i]);
#pragma unroll
    for (int k = 0; k < 5; ++k) hh[k] = cfmap(tb[k].L4, shr2<1>(hh[k]), hh[k]);
#pragma unroll
    for (int k = 0; k < 5; ++k) hh[k] = cfmap(tb[k].L8, shr2<2>(hh[k]), hh[k]);
#pragma unroll
    for (int k = 0; k < 5; ++k) hh[k] = cfmap(tb[k].L16, shr2<4>(hh[k]), hh[k]);
#pragma unroll
    for (int k = 0; k < 5; ++k) hh[k] = cfmap(tb[k].L32, shr2<8>(hh[k]), hh[k]);
    const int ksr = 5 * (g >> 1) + 2 * (g & 1) + a, ks5 = 5 * (g >> 1) + 4;
    f32x2 ag[5];
#pragma unroll
    for (int k = 0; k < 5; ++k) ag[k] = hh[k];
    if (h == 0) {
      if (c == 15) {
        unsigned ta = lb + T_AG + (a * 4 + q) * 48;
        sts128f(ta, (f32x4){ag[0].x, ag[0].y, ag[1].x, ag[1].y});
        sts128f(ta + 16, (f32x4){ag[2].x, ag[2].y, ag[3].x, ag[3].y});
        sts64f(ta + 32, ag[4]);
      }
      WAITL0();
      if (lane == 0) *(volatile AS3 unsigned*)(uintptr_t)(lb + T_FLAG + a * 4) = 1u;
    }
#pragma unroll
    for (int k = 0; k < 5; ++k) hh[k] = shr2<1>(hh[k]);
    __amdgpu_buffer_rsrc_t rs = mkrsrc(ws + WS_HF, 5242880);
    unsigned o5[4];
#pragma unroll
    for (int i = 0; i < 4; ++i) {
      u32x4 o;
#pragma unroll
      for (int k = 0; k < 5; ++k) {
        hh[k] = cfmap(tb[k].L, hh[k], u[k][i]);
        if (k < 4) o[k] = pk2(hh[k].x, hh[k].y); else o5[i] = pk2(hh[k].x, hh[k].y);
      }
      st128(o, rs, lane * 16 + ((rb * 40 + ksr) * 4 + i) * 1024);
    }
    swap32(o5[0], o5[2]); swap32(o5[1], o5[3]);
    swap16(o5[0], o5[1]); swap16(o5[2], o5[3]);
    st128((u32x4){o5[0], o5[1], o5[2], o5[3]}, rs, (c + 16 * (2 * (g & 1) + a)) * 16 + ((rb * 40 + ks5) * 4 + q) * 1024);
    if (h == 1) {
      while (*(volatile AS3 unsigned*)(uintptr_t)(lb + T_FLAG + a * 4) == 0u) __builtin_amdgcn_s_sleep(1);
      asm volatile("" ::: "memory");
      unsigned ta = lb + T_AG + (a * 4 + q) * 48;
      f32x4 l0 = lds128f(ta), l1 = lds128f(ta + 16);
      f32x2 l2 = lds64f(ta + 32);
      ag[0] = cfmap(tb[0].L64, (f32x2){l0.x, l0.y}, ag[0]); ag[1] = cfmap(tb[1].L64, (f32x2){l0.z, l0.w}, ag[1]);
      ag[2] = cfmap(tb[2].L64, (f32x2){l1.x, l1.y}, ag[2]); ag[3] = cfmap(tb[3].L64, (f32x2){l1.z, l1.w}, ag[3]);
      ag[4] = cfmap(tb[4].L64, l2, ag[4]);
    }
    if (c == 15) {
      char* base = ws + (h ? WS_AGG : WS_AGL) + rc * 2560;
      *(u32x4*)(base + (ksr * 16 + 4 * q) * 4) = (u32x4){pk2(ag[0].x, ag[0].y), pk2(ag[1].x, ag[1].y), pk2(ag[2].x, ag[2].y), pk2(ag[3].x, ag[3].y)};
      *(unsigned*)(base + (ks5 * 16 + 8 * (g & 1) + 4 * a + q) * 4) = pk2(ag[4].x, ag[4].y);
    }
    TS(15);
  }
}

constexpr unsigned M_RO = 0;
constexpr unsigned M_RS = 61440;
constexpr unsigned M_T1 = M_RS + 81920;
constexpr unsigned M_CTL = M_T1;
constexpr unsigned K2_LDS = M_CTL + 64;

extern "C" __global__ __launch_bounds__(512, 2) void k2(const u16* x, const u16* Dsk, char* ws, float* out) {
  __shared__ __attribute__((aligned(1024))) char smem[K2_LDS];
  const unsigned lb = (unsigned)(uintptr_t)(AS3 char*)smem;
  const int tid = threadIdx.x, lane = tid & 63;
  const int w = __builtin_amdgcn_readfirstlane(tid >> 6);
  const int c = lane & 15, q = lane >> 4;
  const int b = blockIdx.x, xcd = b & 7, jb = b >> 3;
  const int rb = (xcd >> 1) * 8 + (jb & 7), cg = (xcd & 1) * 4 + (jb >> 3), gh = xcd & 1;
  const int rc = rb >> 1, par = rb & 1;
  const int vo = lane * 16;
  TSDECL;
  TS(0);
  f32x4 acc[4][5];
  if (tid < 4) sts32f(lb + M_CTL + tid * 4, 0.f);
  BARRIER();

  if (w >= 4) {
    const int sw = w - 4;
    __builtin_amdgcn_s_setprio(SPRIO);
    __amdgpu_buffer_rsrc_t rA = mkrsrc(ws + WS_HF + (size_t)(rb * 40) * 4096, 40 * 4096);
    __amdgpu_buffer_rsrc_t rB = mkrsrc(ws + WS_CF + (size_t)(cg * 40) * 5120, 40 * 5120);
    auto kof = [&](int i) { return 5 * sw + (i >> 1) + 20 * ((i & 1) ^ gh); };
    u32x4 A[3][4], B[3][5];
    auto load = [&](int i) {
      int bf = i % 3;
#pragma unroll
      for (int j = 0; j < 9; ++j) {
        const int m = j < 1 ? j : j - 5;
        if (j >= 1 && j < 6) B[bf][j - 1] = __builtin_amdgcn_raw_buffer_load_b128(rB, vo + (j - 1) * 1024, kof(i) * 5120, 0);
        else A[bf][m] = __builtin_amdgcn_raw_buffer_load_b128(rA, vo + m * 1024, kof(i) * 4096, 0);
        SB();
      }
    };
    const unsigned tl = lb + M_RS + sw * 20480;
    {
      const int l40 = lane < 40 ? lane : 39;
      const int sl0 = kof(l40 >> 2) * 16 + 4 * (l40 & 3);
      {
        const unsigned dv = (5 * sw + 20 * ((lane >> 5) ^ gh)) * 512 + (lane & 31) * 16;
#pragma unroll
        for (int u = 0; u < 5; ++u) dma_s0(dv, ws + WS_T2 + u * 512, tl + 5120 + u * 1024);
      }
      __amdgpu_buffer_rsrc_t rG = mkrsrc(ws + WS_T3, 15360);
      f32x4 tv[6];
      u32x4 ag[15], al;
#pragma unroll
      for (int u = 0; u < 6; ++u)
        tv[u] = __builtin_bit_cast(f32x4, __builtin_amdgcn_raw_buffer_load_b128(rG, (sl0 >> 2) * 96 + ((u + 4) % 6) * 16, 0, 0));
#pragma unroll
      for (int m = 0; m < 15; ++m)
        ag[m] = __builtin_amdgcn_raw_buffer_load_b128(mkrsrc(ws + WS_AGG, m < rc ? 40960u : 0u), sl0 * 4, m * 2560, 0);
      al = __builtin_amdgcn_raw_buffer_load_b128(mkrsrc(ws + WS_AGL, par ? 40960u : 0u), sl0 * 4, rc * 2560, 0);
      SB();
      load(0);
      SB();
      f32x2 P[4], gg[4], Ls[4], L64[4];
#pragma unroll
      for (int k = 0; k < 4; ++k) {
        P[k] = (f32x2){tv[k >> 1][2 * (k & 1)], tv[k >> 1][2 * (k & 1) + 1]};
        Ls[k] = (f32x2){tv[2 + (k >> 1)][2 * (k & 1)], tv[2 + (k >> 1)][2 * (k & 1) + 1]};
        L64[k] = (f32x2){tv[4 + (k >> 1)][2 * (k & 1)], tv[4 + (k >> 1)][2 * (k & 1) + 1]};
        gg[k] = (f32x2){0.f, 0.f};
      }
#pragma unroll
      for (int m = 0; m < 15; ++m)
        if (m < rc) {
#pragma unroll
          for (int k = 0; k < 4; ++k) gg[k] = cfmas(P[k], gg[k], (f32x2){bflo(ag[m][k]), bfhi(ag[m][k])});
        }
      if (par) {
#pragma unroll
        for (int k = 0; k < 4; ++k) gg[k] = cfmas(L64[k], gg[k], (f32x2){bflo(al[k]), bfhi(al[k])});
      }
      SB();
      load(1);
      SB();
#pragma unroll
      for (int j = 0; j < 4; ++j) {
        if (j) {
#pragma unroll
          for (int k = 0; k < 4; ++k) gg[k] = cmuls(Ls[k], gg[k]);
        }
        sts128f(tl + l40 * 128 + j * 32, (f32x4){gg[0].x, gg[0].y, gg[1].x, gg[1].y});
        sts128f(tl + l40 * 128 + j * 32 + 16, (f32x4){gg[2].x, gg[2].y, gg[3].x, gg[3].y});
      }
      SB();
      load(2);
      SB();
    }
    WAITV(27);
    TS(1);
#pragma unroll
    for (int m = 0; m < 4; ++m)
#pragma unroll
      for (int n = 0; n < 5; ++n) acc[m][n] = (f32x4){0.f, 0.f, 0.f, 0.f};
    const unsigned tq = tl + q * 128 + (c >> 2) * 32, uq = tl + 5120 + q * 128 + (c & 3) * 32, lq = tl + 5120 + q * 128;
    const unsigned tb = lb + M_RS + sw * 20480 + vo;
    f32x4 tt[4], Lq[2];
    float zf[8], tf[8], e0, e1;
    auto rdt = [&](int i) {
      tt[0] = lds128f(tq + i * 512); tt[1] = lds128f(tq + i * 512 + 16);
      tt[2] = lds128f(uq + i * 512); tt[3] = lds128f(uq + i * 512 + 16);
    };
    auto rdl = [&](int i) { Lq[0] = lds128f(lq + i * 512); Lq[1] = lds128f(lq + i * 512 + 16); };
    auto opA = [&](int k) {
      const int d = k / 4, o = k % 4;
      const float Lx = Lq[d >> 1][2 * (d & 1)], Ly = Lq[d >> 1][2 * (d & 1) + 1];
      if (o == 0) e0 = vmul(Ly, zf[2 * d + 1]);
      else if (o == 1) e1 = vmul(Ly, zf[2 * d]);
      else if (o == 2) zf[2 * d] = vfms(Lx, zf[2 * d], e0);
      else zf[2 * d + 1] = vfma(Lx, zf[2 * d + 1], e1);
    };
    auto opZ = [&](int k) {
      const int d = k / 4, o = k % 4;
      const float gx = tt[d >> 1][2 * (d & 1)], gy = tt[d >> 1][2 * (d & 1) + 1];
      const float ux = tt[2 + (d >> 1)][2 * (d & 1)], uy = tt[2 + (d >> 1)][2 * (d & 1) + 1];
      if (o == 0) e0 = vmul(gy, uy);
      else if (o == 1) e1 = vmul(gy, ux);
      else if (o == 2) zf[2 * d] = vfms(gx, ux, e0);
      else zf[2 * d + 1] = vfma(gx, uy, e1);
    };
    auto opD = [&](int bf, int m, int j) {
      const unsigned av = A[bf][m][j >> 1];
      tf[j] = vadd(zf[j], (j & 1) ? bfhi(av) : bflo(av));
    };
    auto opC = [&](int bf, int m, int d) { A[bf][m][d] = pk2a(tf[2 * d], tf[2 * d + 1]); };
    auto vop = [&](int i, int k) {
      const int bf = i % 3;
      if (k < 100) {
        const int gq = (k + 12) / 28, o = (k + 12) % 28;
        if (o < 12) opA(4 + o);
        else if (o < 20) opD(bf, gq, o - 12);
        else if (o < 24) { if (gq < 3) opA(o - 20); else if (i < 9) opZ(o - 20); }
        else opC(bf, gq, o - 24);
      } else if (i < 9) opZ(k - 96);
    };
    rdt(0); rdl(0);
#pragma unroll
    for (int k = 0; k < 16; ++k) opZ(k);
    SB();
    auto step = [&](auto I) {
      constexpr int i = decltype(I)::v;
      sfor<0, 16>([&](auto K) { vop(i, decltype(K)::v); SB(); });
      sfor<0, 20>([&](auto S) {
        constexpr int sl = decltype(S)::v;
        acc[sl / 5][sl % 5] = mfma(B[i % 3][sl % 5], A[i % 3][sl / 5], acc[sl / 5][sl % 5]);
        constexpr int st[21] = {16, 22, 28, 34, 39, 44, 50, 56, 62, 67, 72, 78, 84, 90, 95, 100, 103, 106, 108, 110, 112};
        SB();
        sfor<st[sl], st[sl + 1]>([&](auto K) { vop(i, decltype(K)::v); SB(); });
        if (i < 9 && sl == 9) rdt(i + 1);
        if (i < 9 && sl == 12) rdl(i + 1);
        if (i == 9 && sl >= 2) sts128f(tb + (sl - 2) * 1024, acc[(sl - 2) / 5][(sl - 2) % 5]);
        SB();
      });
      if (i + 3 < 10) {
        if (SLP != SPRIO) __builtin_amdgcn_s_setprio(SLP);
        load(i + 3);
        SB();
        if (SLP != SPRIO) __builtin_amdgcn_s_setprio(SPRIO);
      }
      TS(2 + i);
    };
    step(IC<0>{}); step(IC<1>{}); step(IC<2>{}); step(IC<3>{}); step(IC<4>{});
    step(IC<5>{}); step(IC<6>{}); step(IC<7>{}); step(IC<8>{}); step(IC<9>{});
    sts128f(tb + 18 * 1024, acc[3][3]);
    sts128f(tb + 19 * 1024, acc[3][4]);
    TS(14);
    BARRIER();
    TS(15);
    TSDUMP(33554432 + 1048576);
    return;
  }

  __builtin_amdgcn_s_setprio(OPRIO);
  __amdgpu_buffer_rsrc_t rA = mkrsrc(ws + WS_H1F + (size_t)rb * 48 * 4096, 48 * 4096);
  __amdgpu_buffer_rsrc_t rB = mkrsrc(ws + WS_W2F + (size_t)cg * 48 * 5120, 48 * 5120);
  u32x4 A[4][4], B[4][5];
  auto loadA = [&](int i) {
    int bf = i & 3, ks = w + 4 * ((i >> 1) + 6 * ((i & 1) ^ gh));
#pragma unroll
    for (int m = 0; m < 4; ++m) { A[bf][m] = __builtin_amdgcn_raw_buffer_load_b128(rA, vo, ks * 4096 + ((w + m) & 3) * 1024, 0); SB(); }
  };
  auto loadB = [&](int i) {
    int bf = i & 3, ks = w + 4 * ((i >> 1) + 6 * ((i & 1) ^ gh));
#pragma unroll
    for (int n = 0; n < 5; ++n) { B[bf][n] = __builtin_amdgcn_raw_buffer_load_b128(rB, vo + n * 1024, ks * 5120, 0); SB(); }
  };
  auto load = [&](int i) { loadA(i); loadB(i); };
  u32x2 xv[5], sk[5];
  f32x2 st;
  load(0);
  load(1);
  load(2);
  TS(1);
#pragma unroll
  for (int m = 0; m < 4; ++m)
#pragma unroll
    for (int n = 0; n < 5; ++n) acc[m][n] = (f32x4){0.f, 0.f, 0.f, 0.f};
  unsigned da[3];
  auto skip = [&](int u) {
    const int n = u >> 1, hf = u & 1;
    unsigned p = pk2a(vsub(bflo(xv[n][hf]), st.x), vsub(bfhi(xv[n][hf]), st.x));
    p = pk2a(vmul(bflo(p), st.y), vmul(bfhi(p), st.y));
    sk[n][hf] = pk2a(vmul(bflo(p), bflo(sk[n][hf])), vmul(bfhi(p), bfhi(sk[n][hf])));
  };
#pragma unroll
  for (int i = 0; i < 12; ++i) {
    if (OTP != OPRIO && i == OTI) __builtin_amdgcn_s_setprio(OTP);
    if (i == 9) {
      int l2 = lane;
      asm volatile("" : "+v"(l2));
      const int trow = 64 * rb + 4 * (l2 & 15) + w;
      const int eo = trow * 640 + 80 * cg + 4 * (l2 >> 4);
      __amdgpu_buffer_rsrc_t rx = mkrsrc(x, 2621440), rd = mkrsrc(Dsk, 1280), rt = mkrsrc(ws + WS_ST, 16384);
      st = __builtin_bit_cast(f32x2, __builtin_amdgcn_raw_buffer_load_b64(rt, trow * 8, 0, 0));
#pragma unroll
      for (int n = 0; n < 5; ++n) {
        xv[n] = __builtin_amdgcn_raw_buffer_load_b64(rx, eo * 2 + n * 32, 0, 0);
        sk[n] = __builtin_amdgcn_raw_buffer_load_b64(rd, (l2 >> 4) * 8 + n * 32, 160 * cg, 0);
      }
      SB();
    }
    if (i + 3 < 12) {
      if (OLP != OPRIO && i >= OLI && (OTP == OPRIO || i < OTI)) __builtin_amdgcn_s_setprio(OLP);
      load(i + 3);
      SB();
      if (OLP != OPRIO && i >= OLI && (OTP == OPRIO || i < OTI)) __builtin_amdgcn_s_setprio(OPRIO);
    }
    SB();
    if (i == 10) {
#pragma unroll
      for (int sl = 0; sl < 20; ++sl) {
        acc[sl / 5][sl % 5] = mfma(B[i & 3][sl % 5], A[i & 3][sl / 5], acc[sl / 5][sl % 5]);
        SB();
        if (sl % 4 == 3) skip(sl / 4);
        SB();
      }
    } else if (i < 11) {
#pragma unroll
      for (int m = 0; m < 4; ++m)
#pragma unroll
        for (int n = 0; n < 5; ++n) acc[m][n] = mfma(B[i & 3][n], A[i & 3][m], acc[m][n]);
    } else {
      int v2 = vo;
      asm volatile("" : "+v"(v2));
#pragma unroll
      for (int j = 1; j < 4; ++j) da[j - 1] = lb + M_RO + (((w + j) & 3) * 3 + (j - 1)) * 5120 + v2;
#pragma unroll
      for (int sl = 0; sl < 20; ++sl) {
        const int m = (sl / 5 + 1) & 3, n = sl % 5;
        acc[m][n] = mfma(B[i & 3][n], A[i & 3][m], acc[m][n]);
        if (sl >= 2 && sl < 17) sts128f(da[(sl - 2) / 5] + ((sl - 2) % 5) * 1024, acc[(sl - 2) / 5 + 1][(sl - 2) % 5]);
        SB();
        if (sl % 4 == 0) skip(5 + sl / 4);
        SB();
      }
    }
    SB();
    TS(2 + i);
  }
  WAITL0();
  if (lane == 0) *(volatile AS3 unsigned*)(uintptr_t)(lb + M_CTL + w * 4) = 1u;
  for (;;) {
    u32x4 f = *(volatile AS3 u32x4*)(uintptr_t)(lb + M_CTL);
    if ((f.x & f.y & f.z & f.w) != 0u) break;
    __builtin_amdgcn_s_sleep(1);
  }
  asm volatile("" ::: "memory");
  TS(14);
  f32x4 xs[5], g2[5];
  {
    f32x4 po[5][3];
#pragma unroll
    for (int n = 0; n < 5; ++n)
#pragma unroll
      for (int j = 0; j < 3; ++j) po[n][j] = lds128f(lb + M_RO + ((w * 3 + j) * 5 + n) * 1024 + vo);
    SB();
#pragma unroll
    for (int n = 0; n < 5; ++n) {
      f32x4 o;
#pragma unroll
      for (int j = 0; j < 4; ++j) o[j] = vadd(vadd(acc[0][n][j], po[n][0][j]), vadd(po[n][1][j], po[n][2][j]));
#pragma unroll
      for (int hf = 0; hf < 2; ++hf) {
        unsigned p = pk2a(o[2 * hf], o[2 * hf + 1]);
        float v0 = bflo(p), v1 = bfhi(p);
        float e0 = __builtin_amdgcn_exp2f(vmul(v0, -1.44269504088896f)), e1 = __builtin_amdgcn_exp2f(vmul(v1, -1.44269504088896f));
        e0 = __builtin_amdgcn_rcpf(vadd(e0, 1.f)); e1 = __builtin_amdgcn_rcpf(vadd(e1, 1.f));
        p = pk2a(vmul(v0, e0), vmul(v1, e1));
        v0 = bflo(p); v1 = bfhi(p);
        xs[n][2 * hf] = vfma(v0, bflo(sk[n][hf]), bflo(xv[n][hf]));
        xs[n][2 * hf + 1] = vfma(v1, bfhi(sk[n][hf]), bfhi(xv[n][hf]));
        g2[n][2 * hf] = v0; g2[n][2 * hf + 1] = v1;
      }
    }
#pragma unroll
    for (int n = 0; n < 5; ++n) asm volatile("" : "+v"(xs[n]), "+v"(g2[n]));
  }
  __amdgpu_buffer_rsrc_t ro = mkrsrc(out, 5242880);
  int l2 = lane;
  asm volatile("" : "+v"(l2));
  const int eo2 = ((64 * rb + 4 * (l2 & 15) + w) * 640 + 80 * cg + 4 * (l2 >> 4)) * 4;
  TS(15);
  BARRIER_NW();
  TS(16);
  f32x4 ps[5][4];
#pragma unroll
  for (int n = 0; n < 5; ++n)
#pragma unroll
    for (int k = 0; k < 4; ++k) ps[n][k] = lds128f(lb + M_RS + (w * 5 + n) * 1024 + k * 20480 + vo);
  SB();
#pragma unroll
  for (int n = 0; n < 5; ++n) {
    f32x4 s = (ps[n][0] + ps[n][1]) + (ps[n][2] + ps[n][3]);
    f32x4 r = __builtin_elementwise_fma(s, g2[n], xs[n]);
    __builtin_amdgcn_raw_buffer_store_b128(__builtin_bit_cast(u32x4, r), ro, eo2 + n * 64, 0, 0);
    SB();
  }
}

extern "C" __device__ __attribute__((used, noinline)) void kend() {}

extern "C" void kernel_launch(void* const* d_in, const int* in_sizes, int n_in, void* d_out, int out_size, void* d_ws,
                              size_t ws_size, hipStream_t stream) {
  const u16* x = (const u16*)d_in[0];
  k1<<<256, 512, 0, stream>>>(x, (const u16*)d_in[1], (const u16*)d_in[2], (const u16*)d_in[3], (const u16*)d_in[4],
                              (const u16*)d_in[5], (const u16*)d_in[6], (const u16*)d_in[7], (const u16*)d_in[10],
                              (const u16*)d_in[11], (char*)d_ws);
  k2<<<256, 512, 0, stream>>>(x, (const u16*)d_in[8], (char*)d_ws, (float*)d_out);
}
```

```cpp
#include <hip/hip_runtime.h>
#include <stdint.h>

typedef unsigned short u16;
typedef __attribute__((ext_vector_type(8))) __bf16 bf16x8;
typedef __attribute__((ext_vector_type(2))) __bf16 bf16x2;
typedef __attribute__((ext_vector_type(4))) float f32x4;
typedef __attribute__((ext_vector_type(2))) float f32x2;
typedef __attribute__((ext_vector_type(4))) unsigned u32x4;
typedef __attribute__((ext_vector_type(2))) unsigned u32x2;
typedef __attribute__((ext_vector_type(4))) short s16x4;
#define AS3 __attribute__((address_space(3)))
#define DEVI __device__ __forceinline__

constexpr size_t WS_H1F = 0;
constexpr size_t WS_HF = 6291456;
constexpr size_t WS_W2F = 11534336;
constexpr size_t WS_CF = 13500416;
constexpr size_t WS_AGG = 15138816;
constexpr size_t WS_AGL = 15220736;
constexpr size_t WS_TAB = 15302656;
constexpr size_t WS_T2 = 15339520;
constexpr size_t WS_T3 = 15360000;
constexpr size_t WS_ST = 15323136;

DEVI float bf2f(unsigned v) { return __uint_as_float(v << 16); }
DEVI float bflo(unsigned v) { return __uint_as_float(v << 16); }
DEVI float bfhi(unsigned v) { return __uint_as_float(v & 0xffff0000u); }
DEVI unsigned pk2(float a, float b) {
  bf16x2 r = {(__bf16)a, (__bf16)b};
  return __builtin_bit_cast(unsigned, r);
}
DEVI unsigned pk2a(float a, float b) { unsigned r; asm("v_cvt_pk_bf16_f32 %0, %1, %2" : "=v"(r) : "v"(a), "v"(b)); return r; }
DEVI float rbf(float a) { return bflo(pk2(a, 0.f)); }
template <int N> DEVI float shr(float v) {
  return __builtin_bit_cast(float, __builtin_amdgcn_update_dpp(0, __builtin_bit_cast(int, v), 0x110 + N, 0xf, 0xf, true));
}
template <int N> DEVI f32x2 shr2(f32x2 v) { f32x2 r; r.x = shr<N>(v.x); r.y = shr<N>(v.y); return r; }
DEVI f32x2 cpow(float a, float b, float n) {
  float mg = __builtin_amdgcn_exp2f(n * a);
  float rv = __builtin_amdgcn_fractf(n * b);
  f32x2 r;
  r.x = mg * __builtin_amdgcn_cosf(rv);
  r.y = mg * __builtin_amdgcn_sinf(rv);
  return r;
}
DEVI f32x2 cmul(f32x2 a, f32x2 b) { f32x2 r; r.x = a.x * b.x - a.y * b.y; r.y = a.x * b.y + a.y * b.x; return r; }
DEVI f32x2 pfma(f32x2 a, f32x2 b, f32x2 c) { return __builtin_elementwise_fma(a, b, c); }
DEVI f32x2 cfmap(f32x2 a, f32x2 b, f32x2 c) {
  f32x2 ax = {a.x, a.x}, ay = {-a.y, a.y}, bs = {b.y, b.x};
  return pfma(ay, bs, pfma(ax, b, c));
}
DEVI f32x2 cmulp(f32x2 a, f32x2 b) {
  f32x2 ax = {a.x, a.x}, ay = {-a.y, a.y}, bs = {b.y, b.x};
  return pfma(ay, bs, ax * b);
}
extern "C" __device__ __attribute__((const)) float mul_legacy_f32(float, float) __asm("llvm.amdgcn.fmul.legacy");
DEVI float vmul(float a, float b) { return mul_legacy_f32(a, b); }
DEVI float vfma(float a, float b, float c) { float r; asm("v_fma_f32 %0, %1, %2, %3" : "=v"(r) : "v"(a), "v"(b), "v"(c)); return r; }
DEVI float vadd(float a, float b) { float r; asm("v_add_f32 %0, %1, %2" : "=v"(r) : "v"(a), "v"(b)); return r; }
DEVI float vsub(float a, float b) { float r; asm("v_sub_f32 %0, %1, %2" : "=v"(r) : "v"(a), "v"(b)); return r; }
DEVI float vfms(float a, float b, float c) { float r; asm("v_fma_f32 %0, %1, %2, -%3" : "=v"(r) : "v"(a), "v"(b), "v"(c)); return r; }
DEVI float vfnm(float a, float b, float c) { float r; asm("v_fma_f32 %0, -%1, %2, %3" : "=v"(r) : "v"(a), "v"(b), "v"(c)); return r; }
DEVI f32x2 cfmas(f32x2 a, f32x2 b, f32x2 c) {
  f32x2 r;
  r.x = vfnm(a.y, b.y, vfma(a.x, b.x, c.x));
  r.y = vfma(a.y, b.x, vfma(a.x, b.y, c.y));
  return r;
}
DEVI f32x2 cmuls(f32x2 a, f32x2 b) {
  f32x2 r;
  r.x = vfnm(a.y, b.y, vmul(a.x, b.x));
  r.y = vfma(a.y, b.x, vmul(a.x, b.y));
  return r;
}
DEVI f32x2 cpows(float a, float b, float n) {
  float mg = __builtin_amdgcn_exp2f(vmul(n, a));
  float rv = __builtin_amdgcn_fractf(vmul(n, b));
  f32x2 r;
  r.x = vmul(mg, __builtin_amdgcn_cosf(rv));
  r.y = vmul(mg, __builtin_amdgcn_sinf(rv));
  return r;
}
DEVI float siluf(float a) { return a * __builtin_amdgcn_rcpf(1.f + __builtin_amdgcn_exp2f(-1.44269504088896f * a)); }
DEVI void swap16(unsigned& a, unsigned& b) { auto r = __builtin_amdgcn_permlane16_swap(a, b, false, false); a = r[0]; b = r[1]; }
DEVI void swap32(unsigned& a, unsigned& b) { auto r = __builtin_amdgcn_permlane32_swap(a, b, false, false); a = r[0]; b = r[1]; }
DEVI void swap16f(float& a, float& b) { unsigned x = __float_as_uint(a), y = __float_as_uint(b); swap16(x, y); a = __uint_as_float(x); b = __uint_as_float(y); }
DEVI void swap32f(float& a, float& b) { unsigned x = __float_as_uint(a), y = __float_as_uint(b); swap32(x, y); a = __uint_as_float(x); b = __uint_as_float(y); }

template <int N> struct IC { static constexpr int v = N; };
template <int A, int B, class F> __device__ __forceinline__ void sfor(F&& f) { if constexpr (A < B) { f(IC<A>{}); sfor<A + 1, B>(f); } }
#define WAITV(n) asm volatile("s_waitcnt vmcnt(%0)" ::"n"(n) : "memory")
#define WAITL0() asm volatile("s_waitcnt lgkmcnt(0)" ::: "memory")
#define BARRIER() do { WAITL0(); __builtin_amdgcn_s_barrier(); asm volatile("" ::: "memory"); } while (0)
#define BARRIER_NW() do { asm volatile("" ::: "memory"); __builtin_amdgcn_s_barrier(); asm volatile("" ::: "memory"); } while (0)
#define SB() __builtin_amdgcn_sched_barrier(0)
DEVI void glds(const void* p, unsigned ldsaddr) {
  unsigned keep;
  asm volatile("s_mov_b32 %0, m0\n\ts_mov_b32 m0, %2\n\ts_nop 0\n\tglobal_load_lds_dwordx4 %1, off\n\ts_mov_b32 m0, %0"
               : "=&s"(keep) : "v"(p), "s"(ldsaddr) : "memory");
}

DEVI u32x4 lds128(unsigned off) { return *(const AS3 u32x4*)(uintptr_t)off; }
DEVI f32x4 lds128f(unsigned off) { return *(const AS3 f32x4*)(uintptr_t)off; }
DEVI f32x2 lds64f(unsigned off) { return *(const AS3 f32x2*)(uintptr_t)off; }
DEVI void sts128f(unsigned off, f32x4 v) { *(AS3 f32x4*)(uintptr_t)off = v; }
DEVI void sts64f(unsigned off, f32x2 v) { *(AS3 f32x2*)(uintptr_t)off = v; }
DEVI void sts32f(unsigned off, float v) { *(AS3 float*)(uintptr_t)off = v; }
DEVI u32x2 ldstr(unsigned off) {
  s16x4 r = __builtin_amdgcn_ds_read_tr16_b64_v4i16((AS3 s16x4*)(uintptr_t)off);
  return __builtin_bit_cast(u32x2, r);
}
DEVI u32x4 ldstr2(unsigned a, unsigned d) { u32x2 lo = ldstr(a), hi = ldstr(a + d); return (u32x4){lo.x, lo.y, hi.x, hi.y}; }
DEVI f32x4 mfma(u32x4 a, u32x4 b, f32x4 c) {
  return __builtin_amdgcn_mfma_f32_16x16x32_bf16(__builtin_bit_cast(bf16x8, a), __builtin_bit_cast(bf16x8, b), c, 0, 0, 0);
}
#define STAUX 16
#define PRO 2
#define SPRIO 2
#define OPRIO 0
#define OLP OPRIO
#define SLP SPRIO
#define OLI 0
#define OTP 3
#define OTI 10
DEVI void st128(u32x4 v, __amdgpu_buffer_rsrc_t rs, int off) { __builtin_amdgcn_raw_buffer_store_b128(v, rs, off, 0, STAUX); }
DEVI __amdgpu_buffer_rsrc_t mkrsrc(const void* p, unsigned bytes) {
  return __builtin_amdgcn_make_buffer_rsrc((void*)p, 0, bytes, 0x00020000);
}
#define TSDECL
#define TS(k)
#define TSDUMP(base)

constexpr unsigned SLOT = 38912, OB = 16384, OW = 26624;
constexpr unsigned SPARE = 4 * SLOT;
constexpr unsigned T_TAB = SPARE;
constexpr unsigned T_AG = SPARE + 2560;
constexpr unsigned T_FLAG = SPARE + 3072;
constexpr unsigned T_PF = SPARE + 4096;
constexpr unsigned K1_LDS = SPARE + 5120;
constexpr unsigned S_W2 = 2 * SLOT;
constexpr unsigned E_ST = 3 * SLOT;
constexpr unsigned E_CS = E_ST + 2048;
constexpr unsigned E_A5 = E_CS + 2048;

DEVI void stat(u32x4 f, float& s, float& q) {
#pragma unroll
  for (int r = 0; r < 4; ++r) {
    float lo = bflo(f[r]), hi = bfhi(f[r]);
    s += lo; q = fmaf(lo, lo, q);
    s += hi; q = fmaf(hi, hi, q);
  }
}
template <int NM, int NV> DEVI void ileave() {
#pragma unroll
  for (int i = 0; i < NM; ++i) {
    __builtin_amdgcn_sched_group_barrier(0x008, 1, 0);
    __builtin_amdgcn_sched_group_barrier(0x002, NV, 0);
  }
}
struct Tab { f32x2 L, L4, L8, L16, L32, L64, co; };
DEVI void rdtab(Tab& t, unsigned a) {
  f32x4 v0 = lds128f(a), v1 = lds128f(a + 16), v2 = lds128f(a + 32);
  f32x2 v3 = lds64f(a + 48);
  t.L = (f32x2){v0.x, v0.y}; t.L4 = (f32x2){v0.z, v0.w}; t.L8 = (f32x2){v1.x, v1.y}; t.L16 = (f32x2){v1.z, v1.w};
  t.L32 = (f32x2){v2.x, v2.y}; t.L64 = (f32x2){v2.z, v2.w}; t.co = v3;
}

DEVI void dma_s(unsigned voff, const char* sbase, unsigned ldsaddr, int t, int lim) {
  asm volatile("s_cmp_lt_i32 %3, %4\n\ts_cbranch_scc0 .Ldma%=\n\ts_mov_b32 m0, %2\n\ts_nop 0\n\tglobal_load_lds_dwordx4 %0, %1\n.Ldma%=:"
               ::"v"(voff), "s"(sbase), "s"(ldsaddr), "s"(t), "n"(lim) : "memory", "scc");
}
DEVI void dma_v(const char* p, unsigned ldsaddr, int t, int lim) {
  asm volatile("s_cmp_lt_i32 %2, %3\n\ts_cbranch_scc0 .Ldmb%=\n\ts_mov_b32 m0, %1\n\ts_nop 0\n\tglobal_load_lds_dwordx4 %0, off\n.Ldmb%=:"
               ::"v"(p), "s"(ldsaddr), "s"(t), "n"(lim) : "memory", "scc");
}
DEVI void dma_s0(unsigned voff, const char* sbase, unsigned ldsaddr) {
  asm volatile("s_mov_b32 m0, %2\n\ts_nop 0\n\tglobal_load_lds_dwordx4 %0, %1" ::"v"(voff), "s"(sbase), "s"(ldsaddr) : "memory");
}
DEVI void dma_v0(const char* p, unsigned ldsaddr) {
  asm volatile("s_mov_b32 m0, %1\n\ts_nop 0\n\tglobal_load_lds_dwordx4 %0, off" ::"v"(p), "s"(ldsaddr) : "memory");
}
DEVI const char* code_base(unsigned& len, unsigned& off2) {
  uint64_t pc;
  unsigned d;
  asm volatile("s_getpc_b64 %0\n.Lpc%=:\n\ts_mov_b32 %1, .Lpc%=-k1\n\ts_mov_b32 %2, kend-k1\n\ts_mov_b32 %3, k2-k1"
               : "=s"(pc), "=s"(d), "=s"(len), "=s"(off2));
  return (const char*)(pc - d);
}
#define WAITV2(t, lim, A, B) asm volatile("s_cmp_lt_i32 %0, %1\n\ts_cbranch_scc1 .Lwv%=\n\ts_waitcnt vmcnt(%3)\n.Lwv%=:\n\ts_waitcnt vmcnt(%2)" ::"s"(t), "n"(lim), "n"(A), "n"(B) : "memory", "scc")
#define LGKM0() __builtin_amdgcn_s_waitcnt(0xc07f)

extern "C" __global__ __launch_bounds__(512, 2) void k1(const u16* x, const u16* Lre, const u16* Lim, const u16* Bre, const u16* Bim,
                                             const u16* Cre, const u16* Cim, const u16* ldt, const u16* w1, const u16* w2,
                                             char* ws, const char* outp) {
  __shared__ __attribute__((aligned(1024))) char smem[K1_LDS];
  const unsigned lb = (unsigned)(uintptr_t)(AS3 char*)smem;
  const int b = blockIdx.x, xcd = b & 7, jb = b >> 3;
  const int rc = (xcd >> 1) * 4 + (jb & 3), g = (xcd & 1) * 8 + (jb >> 2);
  const int tid = threadIdx.x, lane = tid & 63;
  const int w = __builtin_amdgcn_readfirstlane(tid >> 6);
  const int c = lane & 15, q = lane >> 4;
  const int l3 = lane >> 3, l7 = lane & 7;
  const int h = (w >> 1) & 1, a = w & 1;
  const int kk0 = h ^ a;
  TSDECL;
  TS(0);

  const u32x4 ONES = {0x3f803f80u, 0x3f803f80u, 0x3f803f80u, 0x3f803f80u};
  unsigned xo[2];
#pragma unroll
  for (int p = 0; p < 2; ++p) xo[p] = lb + 8192 * h + c * 128 + (((4 * (kk0 ^ p) + q) ^ (c & 7)) << 4);
  float sv[4] = {0.f, 0.f, 0.f, 0.f};
  float t0, t1;
  const int rb = 2 * rc + h;
  unsigned pr[3];
  auto prload = [&]() {
    int p = 40 * g + (lane < 40 ? lane : 0);
    const u16* a0 = Lre + p; const u16* a1 = Lim + p; const u16* a2 = ldt + p;
    asm volatile("global_load_ushort %0, %1, off" : "=v"(pr[0]) : "v"(a0) : "memory");
    asm volatile("global_load_ushort %0, %1, off" : "=v"(pr[1]) : "v"(a1) : "memory");
    asm volatile("global_load_ushort %0, %1, off" : "=v"(pr[2]) : "v"(a2) : "memory");
  };
  float lr, li, ea, eb;
  auto tab0 = [&]() {
    asm volatile("" : "+v"(pr[0]), "+v"(pr[1]), "+v"(pr[2]));
    lr = bf2f(pr[0]); li = bf2f(pr[1]);
    float dt = rbf(expf(bf2f(pr[2])));
    ea = dt * lr * 1.44269504088896f; eb = dt * li * 0.15915494309189535f;
  };
  auto tab1 = [&]() {
    const float n = (w == 0 || w > 5) ? 1.f : (float)(2 << w);
    f32x2 P = cpow(ea, eb, n);
    if (w == 6) {
      float nr = P.x - 1.f, ni = P.y, dd = 2.f / (lr * lr + li * li);
      P = (f32x2){(nr * lr + ni * li) * dd, (ni * lr - nr * li) * dd};
    }
    if (w == 7) {
      P = (f32x2){ea, eb};
      if (lane >= 62) sts32f(lb + T_FLAG + (lane - 62) * 4, 0.f);
    }
    if (lane < 40) sts64f(lb + T_TAB + lane * 64 + w * 8, P);
  };
  auto opS = [&](const u32x4& f, float& s, float& qq, int k) {
    const int r = k / 6, o = k % 6;
    if (o == 0) t0 = bflo(f[r]);
    else if (o == 1) t1 = bfhi(f[r]);
    else if (o == 2) s = vadd(s, t0);
    else if (o == 3) qq = vfma(t0, t0, qq);
    else if (o == 4) s = vadd(s, t1);
    else qq = vfma(t1, t1, qq);
  };

  auto mr4 = [&](float* m, float* r) {
#pragma unroll
    for (int i = 0; i < 4; ++i) {
      f32x4 v = lds128f(lb + E_ST + (64 * h + 4 * c + i) * 16);
      float mean = (v.x + v.z) * (1.f / 640.f);
      float var = fmaxf((v.y + v.w) * (1.f / 640.f) - mean * mean, 0.f);
      float den = rbf(__builtin_amdgcn_sqrtf(rbf(rbf(var) + 1e-5f)));
      m[i] = rbf(mean); r[i] = __builtin_amdgcn_rcpf(den);
    }
  };
  auto put_stats = [&](int i0) {
    swap32f(sv[0], sv[2]); swap32f(sv[1], sv[3]);
    float w0 = sv[0] + sv[2], w1v = sv[1] + sv[3];
    swap16f(w0, w1v);
    sts32f(lb + E_ST + ((64 * h + 4 * c + i0 + (q >> 1)) * 4 + 2 * a + (q & 1)) * 4, w0 + w1v);
  };

  if (w < 4) {
    unsigned vo[4];
#pragma unroll
    for (int v = 0; v < 4; ++v) {
      int rho = 8 * (w + 4 * v) + l3;
      vo[v] = (64 * (rho >> 6) + 4 * (rho & 15) + ((rho >> 4) & 3)) * 1280 + ((l7 ^ l3) << 4);
    }
    const char* sb0 = (const char*)x + rc * 163840;
    const char* sb1 = sb0;
    unsigned dst = lb + w * 1024, dstep = 4096;
    auto issue0 = [&](int st) {
#pragma unroll
      for (int v = 0; v < 4; ++v) dma_s0(vo[v], sb0 + st * 128, dst + st * SLOT + v * 4096);
    };
    issue0(0);
    prload();
    BARRIER_NW();
    issue0(1);
    BARRIER_NW();
    issue0(2);
    {
      unsigned len, off2;
      const char* cb = code_base(len, off2);
      int lm = ((int)len - 1024) & ~15;
      lm = lm > 0 ? lm : 0;
      int po = (4 * jb + w) * 1024;
      po = (po < lm ? po : lm) + lane * 16;
      dma_s0(po, cb, lb + T_PF);
      const unsigned tp = (unsigned)((jb * 2 + (w & 1)) * 8 + (lane & 7));
      dma_s0(tp * (w < 2 ? 32768u : 10240u), w < 2 ? (const char*)ws : outp, lb + T_PF);
    }
    unsigned wo[2][3];
#pragma unroll
    for (int u = 0; u < 3; ++u) {
      int tl = a ? (u == 2 ? 3 : 4 + u) : u;
      wo[0][u] = lb + OW + (8 * q + (c >> 2)) * 192 + (c & 3) * 8 + ((tl ^ (q & 1)) << 5) + kk0 * 6144;
      wo[1][u] = wo[0][u] + 6144 - 2 * kk0 * 6144;
    }
    f32x4 acc[4][3], cs[3];
#pragma unroll
    for (int u = 0; u < 3; ++u) {
      cs[u] = (f32x4){0.f, 0.f, 0.f, 0.f};
#pragma unroll
      for (int i = 0; i < 4; ++i) acc[i][u] = cs[u];
    }
    u32x4 X[2][2][4], W[2][2][3];
    auto rd = [&](int r, int k) {
      const int p = k / 10, o = k % 10;
      if (o < 6) {
        u32x2 v = ldstr(wo[p][o >> 1] + (o & 1) * 768);
        W[r][p][o >> 1][2 * (o & 1)] = v.x; W[r][p][o >> 1][2 * (o & 1) + 1] = v.y;
      } else X[r][p][o - 6] = lds128(xo[p] + 2048 * (o - 6));
    };
    auto adv = [&](unsigned d) {
#pragma unroll
      for (int p = 0; p < 2; ++p) {
        xo[p] += d;
#pragma unroll
        for (int u = 0; u < 3; ++u) wo[p][u] += d;
      }
    };
    int unit = 2 * jb + (w & 1);
    if (w < 2) { if (unit >= 60) unit -= 4; } else { if (unit >= 50) unit -= 14; }
    const int u5 = unit / 5;
    const int uks = (w < 2 ? 12 : 10) * (xcd >> 1) + u5, udb = 5 * (xcd & 1) + (unit - 5 * u5);

    WAITV(10);
    BARRIER_NW();
    TS(1);
#pragma unroll
    for (int k = 0; k < 20; ++k) rd(0, k);
    adv(SLOT);
    SB();
    dma_s0(vo[0], sb0 + 384, dst + 3 * SLOT);
    tab0(); SB();
    dma_s0(vo[1], sb0 + 384, dst + 3 * SLOT + 4096);
    tab1(); SB();
    dma_s0(vo[2], sb0 + 384, dst + 3 * SLOT + 8192);
    dma_s0(vo[3], sb0 + 384, dst + 3 * SLOT + 12288);
    sb0 += 512; sb1 = sb0;
    LGKM0();
    WAITV(10);
    BARRIER_NW();
    TS(2);
    auto stage = [&](int r, int t) {
#pragma unroll
      for (int sl = 0; sl < 27; ++sl) {
        if (sl < 24) {
          const int p = sl / 12, i = (sl % 12) / 3, u = sl % 3;
          acc[i][u] = mfma(W[r][p][u], X[r][p][i], acc[i][u]);
        } else cs[sl - 24] = mfma(W[r][1][sl - 24], ONES, cs[sl - 24]);
        if (sl < 20) rd(r ^ 1, sl);
        if (sl % 6 == 2 && sl < 24) dma_s(vo[sl / 6], sl < 12 ? sb0 : sb1, dst + (sl / 6) * dstep, t, 7);
        if (sl < 12) { opS(X[r][0][0], sv[0], sv[1], 2 * sl); opS(X[r][0][0], sv[0], sv[1], 2 * sl + 1); }
        else if (sl < 24) { opS(X[r][1][1], sv[2], sv[3], 2 * sl - 24); opS(X[r][1][1], sv[2], sv[3], 2 * sl - 23); }
        SB();
      }
    };
    for (int t = 0; t < 10; t += 2) {
      if (t == 6) {
        dst = lb + S_W2 + w * 4096; dstep = 1024;
        if (w < 2) {
          sb0 = (const char*)w2 + uks * 40960 + udb * 128; sb1 = sb0;
#pragma unroll
          for (int v = 0; v < 4; ++v) {
            int r = 8 * v + l3;
            vo[v] = r * 1280 + ((l7 ^ (((r >> 1) & 3) << 1)) << 4);
          }
        } else {
          int G = uks / 5, kr = uks - 5 * G;
          int p0 = 80 * G + (kr < 4 ? 40 * (kr >> 1) + 16 * (kr & 1) : 32);
          sb0 = (const char*)Cre + udb * 81920 + p0 * 2; sb1 = (const char*)Cim + udb * 81920 + p0 * 2;
          vo[0] = lane * 1280; vo[1] = vo[0] + (kr < 4 ? 16 : 80); vo[2] = vo[0]; vo[3] = vo[1];
        }
      }
      stage(0, t);
      adv((t & 2) ? -3 * SLOT : SLOT);
      dst += SLOT; sb0 += 128; sb1 += 128;
      LGKM0();
      WAITV2(t, 8, 8, 0);
      BARRIER_NW();
      TS(3 + t);
      stage(1, t + 1);
      adv(SLOT);
      dst += (t & 2) ? -3 * SLOT : SLOT; sb0 += 128; sb1 += 128;
      LGKM0();
      WAITV2(t, 6, 8, 4);
      BARRIER_NW();
      TS(4 + t);
    }
    put_stats(0);
    if (c == 0) {
#pragma unroll
      for (int u = 0; u < 3; ++u) sts128f(lb + E_CS + ((w * 3 + u) * 4 + q) * 16, cs[u]);
    }
    BARRIER();
    TS(13);
    float m[4], r[4];
    mr4(m, r);
#pragma unroll
    for (int u = 0; u < 3; ++u) cs[u] += lds128f(lb + E_CS + (((w ^ 2) * 3 + u) * 4 + q) * 16);
    if (g == 0 && a == 0 && q == 0) {
      f32x4* st = (f32x4*)(ws + WS_ST + (64 * rb + 4 * c) * 8);
      st[0] = (f32x4){m[0], r[0], m[1], r[1]};
      st[1] = (f32x4){m[2], r[2], m[3], r[3]};
    }
    __amdgpu_buffer_rsrc_t rs = mkrsrc(ws + WS_H1F, 6291456);
    unsigned o[4][6];
#pragma unroll
    for (int i = 0; i < 4; ++i) {
      f32x2 nm = {-m[i], -m[i]}, rr = {r[i], r[i]}, re = {-1.44269504088896f * r[i], -1.44269504088896f * r[i]};
#pragma unroll
      for (int u = 0; u < 3; ++u)
#pragma unroll
        for (int hf = 0; hf < 2; ++hf) {
          f32x2 v = pfma((f32x2){cs[u][2 * hf], cs[u][2 * hf + 1]}, nm, (f32x2){acc[i][u][2 * hf], acc[i][u][2 * hf + 1]});
          f32x2 e = v * re;
          e.x = __builtin_amdgcn_exp2f(e.x); e.y = __builtin_amdgcn_exp2f(e.y);
          e = e + (f32x2){1.f, 1.f};
          e.x = __builtin_amdgcn_rcpf(e.x); e.y = __builtin_amdgcn_rcpf(e.y);
          v = (v * rr) * e;
          o[i][2 * u + hf] = pk2(v.x, v.y);
        }
      st128((u32x4){o[i][0], o[i][1], o[i][2], o[i][3]}, rs, lane * 16 + ((rb * 48 + 3 * g + 2 * a) * 4 + i) * 1024);
      if (i & 1) {
        swap16(o[i - 1][4], o[i][4]); swap16(o[i - 1][5], o[i][5]);
        st128((u32x4){o[i - 1][4], o[i - 1][5], o[i][4], o[i][5]}, rs,
              (c + 16 * (2 * a + (q >> 1))) * 16 + ((rb * 48 + 3 * g + 1) * 4 + (i - 1) + (q & 1)) * 1024);
      }
    }
    TS(14);
    SB();
    if (w < 2) {
      const bool nat = (uks % 3) == 1;
      int r0 = (nat ? 8 * q : 4 * q) + (c >> 2);
      int dr = nat ? 4 : 16;
      unsigned a0 = lb + S_W2 + w * 4096 + r0 * 128 + (c & 3) * 8;
      int f0 = (r0 >> 1) & 3, f1 = ((r0 + dr) >> 1) & 3;
#pragma unroll
      for (int j = 0; j < 4; ++j) {
        u32x2 lo = ldstr(a0 + ((j ^ f0) << 5));
        u32x2 hi = ldstr(a0 + dr * 128 + ((j ^ f1) << 5));
        int Dt = 4 * udb + j, cg = Dt / 5, nt = Dt - 5 * cg;
        *(u32x4*)(ws + WS_W2F + (size_t)(((cg * 48 + uks) * 5 + nt) * 1024) + lane * 16) = (u32x4){lo.x, lo.y, hi.x, hi.y};
      }
      if (rc < 7 && w == 0 && lane < 40) {
        int sg = lane < 32 ? (5 * (g >> 1) + 2 * (g & 1) + (lane >> 4)) * 16 + 4 * ((lane >> 1) & 3) + 2 * ((lane >> 3) & 1) + (lane & 1)
                           : (5 * (g >> 1) + 4) * 16 + 8 * (g & 1) + 4 * (lane & 1) + ((lane - 32) >> 1);
        const f32x2 e = lds64f(lb + T_TAB + lane * 64 + 56);
        const float n = (float)(rc < 4 ? 4 * rc + 1 : (rc == 4 ? 16 : 64 * (rc - 4)));
        char* dp = rc < 4 ? ws + WS_T2 + rc * 32 + (sg >> 2) * 128 : ws + WS_T3 + (rc - 4) * 32 + (sg >> 2) * 96;
        *(f32x2*)(dp + (sg & 3) * 8) = cpow(e.x, e.y, n);
      }
    } else {
      u32x4 sj[4];
#pragma unroll
      for (int v = 0; v < 4; ++v) sj[v] = lds128(lb + S_W2 + w * 4096 + v * 1024 + lane * 16);
      unsigned P[2][4][2];
#pragma unroll
      for (int ga = 0; ga < 2; ++ga)
#pragma unroll
        for (int j = 0; j < 4; ++j) {
          unsigned im = sj[2 + ga][j] ^ 0x80008000u;
          P[ga][j][0] = __builtin_amdgcn_perm(im, sj[ga][j], 0x05040100u);
          P[ga][j][1] = __builtin_amdgcn_perm(im, sj[ga][j], 0x07060302u);
        }
      int Dt = 4 * udb + q, cg = Dt / 5, nt = Dt - 5 * cg;
      char* dd = ws + WS_CF + (size_t)(((cg * 40 + uks) * 5 + nt) * 1024) + c * 16;
      if (uks % 5 < 4) {
#pragma unroll
        for (int j = 0; j < 4; ++j) *(u32x4*)(dd + j * 256) = (u32x4){P[0][j][0], P[0][j][1], P[1][j][0], P[1][j][1]};
      } else {
#pragma unroll
        for (int j = 0; j < 4; ++j)
          *(u32x4*)(dd + j * 256) = (u32x4){P[j >> 1][0][j & 1], P[j >> 1][1][j & 1], P[j >> 1][2][j & 1], P[j >> 1][3][j & 1]};
      }
    }
    TS(15);
  } else {
    const int wb = w - 4;
    const char* bp[3];
    unsigned vo[3];
    const int e2 = wb < 2 ? 8 + wb : 6 + wb;
#pragma unroll
    for (int v = 0; v < 3; ++v) {
      int n = 8 * (v < 2 ? wb + 4 * v : e2) + l3;
      bp[v] = (const char*)((n & 1) ? Bim : Bre) + (40 * g + (n >> 1)) * 1280 + ((l7 ^ l3) << 4);
      int idx = 64 * (wb + 4 * v) + lane, k = idx / 12, uu = idx - 12 * k;
      vo[v] = k * 3072 + (((uu >> 1) ^ ((k >> 3) & 1)) << 5) + ((uu & 1) << 4);
    }
    const char* sb = (const char*)w1 + 192 * g;
    unsigned dst = lb + OB + wb * 1024;
    const unsigned d2 = (e2 - wb) * 1024;
    auto piece0 = [&](int st, int j) {
      const int v = j >> 1;
      if (j & 1) dma_s0(vo[v], sb + st * 196608, dst + st * SLOT + (OW - OB) + v * 4096);
      else { dma_v0(bp[v], dst + st * SLOT + (v < 2 ? v * 4096 : d2)); bp[v] += 128; }
    };
    auto issue0 = [&](int st) {
#pragma unroll
      for (int j = 0; j < 6; ++j) piece0(st, j);
    };
    issue0(0);
    prload();
    BARRIER_NW();
    issue0(1);
    BARRIER_NW();
    issue0(2);
    unsigned bo[2];
#pragma unroll
    for (int p = 0; p < 2; ++p) bo[p] = xo[p] - 8192 * h + OB + 4096 * a;
    const int r5 = c ^ (2 * a);
    unsigned f5 = lb + OB + 8192 + r5 * 128 + (((4 * kk0 + q) ^ (r5 & 7)) << 4);
    f32x4 acc[4][2], ac5[5], cs[2];
#pragma unroll
    for (int i = 0; i < 4; ++i) { acc[i][0] = (f32x4){0.f, 0.f, 0.f, 0.f}; acc[i][1] = acc[i][0]; ac5[i] = acc[i][0]; }
    ac5[4] = ac5[0]; cs[0] = ac5[0]; cs[1] = ac5[0];
    u32x4 X[2][2][4], W[2][2][2], W5[2];
    auto rd = [&](int r, int k) {
      if (k == 6) { W5[r] = lds128(f5); return; }
      const int p = k / 7, o = k % 7;
      if (o < 2) W[r][p][o] = lds128(bo[p] + 2048 * o);
      else X[r][p][o - 2] = lds128(xo[p] + 2048 * (o - 2));
    };
    auto adv = [&](unsigned d) { xo[0] += d; xo[1] += d; bo[0] += d; bo[1] += d; f5 += d; };
    WAITV(12);
    BARRIER_NW();
    TS(1);
#pragma unroll
    for (int k = 0; k < 13; ++k) rd(0, k);
    adv(SLOT);
    SB();
    piece0(3, 0); piece0(3, 1);
    tab0(); SB();
    piece0(3, 2); piece0(3, 3);
    tab1(); SB();
    piece0(3, 4); piece0(3, 5);
    sb += 4 * 196608;
    LGKM0();
    WAITV(12);
    BARRIER_NW();
    TS(2);
    auto stage = [&](int r, int t) {
#pragma unroll
      for (int sl = 0; sl < 23; ++sl) {
        if (sl < 8) acc[sl >> 1][sl & 1] = mfma(W[r][0][sl & 1], X[r][0][sl >> 1], acc[sl >> 1][sl & 1]);
        else if (sl < 12) ac5[sl - 8] = mfma(W5[r], X[r][0][sl - 8], ac5[sl - 8]);
        else if (sl == 12) ac5[4] = mfma(W5[r], ONES, ac5[4]);
        else if (sl < 21) acc[(sl - 13) >> 1][(sl - 13) & 1] = mfma(W[r][1][(sl - 13) & 1], X[r][1][(sl - 13) >> 1], acc[(sl - 13) >> 1][(sl - 13) & 1]);
        else cs[sl - 21] = mfma(W[r][1][sl - 21], ONES, cs[sl - 21]);
        if (sl < 13) rd(r ^ 1, sl);
        if (sl % 4 == 1 && sl < 23) {
          const int j = sl / 4, v = j >> 1;
          if (j & 1) dma_s(vo[v], sb, dst + (OW - OB) + v * 4096, t, 6);
          else { dma_v(bp[v], dst + (v < 2 ? v * 4096 : d2), t, 6); bp[v] += 128; }
        }
        constexpr int st[24] = {0, 2, 4, 6, 8, 10, 12, 14, 16, 18, 20, 22, 24, 26, 29, 31, 34, 36, 39, 41, 44, 46, 48, 48};
#pragma unroll
        for (int k = st[sl]; k < st[sl + 1]; ++k) {
          if (k < 24) opS(X[r][0][2], sv[0], sv[1], k); else opS(X[r][1][3], sv[2], sv[3], k - 24);
        }
        SB();
      }
    };
    for (int t = 0; t < 10; t += 2) {
      stage(0, t);
      adv((t & 2) ? -3 * SLOT : SLOT);
      dst += SLOT; sb += 196608;
      LGKM0();
      WAITV2(t, 6, 12, 6);
      BARRIER_NW();
      TS(3 + t);
      stage(1, t + 1);
      adv(SLOT);
      dst += (t & 2) ? -3 * SLOT : SLOT; sb += 196608;
      LGKM0();
      WAITV2(t, 6, 12, 0);
      BARRIER_NW();
      TS(4 + t);
    }
    Tab tb[5];
#pragma unroll
    for (int k = 0; k < 4; ++k) rdtab(tb[k], lb + T_TAB + (16 * a + 8 * (k >> 1) + 2 * q + (k & 1)) * 64);
    rdtab(tb[4], lb + T_TAB + (32 + 2 * q + a) * 64);
    put_stats(2);
    if (c == 0) {
      sts128f(lb + E_CS + ((w * 3 + 0) * 4 + q) * 16, cs[0]);
      sts128f(lb + E_CS + ((w * 3 + 1) * 4 + q) * 16, cs[1]);
    }
#pragma unroll
    for (int i = 0; i < 5; ++i) sts64f(lb + E_A5 + (((w - 4) * 5 + i) * 64 + lane) * 8, (f32x2){ac5[i].z, ac5[i].w});
    BARRIER();
    TS(13);
    float m[4], r[4];
    mr4(m, r);
    cs[0] += lds128f(lb + E_CS + (((w ^ 2) * 3 + 0) * 4 + q) * 16);
    cs[1] += lds128f(lb + E_CS + (((w ^ 2) * 3 + 1) * 4 + q) * 16);
    f32x2 u[5][4], csv[5];
#pragma unroll
    for (int i = 0; i < 4; ++i) {
      u[0][i] = (f32x2){acc[i][0].x, acc[i][0].y}; u[1][i] = (f32x2){acc[i][0].z, acc[i][0].w};
      u[2][i] = (f32x2){acc[i][1].x, acc[i][1].y}; u[3][i] = (f32x2){acc[i][1].z, acc[i][1].w};
      u[4][i] = (f32x2){ac5[i].x, ac5[i].y} + lds64f(lb + E_A5 + ((((w - 4) ^ 1) * 5 + i) * 64 + lane) * 8);
    }
    csv[0] = (f32x2){cs[0].x, cs[0].y}; csv[1] = (f32x2){cs[0].z, cs[0].w};
    csv[2] = (f32x2){cs[1].x, cs[1].y}; csv[3] = (f32x2){cs[1].z, cs[1].w};
    csv[4] = (f32x2){ac5[4].x, ac5[4].y} + lds64f(lb + E_A5 + ((((w - 4) ^ 1) * 5 + 4) * 64 + lane) * 8);
#pragma unroll
    for (int i = 0; i < 4; ++i) {
      f32x2 nm = {-m[i], -m[i]}, rr = {r[i], r[i]};
#pragma unroll
      for (int k = 0; k < 5; ++k) u[k][i] = cmulp(tb[k].co, pfma(csv[k], nm, u[k][i]) * rr);
    }
    f32x2 hh[5];
#pragma unroll
    for (int k = 0; k < 5; ++k) hh[k] = u[k][0];
#pragma unroll
    for (int i = 1; i < 4; ++i)
#pragma unroll
      for (int k = 0; k < 5; ++k) hh[k] = cfmap(tb[k].L, hh[k], u[k][i]);
#pragma unroll
    for (int k = 0; k < 5; ++k) hh[k] = cfmap(tb[k].L4, shr2<1>(hh[k]), hh[k]);
#pragma unroll
    for (int k = 0; k < 5; ++k) hh[k] = cfmap(tb[k].L8, shr2<2>(hh[k]), hh[k]);
#pragma unroll
    for (int k = 0; k < 5; ++k) hh[k] = cfmap(tb[k].L16, shr2<4>(hh[k]), hh[k]);
#pragma unroll
    for (int k = 0; k < 5; ++k) hh[k] = cfmap(tb[k].L32, shr2<8>(hh[k]), hh[k]);
    const int ksr = 5 * (g >> 1) + 2 * (g & 1) + a, ks5 = 5 * (g >> 1) + 4;
    f32x2 ag[5];
#pragma unroll
    for (int k = 0; k < 5; ++k) ag[k] = hh[k];
    if (h == 0) {
      if (c == 15) {
        unsigned ta = lb + T_AG + (a * 4 + q) * 48;
        sts128f(ta, (f32x4){ag[0].x, ag[0].y, ag[1].x, ag[1].y});
        sts128f(ta + 16, (f32x4){ag[2].x, ag[2].y, ag[3].x, ag[3].y});
        sts64f(ta + 32, ag[4]);
      }
      WAITL0();
      if (lane == 0) *(volatile AS3 unsigned*)(uintptr_t)(lb + T_FLAG + a * 4) = 1u;
    }
#pragma unroll
    for (int k = 0; k < 5; ++k) hh[k] = shr2<1>(hh[k]);
    __amdgpu_buffer_rsrc_t rs = mkrsrc(ws + WS_HF, 5242880);
    unsigned o5[4];
#pragma unroll
    for (int i = 0; i < 4; ++i) {
      u32x4 o;
#pragma unroll
      for (int k = 0; k < 5; ++k) {
        hh[k] = cfmap(tb[k].L, hh[k], u[k][i]);
        if (k < 4) o[k] = pk2(hh[k].x, hh[k].y); else o5[i] = pk2(hh[k].x, hh[k].y);
      }
      st128(o, rs, lane * 16 + ((rb * 40 + ksr) * 4 + i) * 1024);
    }
    swap32(o5[0], o5[2]); swap32(o5[1], o5[3]);
    swap16(o5[0], o5[1]); swap16(o5[2], o5[3]);
    st128((u32x4){o5[0], o5[1], o5[2], o5[3]}, rs, (c + 16 * (2 * (g & 1) + a)) * 16 + ((rb * 40 + ks5) * 4 + q) * 1024);
    if (h == 1) {
      while (*(volatile AS3 unsigned*)(uintptr_t)(lb + T_FLAG + a * 4) == 0u) __builtin_amdgcn_s_sleep(1);
      asm volatile("" ::: "memory");
      unsigned ta = lb + T_AG + (a * 4 + q) * 48;
      f32x4 l0 = lds128f(ta), l1 = lds128f(ta + 16);
      f32x2 l2 = lds64f(ta + 32);
      ag[0] = cfmap(tb[0].L64, (f32x2){l0.x, l0.y}, ag[0]); ag[1] = cfmap(tb[1].L64, (f32x2){l0.z, l0.w}, ag[1]);
      ag[2] = cfmap(tb[2].L64, (f32x2){l1.x, l1.y}, ag[2]); ag[3] = cfmap(tb[3].L64, (f32x2){l1.z, l1.w}, ag[3]);
      ag[4] = cfmap(tb[4].L64, l2, ag[4]);
    }
    if (c == 15) {
      char* base = ws + (h ? WS_AGG : WS_AGL) + rc * 2560;
      *(u32x4*)(base + (ksr * 16 + 4 * q) * 4) = (u32x4){pk2(ag[0].x, ag[0].y), pk2(ag[1].x, ag[1].y), pk2(ag[2].x, ag[2].y), pk2(ag[3].x, ag[3].y)};
      *(unsigned*)(base + (ks5 * 16 + 8 * (g & 1) + 4 * a + q) * 4) = pk2(ag[4].x, ag[4].y);
    }
    TS(15);
  }
}

constexpr unsigned M_RO = 0;
constexpr unsigned M_RS = 61440;
constexpr unsigned M_T1 = M_RS + 81920;
constexpr unsigned M_CTL = M_T1;
constexpr unsigned K2_LDS = M_CTL + 64;

extern "C" __global__ __launch_bounds__(512, 2) void k2(const u16* x, const u16* Dsk, char* ws, float* out) {
  __shared__ __attribute__((aligned(1024))) char smem[K2_LDS];
  const unsigned lb = (unsigned)(uintptr_t)(AS3 char*)smem;
  const int tid = threadIdx.x, lane = tid & 63;
  const int w = __builtin_amdgcn_readfirstlane(tid >> 6);
  const int c = lane & 15, q = lane >> 4;
  const int b = blockIdx.x, xcd = b & 7, jb = b >> 3;
  const int rb = (xcd >> 1) * 8 + (jb & 7), cg = (xcd & 1) * 4 + (jb >> 3), gh = xcd & 1;
  const int rc = rb >> 1, par = rb & 1;
  const int vo = lane * 16;
  TSDECL;
  TS(0);
  f32x4 acc[4][5];
  if (tid < 4) sts32f(lb + M_CTL + tid * 4, 0.f);
  BARRIER();

  if (w >= 4) {
    const int sw = w - 4;
    __builtin_amdgcn_s_setprio(SPRIO);
    __amdgpu_buffer_rsrc_t rA = mkrsrc(ws + WS_HF + (size_t)(rb * 40) * 4096, 40 * 4096);
    __amdgpu_buffer_rsrc_t rB = mkrsrc(ws + WS_CF + (size_t)(cg * 40) * 5120, 40 * 5120);
    auto kof = [&](int i) { return 5 * sw + (i >> 1) + 20 * ((i & 1) ^ gh); };
    u32x4 A[3][4], B[3][5];
    auto load = [&](int i) {
      int bf = i % 3;
#pragma unroll
      for (int j = 0; j < 9; ++j) {
        const int m = j < 1 ? j : j - 5;
        if (j >= 1 && j < 6) B[bf][j - 1] = __builtin_amdgcn_raw_buffer_load_b128(rB, vo + (j - 1) * 1024, kof(i) * 5120, 0);
        else A[bf][m] = __builtin_amdgcn_raw_buffer_load_b128(rA, vo + m * 1024, kof(i) * 4096, 0);
        SB();
      }
    };
    const unsigned tl = lb + M_RS + sw * 20480;
    {
      const int l40 = lane < 40 ? lane : 39;
      const int sl0 = kof(l40 >> 2) * 16 + 4 * (l40 & 3);
      {
        const unsigned dv = (5 * sw + 20 * ((lane >> 5) ^ gh)) * 512 + (lane & 31) * 16;
#pragma unroll
        for (int u = 0; u < 5; ++u) dma_s0(dv, ws + WS_T2 + u * 512, tl + 5120 + u * 1024);
      }
      __amdgpu_buffer_rsrc_t rG = mkrsrc(ws + WS_T3, 15360);
      f32x4 tv[6];
      u32x4 ag[15], al;
#pragma unroll
      for (int u = 0; u < 6; ++u)
        tv[u] = __builtin_bit_cast(f32x4, __builtin_amdgcn_raw_buffer_load_b128(rG, (sl0 >> 2) * 96 + ((u + 4) % 6) * 16, 0, 0));
#pragma unroll
      for (int m = 0; m < 15; ++m)
        ag[m] = __builtin_amdgcn_raw_buffer_load_b128(mkrsrc(ws + WS_AGG, m < rc ? 40960u : 0u), sl0 * 4, m * 2560, 0);
      al = __builtin_amdgcn_raw_buffer_load_b128(mkrsrc(ws + WS_AGL, par ? 40960u : 0u), sl0 * 4, rc * 2560, 0);
      SB();
      load(0);
      SB();
      f32x2 P[4], gg[4], Ls[4], L64[4];
#pragma unroll
      for (int k = 0; k < 4; ++k) {
        P[k] = (f32x2){tv[k >> 1][2 * (k & 1)], tv[k >> 1][2 * (k & 1) + 1]};
        Ls[k] = (f32x2){tv[2 + (k >> 1)][2 * (k & 1)], tv[2 + (k >> 1)][2 * (k & 1) + 1]};
        L64[k] = (f32x2){tv[4 + (k >> 1)][2 * (k & 1)], tv[4 + (k >> 1)][2 * (k & 1) + 1]};
        gg[k] = (f32x2){0.f, 0.f};
      }
#pragma unroll
      for (int m = 0; m < 15; ++m)
        if (m < rc) {
#pragma unroll
          for (int k = 0; k < 4; ++k) gg[k] = cfmas(P[k], gg[k], (f32x2){bflo(ag[m][k]), bfhi(ag[m][k])});
        }
      if (par) {
#pragma unroll
        for (int k = 0; k < 4; ++k) gg[k] = cfmas(L64[k], gg[k], (f32x2){bflo(al[k]), bfhi(al[k])});
      }
      SB();
      load(1);
      SB();
#pragma unroll
      for (int j = 0; j < 4; ++j) {
        if (j) {
#pragma unroll
          for (int k = 0; k < 4; ++k) gg[k] = cmuls(Ls[k], gg[k]);
        }
        sts128f(tl + l40 * 128 + j * 32, (f32x4){gg[0].x, gg[0].y, gg[1].x, gg[1].y});
        sts128f(tl + l40 * 128 + j * 32 + 16, (f32x4){gg[2].x, gg[2].y, gg[3].x, gg[3].y});
      }
      SB();
      load(2);
      SB();
    }
    WAITV(27);
    TS(1);
#pragma unroll
    for (int m = 0; m < 4; ++m)
#pragma unroll
      for (int n = 0; n < 5; ++n) acc[m][n] = (f32x4){0.f, 0.f, 0.f, 0.f};
    const unsigned tq = tl + q * 128 + (c >> 2) * 32, uq = tl + 5120 + q * 128 + (c & 3) * 32, lq = tl + 5120 + q * 128;
    const unsigned tb = lb + M_RS + sw * 20480 + vo;
    f32x4 tt[4], Lq[2];
    float zf[8], tf[8], e0, e1;
    auto rdt = [&](int i) {
      tt[0] = lds128f(tq + i * 512); tt[1] = lds128f(tq + i * 512 + 16);
      tt[2] = lds128f(uq + i * 512); tt[3] = lds128f(uq + i * 512 + 16);
    };
    auto rdl = [&](int i) { Lq[0] = lds128f(lq + i * 512); Lq[1] = lds128f(lq + i * 512 + 16); };
    auto opA = [&](int k) {
      const int d = k / 4, o = k % 4;
      const float Lx = Lq[d >> 1][2 * (d & 1)], Ly = Lq[d >> 1][2 * (d & 1) + 1];
      if (o == 0) e0 = vmul(Ly, zf[2 * d + 1]);
      else if (o == 1) e1 = vmul(Ly, zf[2 * d]);
      else if (o == 2) zf[2 * d] = vfms(Lx, zf[2 * d], e0);
      else zf[2 * d + 1] = vfma(Lx, zf[2 * d + 1], e1);
    };
    auto opZ = [&](int k) {
      const int d = k / 4, o = k % 4;
      const float gx = tt[d >> 1][2 * (d & 1)], gy = tt[d >> 1][2 * (d & 1) + 1];
      const float ux = tt[2 + (d >> 1)][2 * (d & 1)], uy = tt[2 + (d >> 1)][2 * (d & 1) + 1];
      if (o == 0) e0 = vmul(gy, uy);
      else if (o == 1) e1 = vmul(gy, ux);
      else if (o == 2) zf[2 * d] = vfms(gx, ux, e0);
      else zf[2 * d + 1] = vfma(gx, uy, e1);
    };
    auto opD = [&](int bf, int m, int j) {
      const unsigned av = A[bf][m][j >> 1];
      tf[j] = vadd(zf[j], (j & 1) ? bfhi(av) : bflo(av));
    };
    auto opC = [&](int bf, int m, int d) { A[bf][m][d] = pk2a(tf[2 * d], tf[2 * d + 1]); };
    auto vop = [&](int i, int k) {
      const int bf = i % 3;
      if (k < 100) {
        const int gq = (k + 12) / 28, o = (k + 12) % 28;
        if (o < 12) opA(4 + o);
        else if (o < 20) opD(bf, gq, o - 12);
        else if (o < 24) { if (gq < 3) opA(o - 20); else if (i < 9) opZ(o - 20); }
        else opC(bf, gq, o - 24);
      } else if (i < 9) opZ(k - 96);
    };
    rdt(0); rdl(0);
#pragma unroll
    for (int k = 0; k < 16; ++k) opZ(k);
    SB();
    auto step = [&](auto I) {
      constexpr int i = decltype(I)::v;
      sfor<0, 16>([&](auto K) { vop(i, decltype(K)::v); SB(); });
      sfor<0, 20>([&](auto S) {
        constexpr int sl = decltype(S)::v;
        acc[sl / 5][sl % 5] = mfma(B[i % 3][sl % 5], A[i % 3][sl / 5], acc[sl / 5][sl % 5]);
        constexpr int st[21] = {16, 22, 28, 34, 39, 44, 50, 56, 62, 67, 72, 78, 84, 90, 95, 100, 103, 106, 108, 110, 112};
        SB();
        sfor<st[sl], st[sl + 1]>([&](auto K) { vop(i, decltype(K)::v); SB(); });
        if (i < 9 && sl == 9) rdt(i + 1);
        if (i < 9 && sl == 12) rdl(i + 1);
        if (i == 9 && sl >= 2) sts128f(tb + (sl - 2) * 1024, acc[(sl - 2) / 5][(sl - 2) % 5]);
        SB();
      });
      if (i + 3 < 10) {
        if (SLP != SPRIO) __builtin_amdgcn_s_setprio(SLP);
        load(i + 3);
        SB();
        if (SLP != SPRIO) __builtin_amdgcn_s_setprio(SPRIO);
      }
      TS(2 + i);
    };
    step(IC<0>{}); step(IC<1>{}); step(IC<2>{}); step(IC<3>{}); step(IC<4>{});
    step(IC<5>{}); step(IC<6>{}); step(IC<7>{}); step(IC<8>{}); step(IC<9>{});
    sts128f(tb + 18 * 1024, acc[3][3]);
    sts128f(tb + 19 * 1024, acc[3][4]);
    TS(14);
    BARRIER();
    TS(15);
    TSDUMP(33554432 + 1048576);
    return;
  }

  __builtin_amdgcn_s_setprio(OPRIO);
  __amdgpu_buffer_rsrc_t rA = mkrsrc(ws + WS_H1F + (size_t)rb * 48 * 4096, 48 * 4096);
  __amdgpu_buffer_rsrc_t rB = mkrsrc(ws + WS_W2F + (size_t)cg * 48 * 5120, 48 * 5120);
  u32x4 A[4][4], B[4][5];
  auto loadA = [&](int i) {
    int bf = i & 3, ks = w + 4 * ((i >> 1) + 6 * ((i & 1) ^ gh));
#pragma unroll
    for (int m = 0; m < 4; ++m) { A[bf][m] = __builtin_amdgcn_raw_buffer_load_b128(rA, vo, ks * 4096 + ((w + m) & 3) * 1024, 0); SB(); }
  };
  auto loadB = [&](int i) {
    int bf = i & 3, ks = w + 4 * ((i >> 1) + 6 * ((i & 1) ^ gh));
#pragma unroll
    for (int n = 0; n < 5; ++n) { B[bf][n] = __builtin_amdgcn_raw_buffer_load_b128(rB, vo + n * 1024, ks * 5120, 0); SB(); }
  };
  auto load = [&](int i) { loadA(i); loadB(i); };
  u32x2 xv[5], sk[5];
  f32x2 st;
  load(0);
  load(1);
  load(2);
  TS(1);
#pragma unroll
  for (int m = 0; m < 4; ++m)
#pragma unroll
    for (int n = 0; n < 5; ++n) acc[m][n] = (f32x4){0.f, 0.f, 0.f, 0.f};
  unsigned da[3];
  auto skip = [&](int u) {
    const int n = u >> 1, hf = u & 1;
    unsigned p = pk2a(vsub(bflo(xv[n][hf]), st.x), vsub(bfhi(xv[n][hf]), st.x));
    p = pk2a(vmul(bflo(p), st.y), vmul(bfhi(p), st.y));
    sk[n][hf] = pk2a(vmul(bflo(p), bflo(sk[n][hf])), vmul(bfhi(p), bfhi(sk[n][hf])));
  };
#pragma unroll
  for (int i = 0; i < 12; ++i) {
    if (OTP != OPRIO && i == OTI) __builtin_amdgcn_s_setprio(OTP);
    if (i == 9) {
      int l2 = lane;
      asm volatile("" : "+v"(l2));
      const int trow = 64 * rb + 4 * (l2 & 15) + w;
      const int eo = trow * 640 + 80 * cg + 4 * (l2 >> 4);
      __amdgpu_buffer_rsrc_t rx = mkrsrc(x, 2621440), rd = mkrsrc(Dsk, 1280), rt = mkrsrc(ws + WS_ST, 16384);
      st = __builtin_bit_cast(f32x2, __builtin_amdgcn_raw_buffer_load_b64(rt, trow * 8, 0, 0));
#pragma unroll
      for (int n = 0; n < 5; ++n) {
        xv[n] = __builtin_amdgcn_raw_buffer_load_b64(rx, eo * 2 + n * 32, 0, 0);
        sk[n] = __builtin_amdgcn_raw_buffer_load_b64(rd, (l2 >> 4) * 8 + n * 32, 160 * cg, 0);
      }
      SB();
    }
    if (i + 3 < 12) {
      if (OLP != OPRIO && i >= OLI && (OTP == OPRIO || i < OTI)) __builtin_amdgcn_s_setprio(OLP);
      load(i + 3);
      SB();
      if (OLP != OPRIO && i >= OLI && (OTP == OPRIO || i < OTI)) __builtin_amdgcn_s_setprio(OPRIO);
    }
    SB();
    if (i == 10) {
#pragma unroll
      for (int sl = 0; sl < 20; ++sl) {
        acc[sl / 5][sl % 5] = mfma(B[i & 3][sl % 5], A[i & 3][sl / 5], acc[sl / 5][sl % 5]);
        SB();
        if (sl % 4 == 3) skip(sl / 4);
        SB();
      }
    } else if (i < 11) {
#pragma unroll
      for (int m = 0; m < 4; ++m)
#pragma unroll
        for (int n = 0; n < 5; ++n) acc[m][n] = mfma(B[i & 3][n], A[i & 3][m], acc[m][n]);
    } else {
      int v2 = vo;
      asm volatile("" : "+v"(v2));
#pragma unroll
      for (int j = 1; j < 4; ++j) da[j - 1] = lb + M_RO + (((w + j) & 3) * 3 + (j - 1)) * 5120 + v2;
#pragma unroll
      for (int sl = 0; sl < 20; ++sl) {
        const int m = (sl / 5 + 1) & 3, n = sl % 5;
        acc[m][n] = mfma(B[i & 3][n], A[i & 3][m], acc[m][n]);
        if (sl >= 2 && sl < 17) sts128f(da[(sl - 2) / 5] + ((sl - 2) % 5) * 1024, acc[(sl - 2) / 5 + 1][(sl - 2) % 5]);
        SB();
        if (sl % 4 == 0) skip(5 + sl / 4);
        SB();
      }
    }
    SB();
    TS(2 + i);
  }
  WAITL0();
  if (lane == 0) *(volatile AS3 unsigned*)(uintptr_t)(lb + M_CTL + w * 4) = 1u;
  for (;;) {
    u32x4 f = *(volatile AS3 u32x4*)(uintptr_t)(lb + M_CTL);
    if ((f.x & f.y & f.z & f.w) != 0u) break;
    __builtin_amdgcn_s_sleep(1);
  }
  asm volatile("" ::: "memory");
  TS(14);
  f32x4 xs[5], g2[5];
  {
    f32x4 po[5][3];
#pragma unroll
    for (int n = 0; n < 5; ++n)
#pragma unroll
      for (int j = 0; j < 3; ++j) po[n][j] = lds128f(lb + M_RO + ((w * 3 + j) * 5 + n) * 1024 + vo);
    SB();
#pragma unroll
    for (int n = 0; n < 5; ++n) {
      f32x4 o;
#pragma unroll
      for (int j = 0; j < 4; ++j) o[j] = vadd(vadd(acc[0][n][j], po[n][0][j]), vadd(po[n][1][j], po[n][2][j]));
#pragma unroll
      for (int hf = 0; hf < 2; ++hf) {
        unsigned p = pk2a(o[2 * hf], o[2 * hf + 1]);
        float v0 = bflo(p), v1 = bfhi(p);
        float e0 = __builtin_amdgcn_exp2f(vmul(v0, -1.44269504088896f)), e1 = __builtin_amdgcn_exp2f(vmul(v1, -1.44269504088896f));
        e0 = __builtin_amdgcn_rcpf(vadd(e0, 1.f)); e1 = __builtin_amdgcn_rcpf(vadd(e1, 1.f));
        p = pk2a(vmul(v0, e0), vmul(v1, e1));
        v0 = bflo(p); v1 = bfhi(p);
        xs[n][2 * hf] = vfma(v0, bflo(sk[n][hf]), bflo(xv[n][hf]));
        xs[n][2 * hf + 1] = vfma(v1, bfhi(sk[n][hf]), bfhi(xv[n][hf]));
        g2[n][2 * hf] = v0; g2[n][2 * hf + 1] = v1;
      }
    }
#pragma unroll
    for (int n = 0; n < 5; ++n) asm volatile("" : "+v"(xs[n]), "+v"(g2[n]));
  }
  __amdgpu_buffer_rsrc_t ro = mkrsrc(out, 5242880);
  int l2 = lane;
  asm volatile("" : "+v"(l2));
  const int eo2 = ((64 * rb + 4 * (l2 & 15) + w) * 640 + 80 * cg + 4 * (l2 >> 4)) * 4;
  TS(15);
  BARRIER_NW();
  TS(16);
  f32x4 ps[5][4];
#pragma unroll
  for (int n = 0; n < 5; ++n)
#pragma unroll
    for (int k = 0; k < 4; ++k) ps[n][k] = lds128f(lb + M_RS + (w * 5 + n) * 1024 + k * 20480 + vo);
  SB();
#pragma unroll
  for (int n = 0; n < 5; ++n) {
    f32x4 s = (ps[n][0] + ps[n][1]) + (ps[n][2] + ps[n][3]);
    f32x4 r = __builtin_elementwise_fma(s, g2[n], xs[n]);
    __builtin_amdgcn_raw_buffer_store_b128(__builtin_bit_cast(u32x4, r), ro, eo2 + n * 64, 0, 0);
    SB();
  }
}

extern "C" __device__ __attribute__((used, noinline)) void kend() {}

extern "C" void kernel_launch(void* const* d_in, const int* in_sizes, int n_in, void* d_out, int out_size, void* d_ws,
                              size_t ws_size, hipStream_t stream) {
  const u16* x = (const u16*)d_in[0];
  k1<<<256, 512, 0, stream>>>(x, (const u16*)d_in[1], (const u16*)d_in[2], (const u16*)d_in[3], (const u16*)d_in[4],
                              (const u16*)d_in[5], (const u16*)d_in[6], (const u16*)d_in[7], (const u16*)d_in[10],
                              (const u16*)d_in[11], (char*)d_ws, (const char*)d_out);
  k2<<<256, 512, 0, stream>>>(x, (const u16*)d_in[8], (char*)d_ws, (float*)d_out);
}
```

```cpp
#include <hip/hip_runtime.h>
#include <stdint.h>

typedef unsigned short u16;
typedef __attribute__((ext_vector_type(8))) __bf16 bf16x8;
typedef __attribute__((ext_vector_type(2))) __bf16 bf16x2;
typedef __attribute__((ext_vector_type(4))) float f32x4;
typedef __attribute__((ext_vector_type(2))) float f32x2;
typedef __attribute__((ext_vector_type(4))) unsigned u32x4;
typedef __attribute__((ext_vector_type(2))) unsigned u32x2;
typedef __attribute__((ext_vector_type(4))) short s16x4;
#define AS3 __attribute__((address_space(3)))
#define DEVI __device__ __forceinline__

constexpr size_t WS_H1F = 0;
constexpr size_t WS_HF = 6291456;
constexpr size_t WS_W2F = 11534336;
constexpr size_t WS_CF = 13500416;
constexpr size_t WS_AGG = 15138816;
constexpr size_t WS_AGL = 15220736;
constexpr size_t WS_TAB = 15302656;
constexpr size_t WS_ST = 15323136;

DEVI float bf2f(unsigned v) { return __uint_as_float(v << 16); }
DEVI float bflo(unsigned v) { return __uint_as_float(v << 16); }
DEVI float bfhi(unsigned v) { return __uint_as_float(v & 0xffff0000u); }
DEVI unsigned pk2(float a, float b) {
  bf16x2 r = {(__bf16)a, (__bf16)b};
  return __builtin_bit_cast(unsigned, r);
}
DEVI unsigned pk2a(float a, float b) { unsigned r; asm("v_cvt_pk_bf16_f32 %0, %1, %2" : "=v"(r) : "v"(a), "v"(b)); return r; }
DEVI float rbf(float a) { return bflo(pk2(a, 0.f)); }
template <int N> DEVI float shr(float v) {
  return __builtin_bit_cast(float, __builtin_amdgcn_update_dpp(0, __builtin_bit_cast(int, v), 0x110 + N, 0xf, 0xf, true));
}
template <int N> DEVI f32x2 shr2(f32x2 v) { f32x2 r; r.x = shr<N>(v.x); r.y = shr<N>(v.y); return r; }
DEVI f32x2 cpow(float a, float b, float n) {
  float mg = __builtin_amdgcn_exp2f(n * a);
  float rv = __builtin_amdgcn_fractf(n * b);
  f32x2 r;
  r.x = mg * __builtin_amdgcn_cosf(rv);
  r.y = mg * __builtin_amdgcn_sinf(rv);
  return r;
}
DEVI f32x2 cmul(f32x2 a, f32x2 b) { f32x2 r; r.x = a.x * b.x - a.y * b.y; r.y = a.x * b.y + a.y * b.x; return r; }
DEVI f32x2 pfma(f32x2 a, f32x2 b, f32x2 c) { return __builtin_elementwise_fma(a, b, c); }
DEVI f32x2 cfmap(f32x2 a, f32x2 b, f32x2 c) {
  f32x2 ax = {a.x, a.x}, ay = {-a.y, a.y}, bs = {b.y, b.x};
  return pfma(ay, bs, pfma(ax, b, c));
}
DEVI f32x2 cmulp(f32x2 a, f32x2 b) {
  f32x2 ax = {a.x, a.x}, ay = {-a.y, a.y}, bs = {b.y, b.x};
  return pfma(ay, bs, ax * b);
}
extern "C" __device__ __attribute__((const)) float mul_legacy_f32(float, float) __asm("llvm.amdgcn.fmul.legacy");
DEVI float vmul(float a, float b) { return mul_legacy_f32(a, b); }
DEVI float vfma(float a, float b, float c) { float r; asm("v_fma_f32 %0, %1, %2, %3" : "=v"(r) : "v"(a), "v"(b), "v"(c)); return r; }
DEVI float vadd(float a, float b) { float r; asm("v_add_f32 %0, %1, %2" : "=v"(r) : "v"(a), "v"(b)); return r; }
DEVI float vsub(float a, float b) { float r; asm("v_sub_f32 %0, %1, %2" : "=v"(r) : "v"(a), "v"(b)); return r; }
DEVI float vfms(float a, float b, float c) { float r; asm("v_fma_f32 %0, %1, %2, -%3" : "=v"(r) : "v"(a), "v"(b), "v"(c)); return r; }
DEVI float siluf(float a) { return a * __builtin_amdgcn_rcpf(1.f + __builtin_amdgcn_exp2f(-1.44269504088896f * a)); }
DEVI void swap16(unsigned& a, unsigned& b) { auto r = __builtin_amdgcn_permlane16_swap(a, b, false, false); a = r[0]; b = r[1]; }
DEVI void swap32(unsigned& a, unsigned& b) { auto r = __builtin_amdgcn_permlane32_swap(a, b, false, false); a = r[0]; b = r[1]; }
DEVI void swap16f(float& a, float& b) { unsigned x = __float_as_uint(a), y = __float_as_uint(b); swap16(x, y); a = __uint_as_float(x); b = __uint_as_float(y); }
DEVI void swap32f(float& a, float& b) { unsigned x = __float_as_uint(a), y = __float_as_uint(b); swap32(x, y); a = __uint_as_float(x); b = __uint_as_float(y); }

template <int N> struct IC { static constexpr int v = N; };
template <int A, int B, class F> __device__ __forceinline__ void sfor(F&& f) { if constexpr (A < B) { f(IC<A>{}); sfor<A + 1, B>(f); } }
#define WAITV(n) asm volatile("s_waitcnt vmcnt(%0)" ::"n"(n) : "memory")
#define WAITL0() asm volatile("s_waitcnt lgkmcnt(0)" ::: "memory")
#define BARRIER() do { WAITL0(); __builtin_amdgcn_s_barrier(); asm volatile("" ::: "memory"); } while (0)
#define BARRIER_NW() do { asm volatile("" ::: "memory"); __builtin_amdgcn_s_barrier(); asm volatile("" ::: "memory"); } while (0)
#define SB() __builtin_amdgcn_sched_barrier(0)
DEVI void glds(const void* p, unsigned ldsaddr) {
  unsigned keep;
  asm volatile("s_mov_b32 %0, m0\n\ts_mov_b32 m0, %2\n\ts_nop 0\n\tglobal_load_lds_dwordx4 %1, off\n\ts_mov_b32 m0, %0"
               : "=&s"(keep) : "v"(p), "s"(ldsaddr) : "memory");
}

DEVI u32x4 lds128(unsigned off) { return *(const AS3 u32x4*)(uintptr_t)off; }
DEVI f32x4 lds128f(unsigned off) { return *(const AS3 f32x4*)(uintptr_t)off; }
DEVI f32x2 lds64f(unsigned off) { return *(const AS3 f32x2*)(uintptr_t)off; }
DEVI void sts128f(unsigned off, f32x4 v) { *(AS3 f32x4*)(uintptr_t)off = v; }
DEVI void sts64f(unsigned off, f32x2 v) { *(AS3 f32x2*)(uintptr_t)off = v; }
DEVI void sts32f(unsigned off, float v) { *(AS3 float*)(uintptr_t)off = v; }
DEVI u32x2 ldstr(unsigned off) {
  s16x4 r = __builtin_amdgcn_ds_read_tr16_b64_v4i16((AS3 s16x4*)(uintptr_t)off);
  return __builtin_bit_cast(u32x2, r);
}
DEVI u32x4 ldstr2(unsigned a, unsigned d) { u32x2 lo = ldstr(a), hi = ldstr(a + d); return (u32x4){lo.x, lo.y, hi.x, hi.y}; }
DEVI f32x4 mfma(u32x4 a, u32x4 b, f32x4 c) {
  return __builtin_amdgcn_mfma_f32_16x16x32_bf16(__builtin_bit_cast(bf16x8, a), __builtin_bit_cast(bf16x8, b), c, 0, 0, 0);
}
#define STAUX 16
#define SPRIO 2
#define OPRIO 0
#define OLP OPRIO
#define SLP SPRIO
#define OLI 0
#define OTP OPRIO
#define OTI 9
DEVI void st128(u32x4 v, __amdgpu_buffer_rsrc_t rs, int off) { __builtin_amdgcn_raw_buffer_store_b128(v, rs, off, 0, STAUX); }
DEVI __amdgpu_buffer_rsrc_t mkrsrc(const void* p, unsigned bytes) {
  return __builtin_amdgcn_make_buffer_rsrc((void*)p, 0, bytes, 0x00020000);
}
#define TSDECL
#define TS(k)
#define TSDUMP(base)

constexpr unsigned SLOT = 38912, OB = 16384, OW = 26624;
constexpr unsigned SPARE = 4 * SLOT;
constexpr unsigned T_TAB = SPARE;
constexpr unsigned T_AG = SPARE + 2560;
constexpr unsigned T_FLAG = SPARE + 3072;
constexpr unsigned T_PF = SPARE + 4096;
constexpr unsigned K1_LDS = SPARE + 5120;
constexpr unsigned S_W2 = 2 * SLOT;
constexpr unsigned E_ST = 3 * SLOT;
constexpr unsigned E_CS = E_ST + 2048;
constexpr unsigned E_A5 = E_CS + 2048;

DEVI void stat(u32x4 f, float& s, float& q) {
#pragma unroll
  for (int r = 0; r < 4; ++r) {
    float lo = bflo(f[r]), hi = bfhi(f[r]);
    s += lo; q = fmaf(lo, lo, q);
    s += hi; q = fmaf(hi, hi, q);
  }
}
template <int NM, int NV> DEVI void ileave() {
#pragma unroll
  for (int i = 0; i < NM; ++i) {
    __builtin_amdgcn_sched_group_barrier(0x008, 1, 0);
    __builtin_amdgcn_sched_group_barrier(0x002, NV, 0);
  }
}
struct Tab { f32x2 L, L4, L8, L16, L32, L64, co; };
DEVI void rdtab(Tab& t, unsigned a) {
  f32x4 v0 = lds128f(a), v1 = lds128f(a + 16), v2 = lds128f(a + 32);
  f32x2 v3 = lds64f(a + 48);
  t.L = (f32x2){v0.x, v0.y}; t.L4 = (f32x2){v0.z, v0.w}; t.L8 = (f32x2){v1.x, v1.y}; t.L16 = (f32x2){v1.z, v1.w};
  t.L32 = (f32x2){v2.x, v2.y}; t.L64 = (f32x2){v2.z, v2.w}; t.co = v3;
}

DEVI void dma_s(unsigned voff, const char* sbase, unsigned ldsaddr, int t, int lim) {
  asm volatile("s_cmp_lt_i32 %3, %4\n\ts_cbranch_scc0 .Ldma%=\n\ts_mov_b32 m0, %2\n\ts_nop 0\n\tglobal_load_lds_dwordx4 %0, %1\n.Ldma%=:"
               ::"v"(voff), "s"(sbase), "s"(ldsaddr), "s"(t), "n"(lim) : "memory", "scc");
}
DEVI void dma_v(const char* p, unsigned ldsaddr, int t, int lim) {
  asm volatile("s_cmp_lt_i32 %2, %3\n\ts_cbranch_scc0 .Ldmb%=\n\ts_mov_b32 m0, %1\n\ts_nop 0\n\tglobal_load_lds_dwordx4 %0, off\n.Ldmb%=:"
               ::"v"(p), "s"(ldsaddr), "s"(t), "n"(lim) : "memory", "scc");
}
DEVI void dma_s0(unsigned voff, const char* sbase, unsigned ldsaddr) {
  asm volatile("s_mov_b32 m0, %2\n\ts_nop 0\n\tglobal_load_lds_dwordx4 %0, %1" ::"v"(voff), "s"(sbase), "s"(ldsaddr) : "memory");
}
DEVI void dma_v0(const char* p, unsigned ldsaddr) {
  asm volatile("s_mov_b32 m0, %1\n\ts_nop 0\n\tglobal_load_lds_dwordx4 %0, off" ::"v"(p), "s"(ldsaddr) : "memory");
}
DEVI const char* code_base(unsigned& len, unsigned& off2) {
  uint64_t pc;
  unsigned d;
  asm volatile("s_getpc_b64 %0\n.Lpc%=:\n\ts_mov_b32 %1, .Lpc%=-k1\n\ts_mov_b32 %2, kend-k1\n\ts_mov_b32 %3, k2-k1"
               : "=s"(pc), "=s"(d), "=s"(len), "=s"(off2));
  return (const char*)(pc - d);
}
#define WAITV2(t, lim, A, B) asm volatile("s_cmp_lt_i32 %0, %1\n\ts_cbranch_scc1 .Lwv%=\n\ts_waitcnt vmcnt(%3)\n.Lwv%=:\n\ts_waitcnt vmcnt(%2)" ::"s"(t), "n"(lim), "n"(A), "n"(B) : "memory", "scc")
#define LGKM0() __builtin_amdgcn_s_waitcnt(0xc07f)

extern "C" __global__ __launch_bounds__(512, 2) void k1(const u16* x, const u16* Lre, const u16* Lim, const u16* Bre, const u16* Bim,
                                             const u16* Cre, const u16* Cim, const u16* ldt, const u16* w1, const u16* w2,
                                             char* ws) {
  __shared__ __attribute__((aligned(1024))) char smem[K1_LDS];
  const unsigned lb = (unsigned)(uintptr_t)(AS3 char*)smem;
  const int b = blockIdx.x, xcd = b & 7, jb = b >> 3;
  const int rc = (xcd >> 1) * 4 + (jb & 3), g = (xcd & 1) * 8 + (jb >> 2);
  const int tid = threadIdx.x, lane = tid & 63;
  const int w = __builtin_amdgcn_readfirstlane(tid >> 6);
  const int c = lane & 15, q = lane >> 4;
  const int l3 = lane >> 3, l7 = lane & 7;
  const int h = (w >> 1) & 1, a = w & 1;
  const int kk0 = h ^ a;
  TSDECL;
  TS(0);

  const u32x4 ONES = {0x3f803f80u, 0x3f803f80u, 0x3f803f80u, 0x3f803f80u};
  unsigned xo[2];
#pragma unroll
  for (int p = 0; p < 2; ++p) xo[p] = lb + 8192 * h + c * 128 + (((4 * (kk0 ^ p) + q) ^ (c & 7)) << 4);
  float sv[4] = {0.f, 0.f, 0.f, 0.f};
  float t0, t1;
  const int rb = 2 * rc + h;
  unsigned pr[3];
  auto prload = [&]() {
    int p = 40 * g + (lane < 40 ? lane : 0);
    const u16* a0 = Lre + p; const u16* a1 = Lim + p; const u16* a2 = ldt + p;
    asm volatile("global_load_ushort %0, %1, off" : "=v"(pr[0]) : "v"(a0) : "memory");
    asm volatile("global_load_ushort %0, %1, off" : "=v"(pr[1]) : "v"(a1) : "memory");
    asm volatile("global_load_ushort %0, %1, off" : "=v"(pr[2]) : "v"(a2) : "memory");
  };
  float lr, li, ea, eb;
  auto tab0 = [&]() {
    asm volatile("" : "+v"(pr[0]), "+v"(pr[1]), "+v"(pr[2]));
    lr = bf2f(pr[0]); li = bf2f(pr[1]);
    float dt = rbf(expf(bf2f(pr[2])));
    ea = dt * lr * 1.44269504088896f; eb = dt * li * 0.15915494309189535f;
  };
  auto tab1 = [&]() {
    const float n = (w == 0 || w > 5) ? 1.f : (float)(2 << w);
    f32x2 P = cpow(ea, eb, n);
    if (w == 6) {
      float nr = P.x - 1.f, ni = P.y, dd = 2.f / (lr * lr + li * li);
      P = (f32x2){(nr * lr + ni * li) * dd, (ni * lr - nr * li) * dd};
    }
    if (w == 7) {
      P = (f32x2){ea, eb};
      if (lane >= 62) sts32f(lb + T_FLAG + (lane - 62) * 4, 0.f);
    }
    if (lane < 40) sts64f(lb + T_TAB + lane * 64 + w * 8, P);
  };
  auto opS = [&](const u32x4& f, float& s, float& qq, int k) {
    const int r = k / 6, o = k % 6;
    if (o == 0) t0 = bflo(f[r]);
    else if (o == 1) t1 = bfhi(f[r]);
    else if (o == 2) s = vadd(s, t0);
    else if (o == 3) qq = vfma(t0, t0, qq);
    else if (o == 4) s = vadd(s, t1);
    else qq = vfma(t1, t1, qq);
  };

  auto mr4 = [&](float* m, float* r) {
#pragma unroll
    for (int i = 0; i < 4; ++i) {
      f32x4 v = lds128f(lb + E_ST + (64 * h + 4 * c + i) * 16);
      float mean = (v.x + v.z) * (1.f / 640.f);
      float var = fmaxf((v.y + v.w) * (1.f / 640.f) - mean * mean, 0.f);
      float den = rbf(__builtin_amdgcn_sqrtf(rbf(rbf(var) + 1e-5f)));
      m[i] = rbf(mean); r[i] = __builtin_amdgcn_rcpf(den);
    }
  };
  auto put_stats = [&](int i0) {
    swap32f(sv[0], sv[2]); swap32f(sv[1], sv[3]);
    float w0 = sv[0] + sv[2], w1v = sv[1] + sv[3];
    swap16f(w0, w1v);
    sts32f(lb + E_ST + ((64 * h + 4 * c + i0 + (q >> 1)) * 4 + 2 * a + (q & 1)) * 4, w0 + w1v);
  };

  if (w < 4) {
    unsigned vo[4];
#pragma unroll
    for (int v = 0; v < 4; ++v) {
      int rho = 8 * (w + 4 * v) + l3;
      vo[v] = (64 * (rho >> 6) + 4 * (rho & 15) + ((rho >> 4) & 3)) * 1280 + ((l7 ^ l3) << 4);
    }
    const char* sb0 = (const char*)x + rc * 163840;
    const char* sb1 = sb0;
    unsigned dst = lb + w * 1024, dstep = 4096;
    auto issue0 = [&](int st) {
#pragma unroll
      for (int v = 0; v < 4; ++v) dma_s0(vo[v], sb0 + st * 128, dst + st * SLOT + v * 4096);
    };
    issue0(0);
    prload();
    BARRIER_NW();
    issue0(1);
    BARRIER_NW();
    issue0(2);
    {
      unsigned len, off2;
      const char* cb = code_base(len, off2);
      int lm = ((int)len - 1024) & ~15;
      lm = lm > 0 ? lm : 0;
      int po = (4 * jb + w) * 1024;
      po = (po < lm ? po : lm) + lane * 16;
      dma_s0(po, cb, lb + T_PF);
    }
    unsigned wo[2][3];
#pragma unroll
    for (int u = 0; u < 3; ++u) {
      int tl = a ? (u == 2 ? 3 : 4 + u) : u;
      wo[0][u] = lb + OW + (8 * q + (c >> 2)) * 192 + (c & 3) * 8 + ((tl ^ (q & 1)) << 5) + kk0 * 6144;
      wo[1][u] = wo[0][u] + 6144 - 2 * kk0 * 6144;
    }
    f32x4 acc[4][3], cs[3];
#pragma unroll
    for (int u = 0; u < 3; ++u) {
      cs[u] = (f32x4){0.f, 0.f, 0.f, 0.f};
#pragma unroll
      for (int i = 0; i < 4; ++i) acc[i][u] = cs[u];
    }
    u32x4 X[2][2][4], W[2][2][3];
    auto rd = [&](int r, int k) {
      const int p = k / 10, o = k % 10;
      if (o < 6) {
        u32x2 v = ldstr(wo[p][o >> 1] + (o & 1) * 768);
        W[r][p][o >> 1][2 * (o & 1)] = v.x; W[r][p][o >> 1][2 * (o & 1) + 1] = v.y;
      } else X[r][p][o - 6] = lds128(xo[p] + 2048 * (o - 6));
    };
    auto adv = [&](unsigned d) {
#pragma unroll
      for (int p = 0; p < 2; ++p) {
        xo[p] += d;
#pragma unroll
        for (int u = 0; u < 3; ++u) wo[p][u] += d;
      }
    };
    int unit = 2 * jb + (w & 1);
    if (w < 2) { if (unit >= 60) unit -= 4; } else { if (unit >= 50) unit -= 14; }
    const int u5 = unit / 5;
    const int uks = (w < 2 ? 12 : 10) * (xcd >> 1) + u5, udb = 5 * (xcd & 1) + (unit - 5 * u5);

    WAITV(9);
    BARRIER_NW();
    TS(1);
#pragma unroll
    for (int k = 0; k < 20; ++k) rd(0, k);
    adv(SLOT);
    SB();
    dma_s0(vo[0], sb0 + 384, dst + 3 * SLOT);
    tab0(); SB();
    dma_s0(vo[1], sb0 + 384, dst + 3 * SLOT + 4096);
    tab1(); SB();
    dma_s0(vo[2], sb0 + 384, dst + 3 * SLOT + 8192);
    dma_s0(vo[3], sb0 + 384, dst + 3 * SLOT + 12288);
    sb0 += 512; sb1 = sb0;
    LGKM0();
    WAITV(9);
    BARRIER_NW();
    TS(2);
    auto stage = [&](int r, int t) {
#pragma unroll
      for (int sl = 0; sl < 27; ++sl) {
        if (sl < 24) {
          const int p = sl / 12, i = (sl % 12) / 3, u = sl % 3;
          acc[i][u] = mfma(W[r][p][u], X[r][p][i], acc[i][u]);
        } else cs[sl - 24] = mfma(W[r][1][sl - 24], ONES, cs[sl - 24]);
        if (sl < 20) rd(r ^ 1, sl);
        if (sl % 6 == 2 && sl < 24) dma_s(vo[sl / 6], sl < 12 ? sb0 : sb1, dst + (sl / 6) * dstep, t, 7);
        if (sl < 12) { opS(X[r][0][0], sv[0], sv[1], 2 * sl); opS(X[r][0][0], sv[0], sv[1], 2 * sl + 1); }
        else if (sl < 24) { opS(X[r][1][1], sv[2], sv[3], 2 * sl - 24); opS(X[r][1][1], sv[2], sv[3], 2 * sl - 23); }
        SB();
      }
    };
    for (int t = 0; t < 10; t += 2) {
      if (t == 6) {
        dst = lb + S_W2 + w * 4096; dstep = 1024;
        if (w < 2) {
          sb0 = (const char*)w2 + uks * 40960 + udb * 128; sb1 = sb0;
#pragma unroll
          for (int v = 0; v < 4; ++v) {
            int r = 8 * v + l3;
            vo[v] = r * 1280 + ((l7 ^ (((r >> 1) & 3) << 1)) << 4);
          }
        } else {
          int G = uks / 5, kr = uks - 5 * G;
          int p0 = 80 * G + (kr < 4 ? 40 * (kr >> 1) + 16 * (kr & 1) : 32);
          sb0 = (const char*)Cre + udb * 81920 + p0 * 2; sb1 = (const char*)Cim + udb * 81920 + p0 * 2;
          vo[0] = lane * 1280; vo[1] = vo[0] + (kr < 4 ? 16 : 80); vo[2] = vo[0]; vo[3] = vo[1];
        }
      }
      stage(0, t);
      adv((t & 2) ? -3 * SLOT : SLOT);
      dst += SLOT; sb0 += 128; sb1 += 128;
      LGKM0();
      WAITV2(t, 8, 8, 0);
      BARRIER_NW();
      TS(3 + t);
      stage(1, t + 1);
      adv(SLOT);
      dst += (t & 2) ? -3 * SLOT : SLOT; sb0 += 128; sb1 += 128;
      LGKM0();
      WAITV2(t, 6, 8, 4);
      BARRIER_NW();
      TS(4 + t);
    }
    put_stats(0);
    if (c == 0) {
#pragma unroll
      for (int u = 0; u < 3; ++u) sts128f(lb + E_CS + ((w * 3 + u) * 4 + q) * 16, cs[u]);
    }
    BARRIER();
    TS(13);
    float m[4], r[4];
    mr4(m, r);
#pragma unroll
    for (int u = 0; u < 3; ++u) cs[u] += lds128f(lb + E_CS + (((w ^ 2) * 3 + u) * 4 + q) * 16);
    if (g == 0 && a == 0 && q == 0) {
      f32x4* st = (f32x4*)(ws + WS_ST + (64 * rb + 4 * c) * 8);
      st[0] = (f32x4){m[0], r[0], m[1], r[1]};
      st[1] = (f32x4){m[2], r[2], m[3], r[3]};
    }
    __amdgpu_buffer_rsrc_t rs = mkrsrc(ws + WS_H1F, 6291456);
    unsigned o[4][6];
#pragma unroll
    for (int i = 0; i < 4; ++i) {
      f32x2 nm = {-m[i], -m[i]}, rr = {r[i], r[i]}, re = {-1.44269504088896f * r[i], -1.44269504088896f * r[i]};
#pragma unroll
      for (int u = 0; u < 3; ++u)
#pragma unroll
        for (int hf = 0; hf < 2; ++hf) {
          f32x2 v = pfma((f32x2){cs[u][2 * hf], cs[u][2 * hf + 1]}, nm, (f32x2){acc[i][u][2 * hf], acc[i][u][2 * hf + 1]});
          f32x2 e = v * re;
          e.x = __builtin_amdgcn_exp2f(e.x); e.y = __builtin_amdgcn_exp2f(e.y);
          e = e + (f32x2){1.f, 1.f};
          e.x = __builtin_amdgcn_rcpf(e.x); e.y = __builtin_amdgcn_rcpf(e.y);
          v = (v * rr) * e;
          o[i][2 * u + hf] = pk2(v.x, v.y);
        }
      st128((u32x4){o[i][0], o[i][1], o[i][2], o[i][3]}, rs, lane * 16 + ((rb * 48 + 3 * g + 2 * a) * 4 + i) * 1024);
      if (i & 1) {
        swap16(o[i - 1][4], o[i][4]); swap16(o[i - 1][5], o[i][5]);
        st128((u32x4){o[i - 1][4], o[i - 1][5], o[i][4], o[i][5]}, rs,
              (c + 16 * (2 * a + (q >> 1))) * 16 + ((rb * 48 + 3 * g + 1) * 4 + (i - 1) + (q & 1)) * 1024);
      }
    }
    TS(14);
    SB();
    if (w < 2) {
      const bool nat = (uks % 3) == 1;
      int r0 = (nat ? 8 * q : 4 * q) + (c >> 2);
      int dr = nat ? 4 : 16;
      unsigned a0 = lb + S_W2 + w * 4096 + r0 * 128 + (c & 3) * 8;
      int f0 = (r0 >> 1) & 3, f1 = ((r0 + dr) >> 1) & 3;
#pragma unroll
      for (int j = 0; j < 4; ++j) {
        u32x2 lo = ldstr(a0 + ((j ^ f0) << 5));
        u32x2 hi = ldstr(a0 + dr * 128 + ((j ^ f1) << 5));
        int Dt = 4 * udb + j, cg = Dt / 5, nt = Dt - 5 * cg;
        *(u32x4*)(ws + WS_W2F + (size_t)(((cg * 48 + uks) * 5 + nt) * 1024) + lane * 16) = (u32x4){lo.x, lo.y, hi.x, hi.y};
      }
      if (rc == 0 && w == 0 && lane < 40) {
        int sg = lane < 32 ? (5 * (g >> 1) + 2 * (g & 1) + (lane >> 4)) * 16 + 4 * ((lane >> 1) & 3) + 2 * ((lane >> 3) & 1) + (lane & 1)
                           : (5 * (g >> 1) + 4) * 16 + 8 * (g & 1) + 4 * (lane & 1) + ((lane - 32) >> 1);
        *(f32x2*)(ws + WS_TAB + sg * 8) = lds64f(lb + T_TAB + lane * 64 + 56);
      }
    } else {
      u32x4 sj[4];
#pragma unroll
      for (int v = 0; v < 4; ++v) sj[v] = lds128(lb + S_W2 + w * 4096 + v * 1024 + lane * 16);
      unsigned P[2][4][2];
#pragma unroll
      for (int ga = 0; ga < 2; ++ga)
#pragma unroll
        for (int j = 0; j < 4; ++j) {
          unsigned im = sj[2 + ga][j] ^ 0x80008000u;
          P[ga][j][0] = __builtin_amdgcn_perm(im, sj[ga][j], 0x05040100u);
          P[ga][j][1] = __builtin_amdgcn_perm(im, sj[ga][j], 0x07060302u);
        }
      int Dt = 4 * udb + q, cg = Dt / 5, nt = Dt - 5 * cg;
      char* dd = ws + WS_CF + (size_t)(((cg * 40 + uks) * 5 + nt) * 1024) + c * 16;
      if (uks % 5 < 4) {
#pragma unroll
        for (int j = 0; j < 4; ++j) *(u32x4*)(dd + j * 256) = (u32x4){P[0][j][0], P[0][j][1], P[1][j][0], P[1][j][1]};
      } else {
#pragma unroll
        for (int j = 0; j < 4; ++j)
          *(u32x4*)(dd + j * 256) = (u32x4){P[j >> 1][0][j & 1], P[j >> 1][1][j & 1], P[j >> 1][2][j & 1], P[j >> 1][3][j & 1]};
      }
    }
    TS(15);
  } else {
    const int wb = w - 4;
    const char* bp[3];
    unsigned vo[3];
    const int e2 = wb < 2 ? 8 + wb : 6 + wb;
#pragma unroll
    for (int v = 0; v < 3; ++v) {
      int n = 8 * (v < 2 ? wb + 4 * v : e2) + l3;
      bp[v] = (const char*)((n & 1) ? Bim : Bre) + (40 * g + (n >> 1)) * 1280 + ((l7 ^ l3) << 4);
      int idx = 64 * (wb + 4 * v) + lane, k = idx / 12, uu = idx - 12 * k;
      vo[v] = k * 3072 + (((uu >> 1) ^ ((k >> 3) & 1)) << 5) + ((uu & 1) << 4);
    }
    const char* sb = (const char*)w1 + 192 * g;
    unsigned dst = lb + OB + wb * 1024;
    const unsigned d2 = (e2 - wb) * 1024;
    auto piece0 = [&](int st, int j) {
      const int v = j >> 1;
      if (j & 1) dma_s0(vo[v], sb + st * 196608, dst + st * SLOT + (OW - OB) + v * 4096);
      else { dma_v0(bp[v], dst + st * SLOT + (v < 2 ? v * 4096 : d2)); bp[v] += 128; }
    };
    auto issue0 = [&](int st) {
#pragma unroll
      for (int j = 0; j < 6; ++j) piece0(st, j);
    };
    issue0(0);
    prload();
    BARRIER_NW();
    issue0(1);
    BARRIER_NW();
    issue0(2);
    unsigned bo[2];
#pragma unroll
    for (int p = 0; p < 2; ++p) bo[p] = xo[p] - 8192 * h + OB + 4096 * a;
    const int r5 = c ^ (2 * a);
    unsigned f5 = lb + OB + 8192 + r5 * 128 + (((4 * kk0 + q) ^ (r5 & 7)) << 4);
    f32x4 acc[4][2], ac5[5], cs[2];
#pragma unroll
    for (int i = 0; i < 4; ++i) { acc[i][0] = (f32x4){0.f, 0.f, 0.f, 0.f}; acc[i][1] = acc[i][0]; ac5[i] = acc[i][0]; }
    ac5[4] = ac5[0]; cs[0] = ac5[0]; cs[1] = ac5[0];
    u32x4 X[2][2][4], W[2][2][2], W5[2];
    auto rd = [&](int r, int k) {
      if (k == 6) { W5[r] = lds128(f5); return; }
      const int p = k / 7, o = k % 7;
      if (o < 2) W[r][p][o] = lds128(bo[p] + 2048 * o);
      else X[r][p][o - 2] = lds128(xo[p] + 2048 * (o - 2));
    };
    auto adv = [&](unsigned d) { xo[0] += d; xo[1] += d; bo[0] += d; bo[1] += d; f5 += d; };
    WAITV(12);
    BARRIER_NW();
    TS(1);
#pragma unroll
    for (int k = 0; k < 13; ++k) rd(0, k);
    adv(SLOT);
    SB();
    piece0(3, 0); piece0(3, 1);
    tab0(); SB();
    piece0(3, 2); piece0(3, 3);
    tab1(); SB();
    piece0(3, 4); piece0(3, 5);
    sb += 4 * 196608;
    LGKM0();
    WAITV(12);
    BARRIER_NW();
    TS(2);
    auto stage = [&](int r, int t) {
#pragma unroll
      for (int sl = 0; sl < 23; ++sl) {
        if (sl < 8) acc[sl >> 1][sl & 1] = mfma(W[r][0][sl & 1], X[r][0][sl >> 1], acc[sl >> 1][sl & 1]);
        else if (sl < 12) ac5[sl - 8] = mfma(W5[r], X[r][0][sl - 8], ac5[sl - 8]);
        else if (sl == 12) ac5[4] = mfma(W5[r], ONES, ac5[4]);
        else if (sl < 21) acc[(sl - 13) >> 1][(sl - 13) & 1] = mfma(W[r][1][(sl - 13) & 1], X[r][1][(sl - 13) >> 1], acc[(sl - 13) >> 1][(sl - 13) & 1]);
        else cs[sl - 21] = mfma(W[r][1][sl - 21], ONES, cs[sl - 21]);
        if (sl < 13) rd(r ^ 1, sl);
        if (sl % 4 == 1 && sl < 23) {
          const int j = sl / 4, v = j >> 1;
          if (j & 1) dma_s(vo[v], sb, dst + (OW - OB) + v * 4096, t, 6);
          else { dma_v(bp[v], dst + (v < 2 ? v * 4096 : d2), t, 6); bp[v] += 128; }
        }
        constexpr int st[24] = {0, 2, 4, 6, 8, 10, 12, 14, 16, 18, 20, 22, 24, 26, 29, 31, 34, 36, 39, 41, 44, 46, 48, 48};
#pragma unroll
        for (int k = st[sl]; k < st[sl + 1]; ++k) {
          if (k < 24) opS(X[r][0][2], sv[0], sv[1], k); else opS(X[r][1][3], sv[2], sv[3], k - 24);
        }
        SB();
      }
    };
    for (int t = 0; t < 10; t += 2) {
      stage(0, t);
      adv((t & 2) ? -3 * SLOT : SLOT);
      dst += SLOT; sb += 196608;
      LGKM0();
      WAITV2(t, 6, 12, 6);
      BARRIER_NW();
      TS(3 + t);
      stage(1, t + 1);
      adv(SLOT);
      dst += (t & 2) ? -3 * SLOT : SLOT; sb += 196608;
      LGKM0();
      WAITV2(t, 6, 12, 0);
      BARRIER_NW();
      TS(4 + t);
    }
    Tab tb[5];
#pragma unroll
    for (int k = 0; k < 4; ++k) rdtab(tb[k], lb + T_TAB + (16 * a + 8 * (k >> 1) + 2 * q + (k & 1)) * 64);
    rdtab(tb[4], lb + T_TAB + (32 + 2 * q + a) * 64);
    put_stats(2);
    if (c == 0) {
      sts128f(lb + E_CS + ((w * 3 + 0) * 4 + q) * 16, cs[0]);
      sts128f(lb + E_CS + ((w * 3 + 1) * 4 + q) * 16, cs[1]);
    }
#pragma unroll
    for (int i = 0; i < 5; ++i) sts64f(lb + E_A5 + (((w - 4) * 5 + i) * 64 + lane) * 8, (f32x2){ac5[i].z, ac5[i].w});
    BARRIER();
    TS(13);
    float m[4], r[4];
    mr4(m, r);
    cs[0] += lds128f(lb + E_CS + (((w ^ 2) * 3 + 0) * 4 + q) * 16);
    cs[1] += lds128f(lb + E_CS + (((w ^ 2) * 3 + 1) * 4 + q) * 16);
    f32x2 u[5][4], csv[5];
#pragma unroll
    for (int i = 0; i < 4; ++i) {
      u[0][i] = (f32x2){acc[i][0].x, acc[i][0].y}; u[1][i] = (f32x2){acc[i][0].z, acc[i][0].w};
      u[2][i] = (f32x2){acc[i][1].x, acc[i][1].y}; u[3][i] = (f32x2){acc[i][1].z, acc[i][1].w};
      u[4][i] = (f32x2){ac5[i].x, ac5[i].y} + lds64f(lb + E_A5 + ((((w - 4) ^ 1) * 5 + i) * 64 + lane) * 8);
    }
    csv[0] = (f32x2){cs[0].x, cs[0].y}; csv[1] = (f32x2){cs[0].z, cs[0].w};
    csv[2] = (f32x2){cs[1].x, cs[1].y}; csv[3] = (f32x2){cs[1].z, cs[1].w};
    csv[4] = (f32x2){ac5[4].x, ac5[4].y} + lds64f(lb + E_A5 + ((((w - 4) ^ 1) * 5 + 4) * 64 + lane) * 8);
#pragma unroll
    for (int i = 0; i < 4; ++i) {
      f32x2 nm = {-m[i], -m[i]}, rr = {r[i], r[i]};
#pragma unroll
      for (int k = 0; k < 5; ++k) u[k][i] = cmulp(tb[k].co, pfma(csv[k], nm, u[k][i]) * rr);
    }
    f32x2 hh[5];
#pragma unroll
    for (int k = 0; k < 5; ++k) hh[k] = u[k][0];
#pragma unroll
    for (int i = 1; i < 4; ++i)
#pragma unroll
      for (int k = 0; k < 5; ++k) hh[k] = cfmap(tb[k].L, hh[k], u[k][i]);
#pragma unroll
    for (int k = 0; k < 5; ++k) hh[k] = cfmap(tb[k].L4, shr2<1>(hh[k]), hh[k]);
#pragma unroll
    for (int k = 0; k < 5; ++k) hh[k] = cfmap(tb[k].L8, shr2<2>(hh[k]), hh[k]);
#pragma unroll
    for (int k = 0; k < 5; ++k) hh[k] = cfmap(tb[k].L16, shr2<4>(hh[k]), hh[k]);
#pragma unroll
    for (int k = 0; k < 5; ++k) hh[k] = cfmap(tb[k].L32, shr2<8>(hh[k]), hh[k]);
    const int ksr = 5 * (g >> 1) + 2 * (g & 1) + a, ks5 = 5 * (g >> 1) + 4;
    f32x2 ag[5];
#pragma unroll
    for (int k = 0; k < 5; ++k) ag[k] = hh[k];
    if (h == 0) {
      if (c == 15) {
        unsigned ta = lb + T_AG + (a * 4 + q) * 48;
        sts128f(ta, (f32x4){ag[0].x, ag[0].y, ag[1].x, ag[1].y});
        sts128f(ta + 16, (f32x4){ag[2].x, ag[2].y, ag[3].x, ag[3].y});
        sts64f(ta + 32, ag[4]);
      }
      WAITL0();
      if (lane == 0) *(volatile AS3 unsigned*)(uintptr_t)(lb + T_FLAG + a * 4) = 1u;
    }
#pragma unroll
    for (int k = 0; k < 5; ++k) hh[k] = shr2<1>(hh[k]);
    __amdgpu_buffer_rsrc_t rs = mkrsrc(ws + WS_HF, 5242880);
    unsigned o5[4];
#pragma unroll
    for (int i = 0; i < 4; ++i) {
      u32x4 o;
#pragma unroll
      for (int k = 0; k < 5; ++k) {
        hh[k] = cfmap(tb[k].L, hh[k], u[k][i]);
        if (k < 4) o[k] = pk2(hh[k].x, hh[k].y); else o5[i] = pk2(hh[k].x, hh[k].y);
      }
      st128(o, rs, lane * 16 + ((rb * 40 + ksr) * 4 + i) * 1024);
    }
    swap32(o5[0], o5[2]); swap32(o5[1], o5[3]);
    swap16(o5[0], o5[1]); swap16(o5[2], o5[3]);
    st128((u32x4){o5[0], o5[1], o5[2], o5[3]}, rs, (c + 16 * (2 * (g & 1) + a)) * 16 + ((rb * 40 + ks5) * 4 + q) * 1024);
    if (h == 1) {
      while (*(volatile AS3 unsigned*)(uintptr_t)(lb + T_FLAG + a * 4) == 0u) __builtin_amdgcn_s_sleep(1);
      asm volatile("" ::: "memory");
      unsigned ta = lb + T_AG + (a * 4 + q) * 48;
      f32x4 l0 = lds128f(ta), l1 = lds128f(ta + 16);
      f32x2 l2 = lds64f(ta + 32);
      ag[0] = cfmap(tb[0].L64, (f32x2){l0.x, l0.y}, ag[0]); ag[1] = cfmap(tb[1].L64, (f32x2){l0.z, l0.w}, ag[1]);
      ag[2] = cfmap(tb[2].L64, (f32x2){l1.x, l1.y}, ag[2]); ag[3] = cfmap(tb[3].L64, (f32x2){l1.z, l1.w}, ag[3]);
      ag[4] = cfmap(tb[4].L64, l2, ag[4]);
    }
    if (c == 15) {
      char* base = ws + (h ? WS_AGG : WS_AGL) + rc * 2560;
      *(u32x4*)(base + (ksr * 16 + 4 * q) * 4) = (u32x4){pk2(ag[0].x, ag[0].y), pk2(ag[1].x, ag[1].y), pk2(ag[2].x, ag[2].y), pk2(ag[3].x, ag[3].y)};
      *(unsigned*)(base + (ks5 * 16 + 8 * (g & 1) + 4 * a + q) * 4) = pk2(ag[4].x, ag[4].y);
    }
    TS(15);
  }
}

constexpr unsigned M_RO = 0;
constexpr unsigned M_RS = 61440;
constexpr unsigned M_T1 = M_RS + 81920;
constexpr unsigned M_CTL = M_T1;
constexpr unsigned K2_LDS = M_CTL + 64;

extern "C" __global__ __launch_bounds__(512, 2) void k2(const u16* x, const u16* Dsk, char* ws, float* out) {
  __shared__ __attribute__((aligned(1024))) char smem[K2_LDS];
  const unsigned lb = (unsigned)(uintptr_t)(AS3 char*)smem;
  const int tid = threadIdx.x, lane = tid & 63;
  const int w = __builtin_amdgcn_readfirstlane(tid >> 6);
  const int c = lane & 15, q = lane >> 4;
  const int b = blockIdx.x, xcd = b & 7, jb = b >> 3;
  const int rb = (xcd >> 1) * 8 + (jb & 7), cg = (xcd & 1) * 4 + (jb >> 3), gh = xcd & 1;
  const int rc = rb >> 1, par = rb & 1;
  const int vo = lane * 16;
  TSDECL;
  TS(0);
  f32x4 acc[4][5];
  if (tid < 4) sts32f(lb + M_CTL + tid * 4, 0.f);
  BARRIER();

  if (w >= 4) {
    const int sw = w - 4;
    __builtin_amdgcn_s_setprio(SPRIO);
    __amdgpu_buffer_rsrc_t rA = mkrsrc(ws + WS_HF + (size_t)(rb * 40) * 4096, 40 * 4096);
    __amdgpu_buffer_rsrc_t rB = mkrsrc(ws + WS_CF + (size_t)(cg * 40) * 5120, 40 * 5120);
    auto kof = [&](int i) { return 5 * sw + (i >> 1) + 20 * ((i & 1) ^ gh); };
    u32x4 A[3][4], B[3][5];
    auto load = [&](int i) {
      int bf = i % 3;
#pragma unroll
      for (int j = 0; j < 9; ++j) {
        const int m = j < 1 ? j : j - 5;
        if (j >= 1 && j < 6) B[bf][j - 1] = __builtin_amdgcn_raw_buffer_load_b128(rB, vo + (j - 1) * 1024, kof(i) * 5120, 0);
        else A[bf][m] = __builtin_amdgcn_raw_buffer_load_b128(rA, vo + m * 1024, kof(i) * 4096, 0);
        SB();
      }
    };
    const unsigned tl = lb + M_RS + sw * 20480;
    {
      const int l40 = lane < 40 ? lane : 39;
      const int sl0 = kof(l40 >> 2) * 16 + 4 * (l40 & 3);
      __amdgpu_buffer_rsrc_t rG = mkrsrc(ws + WS_AGG, WS_ST - WS_AGG);
      f32x4 tv[2];
      u32x4 ag[15], al;
      tv[0] = __builtin_bit_cast(f32x4, __builtin_amdgcn_raw_buffer_load_b128(rG, sl0 * 8, WS_TAB - WS_AGG, 0));
      tv[1] = __builtin_bit_cast(f32x4, __builtin_amdgcn_raw_buffer_load_b128(rG, sl0 * 8 + 16, WS_TAB - WS_AGG, 0));
#pragma unroll
      for (int m = 0; m < 15; ++m)
        if (m < rc) ag[m] = __builtin_amdgcn_raw_buffer_load_b128(rG, sl0 * 4 + m * 2560, 0, 0);
      if (par) al = __builtin_amdgcn_raw_buffer_load_b128(rG, sl0 * 4, WS_AGL - WS_AGG + rc * 2560, 0);
      SB();
      load(0);
      load(1);
      load(2);
      SB();
      f32x2 P[4], gg[4], Ls[4];
#pragma unroll
      for (int k = 0; k < 4; ++k) {
        gg[k] = cpow(tv[k >> 1][2 * (k & 1)], tv[k >> 1][2 * (k & 1) + 1], 1.f);
        Ls[k] = cpow(tv[k >> 1][2 * (k & 1)], tv[k >> 1][2 * (k & 1) + 1], 4.f);
      }
#pragma unroll
      for (int j = 0; j < 4; ++j) {
        if (j) {
#pragma unroll
          for (int k = 0; k < 4; ++k) gg[k] = cmulp(Ls[k], gg[k]);
        }
        sts128f(tl + 5120 + l40 * 128 + j * 32, (f32x4){gg[0].x, gg[0].y, gg[1].x, gg[1].y});
        sts128f(tl + 5120 + l40 * 128 + j * 32 + 16, (f32x4){gg[2].x, gg[2].y, gg[3].x, gg[3].y});
      }
#pragma unroll
      for (int k = 0; k < 4; ++k) { P[k] = cpow(tv[k >> 1][2 * (k & 1)], tv[k >> 1][2 * (k & 1) + 1], 128.f); gg[k] = (f32x2){0.f, 0.f}; }
      f32x2 L64[4];
#pragma unroll
      for (int k = 0; k < 4; ++k) {
        Ls[k] = cpow(tv[k >> 1][2 * (k & 1)], tv[k >> 1][2 * (k & 1) + 1], 16.f);
        L64[k] = cpow(tv[k >> 1][2 * (k & 1)], tv[k >> 1][2 * (k & 1) + 1], 64.f);
      }
#pragma unroll
      for (int m = 0; m < 15; ++m)
        if (m < rc) {
#pragma unroll
          for (int k = 0; k < 4; ++k) gg[k] = cfmap(P[k], gg[k], (f32x2){bflo(ag[m][k]), bfhi(ag[m][k])});
        }
      if (par) {
#pragma unroll
        for (int k = 0; k < 4; ++k) gg[k] = cfmap(L64[k], gg[k], (f32x2){bflo(al[k]), bfhi(al[k])});
      }
#pragma unroll
      for (int j = 0; j < 4; ++j) {
        if (j) {
#pragma unroll
          for (int k = 0; k < 4; ++k) gg[k] = cmulp(Ls[k], gg[k]);
        }
        sts128f(tl + l40 * 128 + j * 32, (f32x4){gg[0].x, gg[0].y, gg[1].x, gg[1].y});
        sts128f(tl + l40 * 128 + j * 32 + 16, (f32x4){gg[2].x, gg[2].y, gg[3].x, gg[3].y});
      }
    }
    TS(1);
#pragma unroll
    for (int m = 0; m < 4; ++m)
#pragma unroll
      for (int n = 0; n < 5; ++n) acc[m][n] = (f32x4){0.f, 0.f, 0.f, 0.f};
    const unsigned tq = tl + q * 128 + (c >> 2) * 32, uq = tl + 5120 + q * 128 + (c & 3) * 32, lq = tl + 5120 + q * 128;
    const unsigned tb = lb + M_RS + sw * 20480 + vo;
    f32x4 tt[4], Lq[2];
    float zf[8], tf[8], e0, e1;
    auto rdt = [&](int i) {
      tt[0] = lds128f(tq + i * 512); tt[1] = lds128f(tq + i * 512 + 16);
      tt[2] = lds128f(uq + i * 512); tt[3] = lds128f(uq + i * 512 + 16);
    };
    auto rdl = [&](int i) { Lq[0] = lds128f(lq + i * 512); Lq[1] = lds128f(lq + i * 512 + 16); };
    auto opA = [&](int k) {
      const int d = k / 4, o = k % 4;
      const float Lx = Lq[d >> 1][2 * (d & 1)], Ly = Lq[d >> 1][2 * (d & 1) + 1];
      if (o == 0) e0 = vmul(Ly, zf[2 * d + 1]);
      else if (o == 1) e1 = vmul(Ly, zf[2 * d]);
      else if (o == 2) zf[2 * d] = vfms(Lx, zf[2 * d], e0);
      else zf[2 * d + 1] = vfma(Lx, zf[2 * d + 1], e1);
    };
    auto opZ = [&](int k) {
      const int d = k / 4, o = k % 4;
      const float gx = tt[d >> 1][2 * (d & 1)], gy = tt[d >> 1][2 * (d & 1) + 1];
      const float ux = tt[2 + (d >> 1)][2 * (d & 1)], uy = tt[2 + (d >> 1)][2 * (d & 1) + 1];
      if (o == 0) e0 = vmul(gy, uy);
      else if (o == 1) e1 = vmul(gy, ux);
      else if (o == 2) zf[2 * d] = vfms(gx, ux, e0);
      else zf[2 * d + 1] = vfma(gx, uy, e1);
    };
    auto opD = [&](int bf, int m, int j) {
      const unsigned av = A[bf][m][j >> 1];
      tf[j] = vadd(zf[j], (j & 1) ? bfhi(av) : bflo(av));
    };
    auto opC = [&](int bf, int m, int d) { A[bf][m][d] = pk2a(tf[2 * d], tf[2 * d + 1]); };
    auto vop = [&](int i, int k) {
      const int bf = i % 3;
      if (k < 100) {
        const int gq = (k + 12) / 28, o = (k + 12) % 28;
        if (o < 12) opA(4 + o);
        else if (o < 20) opD(bf, gq, o - 12);
        else if (o < 24) { if (gq < 3) opA(o - 20); else if (i < 9) opZ(o - 20); }
        else opC(bf, gq, o - 24);
      } else if (i < 9) opZ(k - 96);
    };
    rdt(0); rdl(0);
#pragma unroll
    for (int k = 0; k < 16; ++k) opZ(k);
    SB();
    auto step = [&](auto I) {
      constexpr int i = decltype(I)::v;
      sfor<0, 16>([&](auto K) { vop(i, decltype(K)::v); SB(); });
      sfor<0, 20>([&](auto S) {
        constexpr int sl = decltype(S)::v;
        acc[sl / 5][sl % 5] = mfma(B[i % 3][sl % 5], A[i % 3][sl / 5], acc[sl / 5][sl % 5]);
        constexpr int st[21] = {16, 22, 28, 34, 39, 44, 50, 56, 62, 67, 72, 78, 84, 90, 95, 100, 103, 106, 108, 110, 112};
        SB();
        sfor<st[sl], st[sl + 1]>([&](auto K) { vop(i, decltype(K)::v); SB(); });
        if (i < 9 && sl == 9) rdt(i + 1);
        if (i < 9 && sl == 12) rdl(i + 1);
        if (i == 9 && sl >= 2) sts128f(tb + (sl - 2) * 1024, acc[(sl - 2) / 5][(sl - 2) % 5]);
        SB();
      });
      if (i + 3 < 10) {
        if (SLP != SPRIO) __builtin_amdgcn_s_setprio(SLP);
        load(i + 3);
        SB();
        if (SLP != SPRIO) __builtin_amdgcn_s_setprio(SPRIO);
      }
      TS(2 + i);
    };
    step(IC<0>{}); step(IC<1>{}); step(IC<2>{}); step(IC<3>{}); step(IC<4>{});
    step(IC<5>{}); step(IC<6>{}); step(IC<7>{}); step(IC<8>{}); step(IC<9>{});
    sts128f(tb + 18 * 1024, acc[3][3]);
    sts128f(tb + 19 * 1024, acc[3][4]);
    TS(14);
    BARRIER();
    TS(15);
    TSDUMP(33554432 + 1048576);
    return;
  }

  __builtin_amdgcn_s_setprio(OPRIO);
  __amdgpu_buffer_rsrc_t rA = mkrsrc(ws + WS_H1F + (size_t)rb * 48 * 4096, 48 * 4096);
  __amdgpu_buffer_rsrc_t rB = mkrsrc(ws + WS_W2F + (size_t)cg * 48 * 5120, 48 * 5120);
  u32x4 A[4][4], B[4][5];
  auto loadA = [&](int i) {
    int bf = i & 3, ks = w + 4 * ((i >> 1) + 6 * ((i & 1) ^ gh));
#pragma unroll
    for (int m = 0; m < 4; ++m) A[bf][m] = __builtin_amdgcn_raw_buffer_load_b128(rA, vo, ks * 4096 + ((w + m) & 3) * 1024, 0);
  };
  auto loadB = [&](int i) {
    int bf = i & 3, ks = w + 4 * ((i >> 1) + 6 * ((i & 1) ^ gh));
#pragma unroll
    for (int n = 0; n < 5; ++n) B[bf][n] = __builtin_amdgcn_raw_buffer_load_b128(rB, vo + n * 1024, ks * 5120, 0);
  };
  auto load = [&](int i) { loadA(i); loadB(i); };
  u32x2 xv[5], sk[5];
  f32x2 st;
  load(0);
  load(1);
  load(2);
  TS(1);
#pragma unroll
  for (int m = 0; m < 4; ++m)
#pragma unroll
    for (int n = 0; n < 5; ++n) acc[m][n] = (f32x4){0.f, 0.f, 0.f, 0.f};
  unsigned da[3];
#pragma unroll
  for (int i = 0; i < 12; ++i) {
    if (OTP != OPRIO && i == OTI) __builtin_amdgcn_s_setprio(OTP);
    if (i == 9) {
      int l2 = lane;
      asm volatile("" : "+v"(l2));
      const int trow = 64 * rb + 4 * (l2 & 15) + w;
      const int eo = trow * 640 + 80 * cg + 4 * (l2 >> 4);
      __amdgpu_buffer_rsrc_t rx = mkrsrc(x, 2621440), rd = mkrsrc(Dsk, 1280), rt = mkrsrc(ws + WS_ST, 16384);
      st = __builtin_bit_cast(f32x2, __builtin_amdgcn_raw_buffer_load_b64(rt, trow * 8, 0, 0));
#pragma unroll
      for (int n = 0; n < 5; ++n) {
        xv[n] = __builtin_amdgcn_raw_buffer_load_b64(rx, eo * 2 + n * 32, 0, 0);
        sk[n] = __builtin_amdgcn_raw_buffer_load_b64(rd, (l2 >> 4) * 8 + n * 32, 160 * cg, 0);
      }
      SB();
    }
    if (i + 3 < 12) {
      if (OLP != OPRIO && i >= OLI && (OTP == OPRIO || i < OTI)) __builtin_amdgcn_s_setprio(OLP);
      load(i + 3);
      SB();
      if (OLP != OPRIO && i >= OLI && (OTP == OPRIO || i < OTI)) __builtin_amdgcn_s_setprio(OPRIO);
    }
    SB();
    if (i < 11) {
#pragma unroll
      for (int m = 0; m < 4; ++m)
#pragma unroll
        for (int n = 0; n < 5; ++n) acc[m][n] = mfma(B[i & 3][n], A[i & 3][m], acc[m][n]);
    } else {
      int v2 = vo;
      asm volatile("" : "+v"(v2));
#pragma unroll
      for (int j = 1; j < 4; ++j) da[j - 1] = lb + M_RO + (((w + j) & 3) * 3 + (j - 1)) * 5120 + v2;
#pragma unroll
      for (int sl = 0; sl < 20; ++sl) {
        const int m = (sl / 5 + 1) & 3, n = sl % 5;
        acc[m][n] = mfma(B[i & 3][n], A[i & 3][m], acc[m][n]);
        if (sl >= 2 && sl < 17) sts128f(da[(sl - 2) / 5] + ((sl - 2) % 5) * 1024, acc[(sl - 2) / 5 + 1][(sl - 2) % 5]);
        SB();
      }
    }
    SB();
    TS(2 + i);
  }
  WAITL0();
  if (lane == 0) *(volatile AS3 unsigned*)(uintptr_t)(lb + M_CTL + w * 4) = 1u;
  {
#pragma unroll
    for (int n = 0; n < 5; ++n)
#pragma unroll
      for (int hf = 0; hf < 2; ++hf) {
        unsigned p = pk2a(vsub(bflo(xv[n][hf]), st.x), vsub(bfhi(xv[n][hf]), st.x));
        p = pk2a(vmul(bflo(p), st.y), vmul(bfhi(p), st.y));
        sk[n][hf] = pk2a(vmul(bflo(p), bflo(sk[n][hf])), vmul(bfhi(p), bfhi(sk[n][hf])));
      }
#pragma unroll
    for (int n = 0; n < 5; ++n) asm volatile("" : "+v"(sk[n]), "+v"(xv[n]));
  }
  for (;;) {
    u32x4 f = *(volatile AS3 u32x4*)(uintptr_t)(lb + M_CTL);
    if ((f.x & f.y & f.z & f.w) != 0u) break;
    __builtin_amdgcn_s_sleep(1);
  }
  asm volatile("" ::: "memory");
  TS(14);
  f32x4 xs[5], g2[5];
  {
    f32x4 po[5][3];
#pragma unroll
    for (int n = 0; n < 5; ++n)
#pragma unroll
      for (int j = 0; j < 3; ++j) po[n][j] = lds128f(lb + M_RO + ((w * 3 + j) * 5 + n) * 1024 + vo);
    SB();
#pragma unroll
    for (int n = 0; n < 5; ++n) {
      f32x4 o;
#pragma unroll
      for (int j = 0; j < 4; ++j) o[j] = vadd(vadd(acc[0][n][j], po[n][0][j]), vadd(po[n][1][j], po[n][2][j]));
#pragma unroll
      for (int hf = 0; hf < 2; ++hf) {
        unsigned p = pk2a(o[2 * hf], o[2 * hf + 1]);
        float v0 = bflo(p), v1 = bfhi(p);
        float e0 = __builtin_amdgcn_exp2f(vmul(v0, -1.44269504088896f)), e1 = __builtin_amdgcn_exp2f(vmul(v1, -1.44269504088896f));
        e0 = __builtin_amdgcn_rcpf(vadd(e0, 1.f)); e1 = __builtin_amdgcn_rcpf(vadd(e1, 1.f));
        p = pk2a(vmul(v0, e0), vmul(v1, e1));
        v0 = bflo(p); v1 = bfhi(p);
        xs[n][2 * hf] = vfma(v0, bflo(sk[n][hf]), bflo(xv[n][hf]));
        xs[n][2 * hf + 1] = vfma(v1, bfhi(sk[n][hf]), bfhi(xv[n][hf]));
        g2[n][2 * hf] = v0; g2[n][2 * hf + 1] = v1;
      }
    }
#pragma unroll
    for (int n = 0; n < 5; ++n) asm volatile("" : "+v"(xs[n]), "+v"(g2[n]));
  }
  __amdgpu_buffer_rsrc_t ro = mkrsrc(out, 5242880);
  int l2 = lane;
  asm volatile("" : "+v"(l2));
  const int eo2 = ((64 * rb + 4 * (l2 & 15) + w) * 640 + 80 * cg + 4 * (l2 >> 4)) * 4;
  TS(15);
  BARRIER_NW();
  TS(16);
  f32x4 ps[5][4];
#pragma unroll
  for (int n = 0; n < 5; ++n)
#pragma unroll
    for (int k = 0; k < 4; ++k) ps[n][k] = lds128f(lb + M_RS + (w * 5 + n) * 1024 + k * 20480 + vo);
  SB();
#pragma unroll
  for (int n = 0; n < 5; ++n) {
    f32x4 s = (ps[n][0] + ps[n][1]) + (ps[n][2] + ps[n][3]);
    f32x4 r = __builtin_elementwise_fma(s, g2[n], xs[n]);
    __builtin_amdgcn_raw_buffer_store_b128(__builtin_bit_cast(u32x4, r), ro, eo2 + n * 64, 0, 0);
    SB();
  }
}

extern "C" __device__ __attribute__((used, noinline)) void kend() {}

extern "C" void kernel_launch(void* const* d_in, const int* in_sizes, int n_in, void* d_out, int out_size, void* d_ws,
                              size_t ws_size, hipStream_t stream) {
  const u16* x = (const u16*)d_in[0];
  k1<<<256, 512, 0, stream>>>(x, (const u16*)d_in[1], (const u16*)d_in[2], (const u16*)d_in[3], (const u16*)d_in[4],
                              (const u16*)d_in[5], (const u16*)d_in[6], (const u16*)d_in[7], (const u16*)d_in[10],
                              (const u16*)d_in[11], (char*)d_ws);
  k2<<<256, 512, 0, stream>>>(x, (const u16*)d_in[8], (char*)d_ws, (float*)d_out);
}
```
